```python
import torch

def _softmax(x, axis=-1):
    return torch.softmax(x, dim=axis)
def _gelu(x):
    return torch.nn.functional.gelu(x, approximate="tanh")
def _silu(x):
    return torch.nn.functional.silu(x)
def _sigmoid(x):
    return torch.sigmoid(x)
def _layernorm(x, axis=-1, eps=1e-5):
    m = x.mean(dim=axis, keepdim=True)
    v = x.var(dim=axis, keepdim=True, unbiased=False)
    return (x - m) / torch.sqrt(v + eps)
def _scatter_add(tgt, idx, src, axis=0):
    return tgt.clone().index_add_(axis, idx, src)
def _segment_sum(src, seg, n):
    out = torch.zeros((n,) + src.shape[1:], dtype=src.dtype)
    return out.index_add_(0, seg, src)
def _pad(x, pw):
    flat = tuple(v for pair in reversed(pw) for v in pair)
    return torch.nn.functional.pad(x, flat)

kernel_name = 'recurrent_scan__sliding_block__geglu_ffn__prompt'

def setup_inputs():

    _w = (lambda a: (lambda t: t.to(torch.bfloat16) if t.is_floating_point() else t)(torch.as_tensor(a)))
    def _A_setup():
        B, S, D = 1, 2048, 448
        return {
            "x": _w((lambda *s: torch.randn(*s, dtype=torch.bfloat16) * 0.05)(B, S, D)),
            "wqkv": _w((lambda *s: torch.randn(*s, dtype=torch.bfloat16) * 0.05)(D, 3 * D)),
            "wo": _w((lambda *s: torch.randn(*s, dtype=torch.bfloat16) * 0.05)(D, D)),
        }
    _A_in = _A_setup()

    def _B_setup():
        B, S, D, F = 1, 2048, 448, 1792
        return {
            "x": _w((lambda *s: torch.randn(*s, dtype=torch.bfloat16) * 0.05)(B, S, D)),
            "w_gate": _w((lambda *s: torch.randn(*s, dtype=torch.bfloat16) * 0.05)(D, F)),
            "w_up": _w((lambda *s: torch.randn(*s, dtype=torch.bfloat16) * 0.05)(D, F)),
            "w_down": _w((lambda *s: torch.randn(*s, dtype=torch.bfloat16) * 0.05)(F, D)),
        }
    _B_in = _B_setup()

    return {
        "A_x": _A_in["x"],
        "A_wqkv": _A_in["wqkv"],
        "A_wo": _A_in["wo"],
        "B_x": _B_in["x"],
        "B_w_gate": _B_in["w_gate"],
        "B_w_up": _B_in["w_up"],
        "B_w_down": _B_in["w_down"],
    }

def reference(A_x, A_wqkv, A_wo, B_x, B_w_gate, B_w_up, B_w_down):
    def _A_ref(x, wqkv, wo):
        B, S, D = x.shape
        H = 16; Dh = D // H; W = 256
        qkv = (lambda x, *d: x.permute(*d))((x @ wqkv).reshape(B, S, 3, H, Dh), 2, 0, 3, 1, 4)
        q, k, v = qkv[0], qkv[1], qkv[2]
        scores = torch.einsum("bhqd,bhkd->bhqk", q, k) * (Dh ** -0.5)
        idx = torch.arange(S)
        mask = (idx[:, None] - idx[None, :] >= 0) & (idx[:, None] - idx[None, :] < W)
        scores = torch.where(mask[None, None], scores, -1e9)
        p = _softmax(scores, axis=-1)
        o = torch.einsum("bhqk,bhkd->bhqd", p, v)
        return (lambda x, *d: x.permute(*d))(o, 0, 2, 1, 3).reshape(B, S, D) @ wo

    def _B_ref(x, w_gate, w_up, w_down):
        return (_gelu(x @ w_gate) * (x @ w_up)) @ w_down

    x = A_x
    xn = _layernorm(x, axis=-1)
    s = _A_ref(xn, A_wqkv, A_wo)
    g = _silu(_B_ref(xn, B_w_gate, B_w_up, B_w_down))
    return x + s * g
```

```cpp
#include <hip/hip_runtime.h>
#include <hip/hip_bf16.h>

typedef short bf16x8 __attribute__((ext_vector_type(8)));
typedef short s16x4 __attribute__((ext_vector_type(4)));
typedef float f32x4 __attribute__((ext_vector_type(4)));
typedef unsigned u32x4 __attribute__((ext_vector_type(4)));
typedef unsigned u32x2 __attribute__((ext_vector_type(2)));
typedef __bf16 bf16x2_t __attribute__((ext_vector_type(2)));
typedef __attribute__((address_space(3))) char* lds_ptr;

#define MFMA16(a, b, c) __builtin_amdgcn_mfma_f32_16x16x32_bf16(a, b, c, 0, 0, 0)
#define WAIT_BAR0() asm volatile("s_waitcnt vmcnt(0) lgkmcnt(0)\n\ts_barrier" ::: "memory")
#define LBAR() asm volatile("s_waitcnt lgkmcnt(0)\n\ts_barrier" ::: "memory")

constexpr int S_ = 2048, D_ = 448, F_ = 1792, QKV_ = 1344;
constexpr int ACT_OCT = 280;
constexpr int ACT_TILE_B = ACT_OCT * 256;

__device__ __forceinline__ void glds16(const void* g, unsigned lds_dst) {
    unsigned sv;
    asm volatile("s_mov_b32 %0, m0\n\ts_mov_b32 m0, %2\n\ts_nop 0\n\tglobal_load_lds_dwordx4 %1, off\n\ts_mov_b32 m0, %0"
                 : "=&s"(sv) : "v"(g), "s"(lds_dst) : "memory");
}
__device__ __forceinline__ void glds16s(const void* sbase, unsigned voff, unsigned lds_dst) {
    unsigned sv;
    asm volatile("s_mov_b32 %0, m0\n\ts_mov_b32 m0, %3\n\ts_nop 0\n\tglobal_load_lds_dwordx4 %1, %2\n\ts_mov_b32 m0, %0"
                 : "=&s"(sv) : "v"(voff), "s"(sbase), "s"(lds_dst) : "memory");
}
__device__ __forceinline__ bf16x8 lds_b128(lds_ptr p) { return *(const __attribute__((address_space(3))) bf16x8*)p; }
__device__ __forceinline__ f32x4 lds_f4(lds_ptr p) { return *(const __attribute__((address_space(3))) f32x4*)p; }
__device__ __forceinline__ s16x4 lds_tr(lds_ptr p) {
    return __builtin_amdgcn_ds_read_tr16_b64_v4i16((__attribute__((address_space(3))) s16x4*)p);
}
__device__ __forceinline__ bf16x8 lds_tr2(lds_ptr p, int second) {
    s16x4 a = lds_tr(p), b = lds_tr(p + second);
    return __builtin_shufflevector(a, b, 0, 1, 2, 3, 4, 5, 6, 7);
}
__device__ __forceinline__ float dot2(unsigned a, unsigned b, float c) {
    return __builtin_amdgcn_fdot2_f32_bf16(__builtin_bit_cast(bf16x2_t, a), __builtin_bit_cast(bf16x2_t, b), c, false);
}
__device__ __forceinline__ float frag_sum(bf16x8 f, float c) {
    u32x4 u = __builtin_bit_cast(u32x4, f);
    c = dot2(u[0], 0x3F803F80u, c); c = dot2(u[1], 0x3F803F80u, c);
    c = dot2(u[2], 0x3F803F80u, c); c = dot2(u[3], 0x3F803F80u, c);
    return c;
}
__device__ __forceinline__ float frag_sq(bf16x8 f, float c) {
    u32x4 u = __builtin_bit_cast(u32x4, f);
    c = dot2(u[0], u[0], c); c = dot2(u[1], u[1], c); c = dot2(u[2], u[2], c); c = dot2(u[3], u[3], c);
    return c;
}
__device__ __forceinline__ float xsum(float x) {
    auto s = __builtin_amdgcn_permlane16_swap(__float_as_uint(x), __float_as_uint(x), false, false);
    x = __uint_as_float(s[0]) + __uint_as_float(s[1]);
    auto t = __builtin_amdgcn_permlane32_swap(__float_as_uint(x), __float_as_uint(x), false, false);
    return __uint_as_float(t[0]) + __uint_as_float(t[1]);
}
__device__ __forceinline__ float xmax(float x) {
    auto s = __builtin_amdgcn_permlane16_swap(__float_as_uint(x), __float_as_uint(x), false, false);
    x = fmaxf(__uint_as_float(s[0]), __uint_as_float(s[1]));
    auto t = __builtin_amdgcn_permlane32_swap(__float_as_uint(x), __float_as_uint(x), false, false);
    return fmaxf(__uint_as_float(t[0]), __uint_as_float(t[1]));
}
__device__ __forceinline__ unsigned pk2(float lo, float hi) {
    bf16x2_t v; v[0] = (__bf16)lo; v[1] = (__bf16)hi;
    return __builtin_bit_cast(unsigned, v);
}
__device__ __forceinline__ float rbf(float x) { return (float)(__bf16)x; }
__device__ __forceinline__ void ln_stats(float sum, float sq, float& mean, float& den) {
    float m = sum * (1.0f / D_);
    float v = fmaxf(sq * (1.0f / D_) - m * m, 0.0f);
    mean = rbf(m);
    den = rbf(sqrtf(rbf(rbf(v) + 1e-5f)));
}

constexpr int FF_X = 32768, FF_W = 28672, FF_STAGE = FF_X + FF_W, FF_CS = 2 * FF_STAGE;

__device__ __forceinline__ void ffn_block(const char* __restrict__ x, const char* __restrict__ wg, const char* __restrict__ wu,
                                          char* __restrict__ act, int rb, int cb, lds_ptr L) {
    const int tid = threadIdx.x, lane = tid & 63;
    const int w = __builtin_amdgcn_readfirstlane(tid >> 6);
    const int wm = w & 3, wn = w >> 2;
    const unsigned lb = (unsigned)(size_t)L;
    unsigned xoff[4];
    const char* wp[4];
#pragma unroll
    for (int j = 0; j < 4; ++j) {
        int R = 32 * w + 8 * j + (lane >> 3);
        xoff[j] = R * 896 + (((lane & 7) ^ ((R >> 1) & 7)) << 4);
        int n = w + 8 * j; if (n > 27) n = 27;
        int ci = n * 64 + lane, row = ci / 28, pos = ci - row * 28;
        int tp = (pos >> 1) ^ ((row >> 3) & 1);
        wp[j] = ((pos & 1) ? wu : wg) + (size_t)row * (F_ * 2) + cb * 224 + tp * 16;
    }
    const char* xb = x + (size_t)rb * 256 * 896;
    auto issue = [&](int c) {
        unsigned st = lb + (c & 1) * FF_STAGE;
#pragma unroll
        for (int j = 0; j < 4; ++j) glds16s(xb + c * 128, xoff[j], st + (32 * w + 8 * j) * 128);
#pragma unroll
        for (int j = 0; j < 4; ++j) {
            if (w + 8 * j < 28) glds16(wp[j], st + FF_X + (w + 8 * j) * 1024);
            wp[j] += 64 * F_ * 2;
        }
    };
    issue(0);
    const int g = lane >> 4, q = (lane >> 2) & 3, p = lane & 3, tk = lane & 15;
    const int xa0 = (64 * wm + tk) * 128 + ((g ^ ((lane >> 1) & 7)) << 4);
    const int xa1 = xa0 ^ 64;
    const int wrow = FF_X + (8 * g + q) * 448 + p * 8;
    const int dlt = (g & 1) * 32 * (wn ? -1 : 1);
    const int wE = wrow + 224 * wn + dlt, wO = wrow + 224 * wn - dlt;
    const int tc1 = (wm + 4 > 6) ? 6 : wm + 4;
    const int wc0 = wrow + (((7 * wn + wm) ^ (g & 1)) << 5), wc1 = wrow + (((7 * wn + tc1) ^ (g & 1)) << 5);
    f32x4 acc[4][7];
#pragma unroll
    for (int i = 0; i < 4; ++i)
#pragma unroll
        for (int t = 0; t < 7; ++t) acc[i][t] = f32x4{0.f, 0.f, 0.f, 0.f};
    float sm[4] = {0.f, 0.f, 0.f, 0.f}, sq[4] = {0.f, 0.f, 0.f, 0.f}, cs0 = 0.f, cs1 = 0.f;
#pragma unroll 1
    for (int c = 0; c < 7; ++c) {
        WAIT_BAR0();
        if (c < 6) issue(c + 1);
        lds_ptr B = L + (c & 1) * FF_STAGE;
#pragma unroll
        for (int ks = 0; ks < 2; ++ks) {
            bf16x8 xf[4], wf[7];
#pragma unroll
            for (int i = 0; i < 4; ++i) xf[i] = lds_b128(B + (ks ? xa1 : xa0) + i * 2048);
#pragma unroll
            for (int t = 0; t < 7; ++t) wf[t] = lds_tr2(B + ((t & 1) ? wO : wE) + ks * (32 * 448) + t * 32, 4 * 448);
            bf16x8 c0 = lds_tr2(B + wc0 + ks * (32 * 448), 4 * 448), c1 = lds_tr2(B + wc1 + ks * (32 * 448), 4 * 448);
#pragma unroll
            for (int t = 0; t < 7; ++t)
#pragma unroll
                for (int i = 0; i < 4; ++i) acc[i][t] = MFMA16(wf[t], xf[i], acc[i][t]);
#pragma unroll
            for (int i = 0; i < 4; ++i) { sm[i] = frag_sum(xf[i], sm[i]); sq[i] = frag_sq(xf[i], sq[i]); }
            cs0 = frag_sum(c0, cs0); cs1 = frag_sum(c1, cs1);
        }
    }
    cs0 = xsum(cs0); cs1 = xsum(cs1);
    if (lane < 16) {
        *(__attribute__((address_space(3))) float*)(L + FF_CS + (7 * wn + wm) * 64 + lane * 4) = cs0;
        if (wm < 3) *(__attribute__((address_space(3))) float*)(L + FF_CS + (7 * wn + wm + 4) * 64 + lane * 4) = cs1;
    }
    float mean[4], den[4];
#pragma unroll
    for (int i = 0; i < 4; ++i) ln_stats(xsum(sm[i]), xsum(sq[i]), mean[i], den[i]);
    LBAR();
    const float K1 = -2.3022081986f, K2 = -0.1029432398f;
#pragma unroll
    for (int ip = 0; ip < 2; ++ip) {
        const float dA = den[2 * ip], dB = den[2 * ip + 1];
        float dn = (lane < 32) ? dA : dB;
        asm volatile("" : "+v"(dn));
        const float a = 1.0f / dn, ia2 = dn * dn, c1 = K1 * a, c2 = K2 * a * a * a;
        char* dst = act + (size_t)(rb * 16 + wm * 4 + 2 * ip + (lane >> 5)) * ACT_TILE_B + (cb * 14 + wn * 7) * 256 + tk * 16 + (g & 1) * 8;
#pragma unroll
        for (int t = 0; t < 7; ++t) {
            const f32x4 cs = lds_f4(L + FF_CS + (7 * wn + t) * 64 + g * 16);
            float h[4];
#pragma unroll
            for (int r = 0; r < 4; ++r) {
                float va = fmaf(-mean[2 * ip], cs[r], acc[2 * ip][t][r]);
                float vb = fmaf(-mean[2 * ip + 1], cs[r], acc[2 * ip + 1][t][r]);
                auto sw = __builtin_amdgcn_permlane32_swap(__float_as_uint(va), __float_as_uint(vb), false, false);
                const float G = __uint_as_float(sw[0]), U = __uint_as_float(sw[1]);
                const float e = __builtin_amdgcn_exp2f(G * fmaf(G * G, c2, c1));
                h[r] = (G * U) * __builtin_amdgcn_rcpf(fmaf(e, ia2, ia2));
            }
            *(u32x2*)(dst + t * 256) = u32x2{pk2(h[0], h[1]), pk2(h[2], h[3])};
        }
    }
}

constexpr int AT_WM = 28672, AT_X = 3 * AT_WM, AT_CS = AT_X + 65536, AT_ST = AT_CS + 512, AT_END = AT_ST + 4096;
constexpr int AT_K = 0, AT_V = 32768;

__device__ __forceinline__ void att_block(const char* __restrict__ x, const char* __restrict__ wqkv, char* __restrict__ act,
                                          int ch, int head, lds_ptr L) {
    const int tid = threadIdx.x, lane = tid & 63;
    const int w = __builtin_amdgcn_readfirstlane(tid >> 6);
    const unsigned lb = (unsigned)(size_t)L;
    const int g = lane >> 4, q = (lane >> 2) & 3, p = lane & 3, tk = lane & 15;
    const unsigned xo0 = (lane >> 3) * 896 + (((lane & 7) ^ (lane >> 4)) << 4), xo1 = xo0 ^ 64;
    const char* xh = x + (size_t)((ch ? ch - 1 : 0) * 256 + 32 * w) * 896;
    const char* xs = x + (size_t)(ch * 256 + 32 * w) * 896;
    const unsigned xst = lb + AT_X + w * 8192;
    auto issue_x = [&](int c) {
#pragma unroll
        for (int j = 0; j < 4; ++j) glds16s(xh + j * (8 * 896) + c * 128, (j & 1) ? xo1 : xo0, xst + j * 1024);
#pragma unroll
        for (int j = 0; j < 4; ++j) glds16s(xs + j * (8 * 896) + c * 128, (j & 1) ? xo1 : xo0, xst + 4096 + j * 1024);
    };
    issue_x(0);
    {
        const unsigned wo = (lane >> 2) * (QKV_ * 2) + (((lane & 3) ^ (((lane >> 5) & 1) << 1)) << 4);
#pragma unroll
        for (int j = 0; j < 11; ++j) {
            const int n = w + 8 * j;
            if (n < 84) {
                const int rblk = n / 3, mat = n - rblk * 3;
                glds16s(wqkv + (size_t)rblk * 16 * (QKV_ * 2) + mat * 896 + head * 56 - (mat == 2 ? 8 : 0), wo,
                        lb + mat * AT_WM + rblk * 1024);
            }
        }
    }
    const int xa0 = AT_X + w * 8192 + tk * 128 + ((g ^ ((lane >> 1) & 7)) << 4), xa1 = xa0 ^ 64;
    const int wA0 = (8 * g + q) * 64 + ((g & 1) << 5) + p * 8, wA1 = wA0 ^ 32;
    const int cidx = w > 5 ? 5 : w;
    const int wC = (cidx >> 1) * AT_WM + (wA0 ^ ((cidx & 1) << 5));
    f32x4 aK[4][2], aV[4][2], aQ[2][2];
#pragma unroll
    for (int i = 0; i < 4; ++i) { aK[i][0] = aK[i][1] = aV[i][0] = aV[i][1] = f32x4{0.f, 0.f, 0.f, 0.f}; }
    aQ[0][0] = aQ[0][1] = aQ[1][0] = aQ[1][1] = f32x4{0.f, 0.f, 0.f, 0.f};
    float sm[4] = {0.f, 0.f, 0.f, 0.f}, sq[4] = {0.f, 0.f, 0.f, 0.f}, cs = 0.f;
    WAIT_BAR0();
#pragma unroll 1
    for (int c = 0; c < 7; ++c) {
        bf16x8 xf[2][4];
#pragma unroll
        for (int ks = 0; ks < 2; ++ks)
#pragma unroll
            for (int i = 0; i < 4; ++i) xf[ks][i] = lds_b128(L + (ks ? xa1 : xa0) + i * 2048);
        asm volatile("s_waitcnt lgkmcnt(0)" : "+v"(xf[0][0]), "+v"(xf[0][1]), "+v"(xf[0][2]), "+v"(xf[0][3]),
                     "+v"(xf[1][0]), "+v"(xf[1][1]), "+v"(xf[1][2]), "+v"(xf[1][3]) :: "memory");
        if (c < 6) issue_x(c + 1);
        lds_ptr B = L + c * 4096;
#pragma unroll
        for (int ks = 0; ks < 2; ++ks) {
            bf16x8 wf[3][2];
#pragma unroll
            for (int m = 0; m < 3; ++m) {
                wf[m][0] = lds_tr2(B + wA0 + m * AT_WM + ks * 2048, 256);
                wf[m][1] = lds_tr2(B + wA1 + m * AT_WM + ks * 2048, 256);
            }
            bf16x8 cf = lds_tr2(B + wC + ks * 2048, 256);
#pragma unroll
            for (int i = 0; i < 4; ++i)
#pragma unroll
                for (int t = 0; t < 2; ++t) {
                    aK[i][t] = MFMA16(wf[1][t], xf[ks][i], aK[i][t]);
                    aV[i][t] = MFMA16(xf[ks][i], wf[2][t], aV[i][t]);
                    if (i >= 2) aQ[i - 2][t] = MFMA16(wf[0][t], xf[ks][i], aQ[i - 2][t]);
                }
#pragma unroll
            for (int i = 0; i < 4; ++i) { sm[i] = frag_sum(xf[ks][i], sm[i]); sq[i] = frag_sq(xf[ks][i], sq[i]); }
            cs = frag_sum(cf, cs);
        }
        asm volatile("s_waitcnt vmcnt(0)" ::: "memory");
    }
    cs = xsum(cs);
    float mean[4], rs[4];
#pragma unroll
    for (int i = 0; i < 4; ++i) { float dn; ln_stats(xsum(sm[i]), xsum(sq[i]), mean[i], dn); rs[i] = 1.0f / dn; }
    if (lane < 16) {
        if (w < 6) *(__attribute__((address_space(3))) float*)(L + AT_CS + w * 64 + lane * 4) = cs;
#pragma unroll
        for (int i = 0; i < 4; ++i) {
            *(__attribute__((address_space(3))) float*)(L + AT_ST + w * 512 + i * 128 + lane * 8) = mean[i];
            *(__attribute__((address_space(3))) float*)(L + AT_ST + w * 512 + i * 128 + lane * 8 + 4) = rs[i];
        }
    }
    LBAR();
    bf16x8 qf[2];
    {
        f32x4 cq[2], ck[2];
        float cv[2];
#pragma unroll
        for (int t = 0; t < 2; ++t) {
            cq[t] = lds_f4(L + AT_CS + t * 64 + g * 16);
            ck[t] = lds_f4(L + AT_CS + 128 + t * 64 + g * 16);
            cv[t] = *(const __attribute__((address_space(3))) float*)(L + AT_CS + 256 + t * 64 + tk * 4);
        }
        const bool pad = (lane >= 48);
#pragma unroll
        for (int i = 0; i < 4; ++i) {
            float v[8];
#pragma unroll
            for (int t = 0; t < 2; ++t)
#pragma unroll
                for (int r = 0; r < 4; ++r) {
                    float y = fmaf(-mean[i], ck[t][r], aK[i][t][r]) * rs[i];
                    v[4 * t + r] = (t == 1 && pad) ? 0.f : y;
                }
            const int kt = (i >> 1) * 16 + 2 * w + (i & 1);
            *(__attribute__((address_space(3))) u32x4*)(L + AT_K + kt * 1024 + lane * 16) =
                u32x4{pk2(v[0], v[1]), pk2(v[2], v[3]), pk2(v[4], v[5]), pk2(v[6], v[7])};
            if (i >= 2) {
#pragma unroll
                for (int t = 0; t < 2; ++t)
#pragma unroll
                    for (int r = 0; r < 4; ++r) {
                        float y = fmaf(-mean[i], cq[t][r], aQ[i - 2][t][r]) * rs[i];
                        v[4 * t + r] = (t == 1 && pad) ? 0.f : y;
                    }
                qf[i - 2] = __builtin_bit_cast(bf16x8, u32x4{pk2(v[0], v[1]), pk2(v[2], v[3]), pk2(v[4], v[5]), pk2(v[6], v[7])});
            }
        }
#pragma unroll
        for (int b = 0; b < 2; ++b) {
            f32x4 s0[2], s1[2];
#pragma unroll
            for (int u = 0; u < 2; ++u) {
                s0[u] = lds_f4(L + AT_ST + w * 512 + (2 * b + u) * 128 + g * 32);
                s1[u] = lds_f4(L + AT_ST + w * 512 + (2 * b + u) * 128 + g * 32 + 16);
            }
#pragma unroll
            for (int t = 0; t < 2; ++t) {
                float v[8];
#pragma unroll
                for (int u = 0; u < 2; ++u) {
                    v[4 * u + 0] = fmaf(-s0[u][0], cv[t], aV[2 * b + u][t][0]) * s0[u][1];
                    v[4 * u + 1] = fmaf(-s0[u][2], cv[t], aV[2 * b + u][t][1]) * s0[u][3];
                    v[4 * u + 2] = fmaf(-s1[u][0], cv[t], aV[2 * b + u][t][2]) * s1[u][1];
                    v[4 * u + 3] = fmaf(-s1[u][2], cv[t], aV[2 * b + u][t][3]) * s1[u][3];
                }
                u32x4 o = u32x4{pk2(v[0], v[1]), pk2(v[2], v[3]), pk2(v[4], v[5]), pk2(v[6], v[7])};
                if (t == 0 && tk == 0) o = u32x4{0x3F803F80u, 0x3F803F80u, 0x3F803F80u, 0x3F803F80u};
                *(__attribute__((address_space(3))) u32x4*)(L + AT_V + ((b * 8 + w) * 2 + t) * 1024 + lane * 16) = o;
            }
        }
    }
    LBAR();
    const float NEG = -1e30f;
    f32x4 mle, mgt;
#pragma unroll
    for (int r = 0; r < 4; ++r) { mle[r] = (4 * g + r <= tk) ? 0.f : NEG; mgt[r] = (4 * g + r > tk) ? 0.f : NEG; }
    const int jmin = ch ? 0 : 16 - 2 * w;
    f32x4 Sc[2][18];
#pragma unroll
    for (int j = 0; j < 18; ++j) {
        const bf16x8 kf = lds_b128(L + AT_K + (2 * w + j) * 1024 + lane * 16);
        const float bias = (j < jmin) ? NEG : 0.f;
#pragma unroll
        for (int qt = 0; qt < 2; ++qt) {
            if (j >= qt && j <= qt + 16) {
                f32x4 c0;
                if (j == qt + 16) c0 = mle;
                else if (j == qt) c0 = mgt + bias;
                else c0 = f32x4{bias, bias, bias, bias};
                Sc[qt][j] = MFMA16(kf, qf[qt], c0);
            } else {
                Sc[qt][j] = f32x4{0.f, 0.f, 0.f, 0.f};
            }
        }
    }
    const float CS = 0.18898223650461362f * 1.4426950408889634f;
    u32x4 pf[2][9];
#pragma unroll
    for (int qt = 0; qt < 2; ++qt) {
        float m = NEG;
#pragma unroll
        for (int j = qt; j <= qt + 16; ++j) m = fmaxf(fmaxf(m, fmaxf(Sc[qt][j][0], Sc[qt][j][1])), fmaxf(Sc[qt][j][2], Sc[qt][j][3]));
        m = xmax(m);
        const float mc = -m * CS;
#pragma unroll
        for (int j = qt; j <= qt + 16; ++j)
#pragma unroll
            for (int r = 0; r < 4; ++r) Sc[qt][j][r] = __builtin_amdgcn_exp2f(fmaf(Sc[qt][j][r], CS, mc));
#pragma unroll
        for (int b = 0; b < 9; ++b)
            pf[qt][b] = u32x4{pk2(Sc[qt][2 * b][0], Sc[qt][2 * b][1]), pk2(Sc[qt][2 * b][2], Sc[qt][2 * b][3]),
                              pk2(Sc[qt][2 * b + 1][0], Sc[qt][2 * b + 1][1]), pk2(Sc[qt][2 * b + 1][2], Sc[qt][2 * b + 1][3])};
    }
    f32x4 O[2][2];
    O[0][0] = O[0][1] = O[1][0] = O[1][1] = f32x4{0.f, 0.f, 0.f, 0.f};
#pragma unroll
    for (int b = 0; b < 9; ++b)
#pragma unroll
        for (int t = 0; t < 2; ++t) {
            const bf16x8 vf = lds_b128(L + AT_V + ((w + b) * 2 + t) * 1024 + lane * 16);
#pragma unroll
            for (int qt = 0; qt < 2; ++qt) O[qt][t] = MFMA16(vf, __builtin_bit_cast(bf16x8, pf[qt][b]), O[qt][t]);
        }
#pragma unroll
    for (int qt = 0; qt < 2; ++qt) {
        const float l = __shfl(O[qt][0][0], tk);
        const float inv = 1.0f / l;
        char* dst = act + (size_t)(ch * 16 + 2 * w + qt) * ACT_TILE_B + 224 * 256 + tk * 16;
#pragma unroll
        for (int t = 0; t < 2; ++t) {
            const int u = 7 * head + 4 * t + g - 1;
            if (t == 1 || g > 0)
                *(u32x2*)(dst + (u >> 1) * 256 + (u & 1) * 8) =
                    u32x2{pk2(O[qt][t][0] * inv, O[qt][t][1] * inv), pk2(O[qt][t][2] * inv, O[qt][t][3] * inv)};
        }
    }
}

constexpr int P1_LDS = AT_END;

__global__ __launch_bounds__(512, 2) void k_phase1(const char* __restrict__ x, const char* __restrict__ wqkv,
                                                   const char* __restrict__ wg, const char* __restrict__ wu,
                                                   char* __restrict__ act) {
    extern __shared__ __attribute__((aligned(16))) char smem[];
    lds_ptr L = (lds_ptr)smem;
    const int xcd = blockIdx.x & 7, idx = blockIdx.x >> 3;
    const int rblk = 4 * (xcd & 1) + (idx & 3), cblk = 4 * (xcd >> 1) + ((idx >> 2) & 3);
    if (idx < 16) att_block(x, wqkv, act, rblk, cblk, L);
    else ffn_block(x, wg, wu, act, rblk, cblk, L);
}

constexpr int P2_LDS = 163840;

__global__ __launch_bounds__(512, 2) void k_phase2(const char* x, const char* wo, const char* wd, const char* act, char* out) {
    extern __shared__ __attribute__((aligned(16))) char smem[];
    lds_ptr L = (lds_ptr)smem;
    const unsigned lb = (unsigned)(size_t)L;
    const int tid = threadIdx.x, lane = tid & 63;
    const int w = __builtin_amdgcn_readfirstlane(tid >> 6);
    const int th = w & 1, kg = w >> 1;
    const int xcd = blockIdx.x & 7, idx = blockIdx.x >> 3;
    const int rb = 4 * xcd + (idx & 3), cb = idx >> 2;
    const int g = lane >> 4, q = (lane >> 2) & 3, p = lane & 3, tk = lane & 15;
    const int a1 = kg ? 14 * (kg - 1) : 56;
    const int a2 = 42 + (kg < 2 ? 3 * kg : 4 * kg - 2);
    const int n2 = kg < 2 ? 3 : 4;
    const char* w1 = (kg ? wd + (size_t)(14 * (kg - 1)) * 32 * 896 : wo) + cb * 128 + th * 16 * 896;
    const char* w2 = wd + (size_t)a2 * 32 * 896 + cb * 128 + th * 16 * 896;
    const unsigned wv0 = (lane >> 3) * 896 + (((lane & 7) ^ (((lane >> 4) & 1) << 1)) << 4), wv1 = wv0 ^ 64;
    const unsigned ring = lb + kg * 40960 + th * 2048;
    auto issue_w = [&](const char* src, int slot) {
        glds16s(src, wv0, ring + slot * 4096);
        glds16s(src + 8 * 896, wv1, ring + slot * 4096 + 1024);
    };
#pragma unroll
    for (int s = 0; s < 10; ++s) issue_w(w1 + s * (32 * 896), s);
    const char* ab = act + (size_t)(rb * 4 + 2 * th) * ACT_TILE_B + lane * 16;
    bf16x8 af[14][2], ag[4][2];
#pragma unroll
    for (int s = 0; s < 14; ++s)
#pragma unroll
        for (int i = 0; i < 2; ++i) af[s][i] = *(const bf16x8*)(ab + i * ACT_TILE_B + (a1 + s) * 1024);
    const int f = ((q >> 1) & 1) | ((g & 1) << 1);
    int wa[4];
#pragma unroll
    for (int t = 0; t < 4; ++t) wa[t] = kg * 40960 + (8 * g + q) * 128 + ((t ^ f) << 5) + p * 8;
    f32x4 c1[2][4], c2[2][4];
#pragma unroll
    for (int i = 0; i < 2; ++i)
#pragma unroll
        for (int t = 0; t < 4; ++t) c1[i][t] = c2[i][t] = f32x4{0.f, 0.f, 0.f, 0.f};
#define STEP(ACC, FR, SLOT)                                                          \
    {                                                                                \
        bf16x8 wf[4];                                                                \
        _Pragma("unroll") for (int t = 0; t < 4; ++t) wf[t] = lds_tr2(L + wa[t] + (SLOT) * 4096, 512); \
        _Pragma("unroll") for (int t = 0; t < 4; ++t) {                              \
            ACC[0][t] = MFMA16(wf[t], FR[0], ACC[0][t]);                             \
            ACC[1][t] = MFMA16(wf[t], FR[1], ACC[1][t]);                             \
        }                                                                            \
    }
    asm volatile("s_waitcnt vmcnt(28)\n\ts_barrier" ::: "memory");
#pragma unroll
    for (int s = 0; s < 4; ++s) STEP(c1, af[s], s)
    LBAR();
#pragma unroll
    for (int s = 10; s < 14; ++s) issue_w(w1 + s * (32 * 896), s - 10);
#pragma unroll
    for (int s = 0; s < 4; ++s) {
        const int sc = s < n2 ? s : n2 - 1;
#pragma unroll
        for (int i = 0; i < 2; ++i) ag[s][i] = *(const bf16x8*)(ab + i * ACT_TILE_B + (a2 + sc) * 1024);
    }
#pragma unroll
    for (int s = 4; s < 8; ++s) STEP(c1, af[s], s)
    LBAR();
#pragma unroll
    for (int s = 0; s < 4; ++s) issue_w(w2 + (s < n2 ? s : n2 - 1) * (32 * 896), 4 + s);
    const size_t oo = (size_t)(rb * 64 + 32 * th + tk) * 896 + cb * 128 + kg * 32 + g * 8;
    const u32x2 xr0 = *(const u32x2*)(x + oo), xr1 = *(const u32x2*)(x + oo + 16 * 896);
#pragma unroll
    for (int s = 8; s < 10; ++s) STEP(c1, af[s], s)
    asm volatile("s_waitcnt vmcnt(18) lgkmcnt(0)\n\ts_barrier" ::: "memory");
#pragma unroll
    for (int s = 10; s < 14; ++s) STEP(c1, af[s], s - 10)
    WAIT_BAR0();
#pragma unroll
    for (int s = 0; s < 3; ++s) STEP(c2, ag[s], 4 + s)
    if (n2 == 4) STEP(c2, ag[3], 7)
    LBAR();
    if (kg) {
#pragma unroll
        for (int i = 0; i < 2; ++i)
#pragma unroll
            for (int t = 0; t < 4; ++t) c2[i][t] += c1[i][t];
    } else {
#pragma unroll
        for (int i = 0; i < 2; ++i)
#pragma unroll
            for (int t = 0; t < 4; ++t)
                *(__attribute__((address_space(3))) f32x4*)(L + 65536 + th * 8192 + (i * 4 + t) * 1024 + lane * 16) = c1[i][t];
    }
#pragma unroll
    for (int i = 0; i < 2; ++i)
#pragma unroll
        for (int t = 0; t < 4; ++t)
            *(__attribute__((address_space(3))) f32x4*)(L + w * 8192 + (i * 4 + t) * 1024 + lane * 16) = c2[i][t];
    LBAR();
#pragma unroll
    for (int i = 0; i < 2; ++i) {
        f32x4 fs = f32x4{0.f, 0.f, 0.f, 0.f};
#pragma unroll
        for (int k = 0; k < 4; ++k) fs += lds_f4(L + (2 * k + th) * 8192 + (i * 4 + kg) * 1024 + lane * 16);
        const f32x4 sv = lds_f4(L + 65536 + th * 8192 + (i * 4 + kg) * 1024 + lane * 16);
        const u32x2 xr = i ? xr1 : xr0;
        float o[4];
#pragma unroll
        for (int r = 0; r < 4; ++r) {
            const float fb = rbf(fs[r]);
            const float gb = rbf(fb * __builtin_amdgcn_rcpf(1.0f + __builtin_amdgcn_exp2f(-fb * 1.4426950408889634f)));
            const float pb = rbf(rbf(sv[r]) * gb);
            const unsigned xw = xr[r >> 1];
            const float xv = __uint_as_float((r & 1) ? (xw & 0xFFFF0000u) : (xw << 16));
            o[r] = xv + pb;
        }
        *(u32x2*)(out + oo + (size_t)i * 16 * 896) = u32x2{pk2(o[0], o[1]), pk2(o[2], o[3])};
    }
}

extern "C" void kernel_launch(void* const* d_in, const int* in_sizes, int n_in, void* d_out, int out_size, void* d_ws,
                              size_t ws_size, hipStream_t stream) {
    const char* x = (const char*)d_in[0];
    const char* wqkv = (const char*)d_in[1];
    const char* wo = (const char*)d_in[2];
    const char* wg = (const char*)d_in[4];
    const char* wu = (const char*)d_in[5];
    const char* wd = (const char*)d_in[6];
    char* act = (char*)d_ws;
    (void)hipFuncSetAttribute((const void*)k_phase1, hipFuncAttributeMaxDynamicSharedMemorySize, P1_LDS);
    (void)hipFuncSetAttribute((const void*)k_phase2, hipFuncAttributeMaxDynamicSharedMemorySize, P2_LDS);
    k_phase1<<<256, 512, P1_LDS, stream>>>(x, wqkv, wg, wu, act);
    k_phase2<<<224, 512, P2_LDS, stream>>>(x, wo, wd, act, (char*)d_out);
}
```

```cpp
#include <hip/hip_runtime.h>
#include <hip/hip_bf16.h>

typedef short bf16x8 __attribute__((ext_vector_type(8)));
typedef short s16x4 __attribute__((ext_vector_type(4)));
typedef float f32x4 __attribute__((ext_vector_type(4)));
typedef unsigned u32x4 __attribute__((ext_vector_type(4)));
typedef unsigned u32x2 __attribute__((ext_vector_type(2)));
typedef __bf16 bf16x2_t __attribute__((ext_vector_type(2)));
typedef __attribute__((address_space(3))) char* lds_ptr;

#define MFMA16(a, b, c) __builtin_amdgcn_mfma_f32_16x16x32_bf16(a, b, c, 0, 0, 0)
#define WAIT_BAR0() asm volatile("s_waitcnt vmcnt(0) lgkmcnt(0)\n\ts_barrier" ::: "memory")
#define LBAR() asm volatile("s_waitcnt lgkmcnt(0)\n\ts_barrier" ::: "memory")

#define STAMP_ARG
#define STAMP_PASS
#define STAMP_DECL
#define STAMP(i)
#define STAMP_FLUSH(actp, lo, hi)
constexpr int S_ = 2048, D_ = 448, F_ = 1792, QKV_ = 1344;
constexpr int ACT_OCT = 280;
constexpr int ACT_TILE_B = ACT_OCT * 256;

__device__ __forceinline__ void glds16(const void* g, unsigned lds_dst) {
    unsigned sv;
    asm volatile("s_mov_b32 %0, m0\n\ts_mov_b32 m0, %2\n\ts_nop 0\n\tglobal_load_lds_dwordx4 %1, off\n\ts_mov_b32 m0, %0"
                 : "=&s"(sv) : "v"(g), "s"(lds_dst) : "memory");
}
__device__ __forceinline__ void glds16s(const void* sbase, unsigned voff, unsigned lds_dst) {
    unsigned sv;
    asm volatile("s_mov_b32 %0, m0\n\ts_mov_b32 m0, %3\n\ts_nop 0\n\tglobal_load_lds_dwordx4 %1, %2\n\ts_mov_b32 m0, %0"
                 : "=&s"(sv) : "v"(voff), "s"(sbase), "s"(lds_dst) : "memory");
}
__device__ __forceinline__ bf16x8 lds_b128(lds_ptr p) { return *(const __attribute__((address_space(3))) bf16x8*)p; }
__device__ __forceinline__ f32x4 lds_f4(lds_ptr p) { return *(const __attribute__((address_space(3))) f32x4*)p; }
__device__ __forceinline__ s16x4 lds_tr(lds_ptr p) {
    return __builtin_amdgcn_ds_read_tr16_b64_v4i16((__attribute__((address_space(3))) s16x4*)p);
}
__device__ __forceinline__ bf16x8 lds_tr2(lds_ptr p, int second) {
    s16x4 a = lds_tr(p), b = lds_tr(p + second);
    return __builtin_shufflevector(a, b, 0, 1, 2, 3, 4, 5, 6, 7);
}
__device__ __forceinline__ float dot2(unsigned a, unsigned b, float c) {
    return __builtin_amdgcn_fdot2_f32_bf16(__builtin_bit_cast(bf16x2_t, a), __builtin_bit_cast(bf16x2_t, b), c, false);
}
__device__ __forceinline__ float frag_sum(bf16x8 f, float c) {
    u32x4 u = __builtin_bit_cast(u32x4, f);
    c = dot2(u[0], 0x3F803F80u, c); c = dot2(u[1], 0x3F803F80u, c);
    c = dot2(u[2], 0x3F803F80u, c); c = dot2(u[3], 0x3F803F80u, c);
    return c;
}
__device__ __forceinline__ float frag_sq(bf16x8 f, float c) {
    u32x4 u = __builtin_bit_cast(u32x4, f);
    c = dot2(u[0], u[0], c); c = dot2(u[1], u[1], c); c = dot2(u[2], u[2], c); c = dot2(u[3], u[3], c);
    return c;
}
__device__ __forceinline__ float xsum(float x) {
    auto s = __builtin_amdgcn_permlane16_swap(__float_as_uint(x), __float_as_uint(x), false, false);
    x = __uint_as_float(s[0]) + __uint_as_float(s[1]);
    auto t = __builtin_amdgcn_permlane32_swap(__float_as_uint(x), __float_as_uint(x), false, false);
    return __uint_as_float(t[0]) + __uint_as_float(t[1]);
}
__device__ __forceinline__ float xmax(float x) {
    auto s = __builtin_amdgcn_permlane16_swap(__float_as_uint(x), __float_as_uint(x), false, false);
    x = fmaxf(__uint_as_float(s[0]), __uint_as_float(s[1]));
    auto t = __builtin_amdgcn_permlane32_swap(__float_as_uint(x), __float_as_uint(x), false, false);
    return fmaxf(__uint_as_float(t[0]), __uint_as_float(t[1]));
}
__device__ __forceinline__ unsigned pk2(float lo, float hi) {
    bf16x2_t v; v[0] = (__bf16)lo; v[1] = (__bf16)hi;
    return __builtin_bit_cast(unsigned, v);
}
__device__ __forceinline__ float rbf(float x) { return (float)(__bf16)x; }
__device__ __forceinline__ void ln_stats(float sum, float sq, float& mean, float& den) {
    float m = sum * (1.0f / D_);
    float v = fmaxf(sq * (1.0f / D_) - m * m, 0.0f);
    mean = rbf(m);
    den = rbf(sqrtf(rbf(rbf(v) + 1e-5f)));
}

typedef __attribute__((address_space(1))) unsigned long long gu64;
#define RLX_AGENT __ATOMIC_RELAXED, __HIP_MEMORY_SCOPE_AGENT
#define SB() __builtin_amdgcn_sched_barrier(0)
#define LDSF(p) (*(__attribute__((address_space(3))) float*)(p))
#define LDSV(T, p) (*(__attribute__((address_space(3))) T*)(p))
constexpr size_t SYNC_OFF = 16u << 20, FLAG_OFF = SYNC_OFF + 4096, STAT_OFF = SYNC_OFF + 65536;
constexpr int ACT_BYTES = 128 * ACT_TILE_B;
constexpr unsigned long long SPIN_TICKS = 20000000ull;

template <int N> __device__ __forceinline__ float ror_add(float x) {
    return x + __uint_as_float(__builtin_amdgcn_update_dpp(0u, __float_as_uint(x), 0x120 + N, 0xF, 0xF, false));
}
__device__ __forceinline__ float wave_sum(float x) { return xsum(ror_add<1>(ror_add<2>(ror_add<4>(ror_add<8>(x))))); }

__device__ __forceinline__ void start_loads(const char* x, char* ws, int row, int lane, unsigned long long& ep, u32x4& rv) {
    const unsigned zero = 0, ro = (lane < 56 ? lane : 55) * 16;
    asm volatile("global_load_dwordx2 %0, %1, %2 sc1" : "=v"(ep) : "v"(zero), "s"(ws + SYNC_OFF) : "memory");
    asm volatile("global_load_dwordx4 %0, %1, %2" : "=v"(rv) : "v"(ro), "s"(x + (size_t)row * 896) : "memory");
}
__device__ __forceinline__ unsigned long long publish_stats(char* ws, int row, int lane, unsigned long long ep, u32x4 rv) {
    const unsigned long long tag = (((unsigned long long)__builtin_amdgcn_readfirstlane((unsigned)(ep >> 32)) << 32) |
                                    (unsigned)__builtin_amdgcn_readfirstlane((unsigned)ep)) + 1ull;
    if (lane >= 56) rv = u32x4{0u, 0u, 0u, 0u};
    const bf16x8 f = __builtin_bit_cast(bf16x8, rv);
    float mean, den;
    ln_stats(wave_sum(frag_sum(f, 0.f)), wave_sum(frag_sq(f, 0.f)), mean, den);
    const unsigned val = (__float_as_uint(mean) & 0xFFFF0000u) | (__float_as_uint(den) >> 16);
    if (lane == 0) __hip_atomic_store((gu64*)(ws + STAT_OFF) + row, ((unsigned long long)(unsigned)tag << 32) | val, RLX_AGENT);
    return tag;
}
__device__ __forceinline__ unsigned fetch_stats(char* ws, int row, unsigned long long tag) {
    const unsigned long long t0 = __builtin_amdgcn_s_memrealtime();
    unsigned long long v;
    for (;;) {
        v = __hip_atomic_load((gu64*)(ws + STAT_OFF) + row, RLX_AGENT);
        if (__all((unsigned)(v >> 32) == (unsigned)tag)) break;
        if (__builtin_amdgcn_s_memrealtime() - t0 > SPIN_TICKS) break;
        __builtin_amdgcn_s_sleep(2);
    }
    return (unsigned)v;
}
__device__ __forceinline__ void wait_flags(char* ws, int first, int n, int lane, unsigned long long tag) {
    const unsigned long long t0 = __builtin_amdgcn_s_memrealtime();
    const int i = first + (lane < n ? lane : n - 1);
    for (;;) {
        const unsigned long long v = __hip_atomic_load((gu64*)(ws + FLAG_OFF + (size_t)i * 64), RLX_AGENT);
        if (__all(v == tag)) break;
        if (__builtin_amdgcn_s_memrealtime() - t0 > SPIN_TICKS) break;
        __builtin_amdgcn_s_sleep(1);
    }
}

struct P2 {
    const char* w1; const char* w2; unsigned wv0, ring; int n2;
};
__device__ __forceinline__ void p2_dma(const P2& P, const char* src, int slot, int half) {
    glds16s(src + half * (8 * 896), half ? (P.wv0 ^ 64) : P.wv0, P.ring + slot * 4096 + half * 1024);
}

constexpr int FF_XS = 32768, FF_UOFF = 15376, FF_WS = 2 * FF_UOFF, FF_W0 = 2 * FF_XS, FF_CS = 163840 - 1024;

__device__ __forceinline__ unsigned long long ffn_block(const char* x, const char* wg, const char* wu, char* ws, int rb, int cb,
                                                        lds_ptr L, const P2& P, bool has_p2 STAMP_ARG) {
    const int tid = threadIdx.x, lane = tid & 63;
    const int w = __builtin_amdgcn_readfirstlane(tid >> 6);
    const int wm = w & 3, wn = w >> 2;
    const unsigned lb = (unsigned)(size_t)L;
    unsigned long long ep; u32x4 rv;
    const int srow = rb * 256 + (16 + cb) * 8 + w;
    start_loads(x, ws, srow, lane, ep, rv);
    const unsigned xo0 = (lane >> 3) * 896 + (((lane & 7) ^ (lane >> 4)) << 4), xo1 = xo0 ^ 64;
    const char* xb = x + (size_t)(rb * 256 + 32 * w) * 896;
    auto dma_x = [&](int c, int j) { glds16s(xb + j * (8 * 896) + c * 128, (j & 1) ? xo1 : xo0, lb + (c & 1) * FF_XS + (32 * w + 8 * j) * 128); };
#pragma unroll
    for (int j = 0; j < 4; ++j) dma_x(0, j);
    unsigned woff[4];
    const char* wsrc[4];
    unsigned wdst[4];
#pragma unroll
    for (int j = 0; j < 4; ++j) {
        int n = w + 8 * j; if (n > 29) n = 29;
        const int mat = n >= 15, nn = n - 15 * mat;
        const int ci = nn * 64 + lane, grp = ci / 120;
        int rem = ci - grp * 120; if (rem >= 112) rem -= 8;
        const int r = rem / 14, chn = rem - r * 14;
        woff[j] = (8 * grp + r) * (F_ * 2) + chn * 16;
        wsrc[j] = (mat ? wu : wg) + cb * 224;
        wdst[j] = lb + FF_W0 + mat * FF_UOFF + nn * 1024;
    }
    auto dma_w = [&](int c, int buf, int j) { if (j < 3 || w < 6) glds16s(wsrc[j] + (size_t)c * (64 * F_ * 2), woff[j], wdst[j] + buf); };
#pragma unroll
    for (int j = 0; j < 4; ++j) dma_w(0, 0, j);
#pragma unroll
    for (int j = 0; j < 4; ++j) dma_w(1, FF_WS, j);
    dma_x(1, 0); dma_x(1, 1);
    asm volatile("s_waitcnt vmcnt(12)" : "+v"(ep), "+v"(rv) :: "memory");
    const unsigned long long tag = publish_stats(ws, srow, lane, ep, rv);
    const int g = lane >> 4, q = (lane >> 2) & 3, p = lane & 3, tk = lane & 15;
    const int xa = (64 * wm + tk) * 128 + ((g ^ ((lane >> 1) & 7)) << 4);
    const int wa = FF_W0 + g * 1920 + q * 224 + (p & 1) * 8 + (p >> 1) * FF_UOFF + wn * 112;
    const int tc1 = (wm + 4 > 6) ? 6 : wm + 4;
    f32x4 acc[4][7];
#pragma unroll
    for (int i = 0; i < 4; ++i)
#pragma unroll
        for (int t = 0; t < 7; ++t) acc[i][t] = f32x4{0.f, 0.f, 0.f, 0.f};
    f32x4 cs0 = f32x4{0.f, 0.f, 0.f, 0.f}, cs1 = cs0;
    const bf16x8 ones = __builtin_bit_cast(bf16x8, u32x4{0x3F803F80u, 0x3F803F80u, 0x3F803F80u, 0x3F803F80u});
    bf16x8 xf[2][4], wf[3], cf;
    if (w < 6) asm volatile("s_waitcnt vmcnt(7)\n\ts_barrier" ::: "memory");
    else asm volatile("s_waitcnt vmcnt(6)\n\ts_barrier" ::: "memory");
    STAMP(1)
#define XRD(B, ks, i) xf[ks][i] = lds_b128((B) + (xa ^ ((ks) * 64)) + (i) * 2048)
#define WRD(B, s) lds_tr2((B) + ((s) / 7) * 7680 + ((s) % 7) * 16, 896)
    {
        lds_ptr W = L + wa;
        XRD(L, 0, 0); XRD(L, 0, 1); XRD(L, 0, 2); XRD(L, 0, 3);
        wf[0] = WRD(W, 0); wf[1] = WRD(W, 1);
    }
    int wb = 0;
#pragma unroll 1
    for (int c = 0; c < 7; ++c) {
        const int wb1 = (wb == 2 * FF_WS) ? 0 : wb + FF_WS, wb2 = (wb == 0) ? 2 * FF_WS : wb - FF_WS;
        lds_ptr X = L + (c & 1) * FF_XS, Xn = L + ((c + 1) & 1) * FF_XS, W = L + wa + wb, Wn = L + wa + wb1;
#pragma unroll
        for (int s = 0; s < 14; ++s) {
            const int ks = s / 7, t = s % 7;
            if (s == 12) {
                if (c < 5) {
                    if (w < 6) asm volatile("s_waitcnt vmcnt(4) lgkmcnt(0)\n\ts_barrier" ::: "memory");
                    else asm volatile("s_waitcnt vmcnt(3) lgkmcnt(0)\n\ts_barrier" ::: "memory");
                } else WAIT_BAR0();
            }
            if (s < 12) wf[(s + 2) % 3] = WRD(W, s + 2); else wf[(s + 2) % 3] = WRD(Wn, s - 12);
            if (s < 4) XRD(X, 1, s);
            if (s == 12) { XRD(Xn, 0, 0); XRD(Xn, 0, 1); }
            if (s == 13) { XRD(Xn, 0, 2); XRD(Xn, 0, 3); }
            if (s == 4 || s == 6 || s == 8 || s == 10) cf = lds_tr2(W + (s >= 8) * 7680 + ((s == 4 || s == 8) ? wm : tc1) * 16, 896);
            if (s < 2) { if (c < 6) dma_x(c + 1, 2 + s); }
            if (s >= 2 && s < 6) { if (c < 5) dma_w(c + 2, wb2, s - 2); }
            if (s >= 12) { if (c < 5) dma_x(c + 2, s - 12); }
#pragma unroll
            for (int i = 0; i < 4; ++i) acc[i][t] = MFMA16(wf[s % 3], xf[ks][i], acc[i][t]);
            if (s == 5 || s == 9) cs0 = MFMA16(cf, ones, cs0);
            if (s == 7 || s == 11) cs1 = MFMA16(cf, ones, cs1);
            SB();
        }
        { bf16x8 a = wf[2], b = wf[0]; wf[0] = a; wf[1] = b; }
        wb = wb1;
    }
    STAMP(4)
    if (tk == 0) {
        LDSV(f32x4, L + FF_CS + (7 * wn + wm) * 64 + g * 16) = cs0;
        if (wm < 3) LDSV(f32x4, L + FF_CS + (7 * wn + wm + 4) * 64 + g * 16) = cs1;
    }
    const unsigned sv = fetch_stats(ws, rb * 256 + 64 * wm + lane, tag);
    LBAR();
    STAMP(5)
    const __amdgpu_buffer_rsrc_t ra = __builtin_amdgcn_make_buffer_rsrc(ws, (short)0, ACT_BYTES, 0x00020000);
    const float K1 = -2.3022081986f, K2 = -0.1029432398f;
#pragma unroll
    for (int ip = 0; ip < 2; ++ip) {
        const unsigned sa = __shfl(sv, 32 * ip + tk), sb = __shfl(sv, 32 * ip + 16 + tk);
        const float ma = -__uint_as_float(sa & 0xFFFF0000u), mb = -__uint_as_float(sb & 0xFFFF0000u);
        const float dn = __uint_as_float((lane < 32 ? sa : sb) << 16);
        const float a = __builtin_amdgcn_rcpf(dn), ia2 = dn * dn, c1 = K1 * a, c2 = K2 * a * a * a;
        const unsigned dst = (rb * 16 + wm * 4 + 2 * ip + (lane >> 5)) * ACT_TILE_B + (cb * 14 + wn * 7) * 256 + tk * 16;
        u32x2 hp[7];
#pragma unroll
        for (int t = 0; t < 7; ++t) {
            const f32x4 cs = lds_f4(L + FF_CS + (7 * wn + t) * 64 + g * 16);
            float h[4];
#pragma unroll
            for (int r = 0; r < 4; ++r) {
                float va = fmaf(ma, cs[r], acc[2 * ip][t][r]);
                float vb = fmaf(mb, cs[r], acc[2 * ip + 1][t][r]);
                auto sw = __builtin_amdgcn_permlane32_swap(__float_as_uint(va), __float_as_uint(vb), false, false);
                const float G = __uint_as_float(sw[0]), U = __uint_as_float(sw[1]);
                const float e = __builtin_amdgcn_exp2f(G * fmaf(G * G, c2, c1));
                h[r] = (G * U) * __builtin_amdgcn_rcpf(fmaf(e, ia2, ia2));
            }
            hp[t] = u32x2{pk2(h[0], h[1]), pk2(h[2], h[3])};
            if (t & 1) {
                auto s0 = __builtin_amdgcn_permlane16_swap(hp[t - 1][0], hp[t][0], false, false);
                auto s1 = __builtin_amdgcn_permlane16_swap(hp[t - 1][1], hp[t][1], false, false);
                __builtin_amdgcn_raw_buffer_store_b128(u32x4{s0[0], s1[0], s0[1], s1[1]}, ra, dst + (g & 1) * 256, (t - 1) * 256, 16);
            }
            if (t == 6) __builtin_amdgcn_raw_buffer_store_b64(hp[6], ra, dst + (g & 1) * 8, 6 * 256, 16);
            if (has_p2) {
                const int k = ip * 7 + t;
                if (k < 9) { p2_dma(P, P.w1 + k * (32 * 896), k, 0); p2_dma(P, P.w1 + k * (32 * 896), k, 1); }
            }
            SB();
        }
    }
    STAMP(6)
    WAIT_BAR0();
    if (tid == 0) __hip_atomic_store((gu64*)(ws + FLAG_OFF + (size_t)(rb * 32 + 16 + cb) * 64), tag, RLX_AGENT);
    if (has_p2) { p2_dma(P, P.w1 + 9 * (32 * 896), 9, 0); p2_dma(P, P.w1 + 9 * (32 * 896), 9, 1); }
    STAMP(7)
    return tag;
}

constexpr int AT_WM = 28672, AT_CS = 3 * AT_WM, AT_ST = AT_CS + 512;
constexpr int AT_K = 0, AT_V = 32768;

__device__ __forceinline__ unsigned long long att_block(const char* x, const char* wqkv, char* ws, int ch, int head, lds_ptr L,
                                                        const P2& P STAMP_ARG) {
    const int tid = threadIdx.x, lane = tid & 63;
    const int w = __builtin_amdgcn_readfirstlane(tid >> 6);
    const unsigned lb = (unsigned)(size_t)L;
    const int g = lane >> 4, q = (lane >> 2) & 3, p = lane & 3, tk = lane & 15;
    unsigned long long ep; u32x4 rv;
    const int srow = ch * 256 + head * 8 + w;
    start_loads(x, ws, srow, lane, ep, rv);
    const unsigned wo = (lane >> 2) * (QKV_ * 2) + (((lane & 3) ^ (((lane >> 5) & 1) << 1)) << 4);
    auto dma_w = [&](int j) {
        const int n = w + 8 * j, rblk = (n * 43) >> 7, mat = n - rblk * 3;
        glds16s(wqkv + rblk * (16 * QKV_ * 2) + mat * 896 + head * 56 - (mat == 2 ? 8 : 0), wo, lb + mat * AT_WM + rblk * 1024);
    };
    const int hrow = (ch ? ch - 1 : 0) * 256 + 32 * w, orow = ch * 256 + 32 * w;
    const unsigned xo = tk * 896 + g * 16;
    const char* xh = x + (size_t)hrow * 896 + xo;
    const char* xs = x + (size_t)orow * 896 + xo;
    bf16x8 xf[14][4];
#define XLD(ks) { xf[ks][0] = *(const bf16x8*)(xh + (ks) * 64); xf[ks][1] = *(const bf16x8*)(xh + 16 * 896 + (ks) * 64); \
                  xf[ks][2] = *(const bf16x8*)(xs + (ks) * 64); xf[ks][3] = *(const bf16x8*)(xs + 16 * 896 + (ks) * 64); SB(); }
#pragma unroll
    for (int j = 0; j < 5; ++j) dma_w(j);
    XLD(0) XLD(1) XLD(2)
#pragma unroll
    for (int j = 5; j < 10; ++j) dma_w(j);
    if (w < 4) dma_w(10);
    XLD(3) XLD(4) XLD(5)
    asm volatile("s_waitcnt vmcnt(34)" : "+v"(ep), "+v"(rv) :: "memory");
    const unsigned long long tag = publish_stats(ws, srow, lane, ep, rv);
    const int wA0 = (8 * g + q) * 64 + ((g & 1) << 5) + p * 8, wA1 = wA0 ^ 32;
    const int cidx = w > 5 ? 5 : w;
    const int wC = (cidx >> 1) * AT_WM + (wA0 ^ ((cidx & 1) << 5));
    f32x4 aK[4][2], aV[4][2], aQ[2][2], cs = f32x4{0.f, 0.f, 0.f, 0.f};
#pragma unroll
    for (int i = 0; i < 4; ++i) { aK[i][0] = aK[i][1] = aV[i][0] = aV[i][1] = f32x4{0.f, 0.f, 0.f, 0.f}; }
    aQ[0][0] = aQ[0][1] = aQ[1][0] = aQ[1][1] = f32x4{0.f, 0.f, 0.f, 0.f};
    const bf16x8 ones = __builtin_bit_cast(bf16x8, u32x4{0x3F803F80u, 0x3F803F80u, 0x3F803F80u, 0x3F803F80u});
    asm volatile("s_waitcnt vmcnt(30)\n\ts_barrier" ::: "memory");
    STAMP(1)
#pragma unroll
    for (int ks = 0; ks < 14; ++ks) {
        if (ks == 6) asm volatile("s_waitcnt vmcnt(24)\n\ts_barrier" ::: "memory");
        bf16x8 wf[3][2];
#pragma unroll
        for (int m = 0; m < 3; ++m) {
            wf[m][0] = lds_tr2(L + wA0 + m * AT_WM + ks * 2048, 256);
            wf[m][1] = lds_tr2(L + wA1 + m * AT_WM + ks * 2048, 256);
        }
        bf16x8 cf = lds_tr2(L + wC + ks * 2048, 256);
#pragma unroll
        for (int i = 0; i < 4; ++i)
#pragma unroll
            for (int t = 0; t < 2; ++t) {
                aK[i][t] = MFMA16(wf[1][t], xf[ks][i], aK[i][t]);
                aV[i][t] = MFMA16(xf[ks][i], wf[2][t], aV[i][t]);
                if (i >= 2) aQ[i - 2][t] = MFMA16(wf[0][t], xf[ks][i], aQ[i - 2][t]);
            }
        cs = MFMA16(cf, ones, cs);
        SB();
        if (ks + 6 < 14) XLD(ks + 6)
    }
    STAMP(4)
    if (tk == 0 && w < 6) LDSV(f32x4, L + AT_CS + w * 64 + g * 16) = cs;
    {
        const unsigned sv = fetch_stats(ws, (lane < 32 ? hrow : orow - 32) + lane, tag);
        LDSF(L + AT_ST + w * 512 + lane * 8) = -__uint_as_float(sv & 0xFFFF0000u);
        LDSF(L + AT_ST + w * 512 + lane * 8 + 4) = __builtin_amdgcn_rcpf(__uint_as_float(sv << 16));
    }
    LBAR();
    bf16x8 qf[2];
    {
        f32x4 cq[2], ck[2];
        float cv[2];
#pragma unroll
        for (int t = 0; t < 2; ++t) {
            cq[t] = lds_f4(L + AT_CS + t * 64 + g * 16);
            ck[t] = lds_f4(L + AT_CS + 128 + t * 64 + g * 16);
            cv[t] = LDSF(L + AT_CS + 256 + t * 64 + tk * 4);
        }
        const bool pad = (lane >= 48);
#pragma unroll
        for (int i = 0; i < 4; ++i) {
            const float nm = LDSF(L + AT_ST + w * 512 + i * 128 + tk * 8), rs = LDSF(L + AT_ST + w * 512 + i * 128 + tk * 8 + 4);
            const float rs1 = pad ? 0.f : rs;
            float v[8];
#pragma unroll
            for (int t = 0; t < 2; ++t)
#pragma unroll
                for (int r = 0; r < 4; ++r) v[4 * t + r] = fmaf(nm, ck[t][r], aK[i][t][r]) * (t ? rs1 : rs);
            const int kt = (i >> 1) * 16 + 2 * w + (i & 1);
            LDSV(u32x4, L + AT_K + kt * 1024 + lane * 16) = u32x4{pk2(v[0], v[1]), pk2(v[2], v[3]), pk2(v[4], v[5]), pk2(v[6], v[7])};
            if (i >= 2) {
#pragma unroll
                for (int t = 0; t < 2; ++t)
#pragma unroll
                    for (int r = 0; r < 4; ++r) v[4 * t + r] = fmaf(nm, cq[t][r], aQ[i - 2][t][r]) * (t ? rs1 : rs);
                qf[i - 2] = __builtin_bit_cast(bf16x8, u32x4{pk2(v[0], v[1]), pk2(v[2], v[3]), pk2(v[4], v[5]), pk2(v[6], v[7])});
            }
        }
#pragma unroll
        for (int b = 0; b < 2; ++b) {
            f32x4 s0[2], s1[2];
#pragma unroll
            for (int u = 0; u < 2; ++u) {
                s0[u] = lds_f4(L + AT_ST + w * 512 + (2 * b + u) * 128 + g * 32);
                s1[u] = lds_f4(L + AT_ST + w * 512 + (2 * b + u) * 128 + g * 32 + 16);
            }
#pragma unroll
            for (int t = 0; t < 2; ++t) {
                float v[8];
#pragma unroll
                for (int u = 0; u < 2; ++u) {
                    v[4 * u + 0] = fmaf(s0[u][0], cv[t], aV[2 * b + u][t][0]) * s0[u][1];
                    v[4 * u + 1] = fmaf(s0[u][2], cv[t], aV[2 * b + u][t][1]) * s0[u][3];
                    v[4 * u + 2] = fmaf(s1[u][0], cv[t], aV[2 * b + u][t][2]) * s1[u][1];
                    v[4 * u + 3] = fmaf(s1[u][2], cv[t], aV[2 * b + u][t][3]) * s1[u][3];
                }
                u32x4 o = u32x4{pk2(v[0], v[1]), pk2(v[2], v[3]), pk2(v[4], v[5]), pk2(v[6], v[7])};
                if (t == 0 && tk == 0) o = u32x4{0x3F803F80u, 0x3F803F80u, 0x3F803F80u, 0x3F803F80u};
                LDSV(u32x4, L + AT_V + ((b * 8 + w) * 2 + t) * 1024 + lane * 16) = o;
            }
        }
    }
    LBAR();
    STAMP(5)
    const float NEG = -1e30f;
    f32x4 mle, mgt;
#pragma unroll
    for (int r = 0; r < 4; ++r) { mle[r] = (4 * g + r <= tk) ? 0.f : NEG; mgt[r] = (4 * g + r > tk) ? 0.f : NEG; }
    const int jmin = ch ? 0 : 16 - 2 * w;
    f32x4 Sc[2][18];
#pragma unroll
    for (int j = 0; j < 18; ++j) {
        const bf16x8 kf = lds_b128(L + AT_K + (2 * w + j) * 1024 + lane * 16);
        const float bias = (j < jmin) ? NEG : 0.f;
#pragma unroll
        for (int qt = 0; qt < 2; ++qt) {
            if (j >= qt && j <= qt + 16) {
                f32x4 c0;
                if (j == qt + 16) c0 = mle;
                else if (j == qt) c0 = mgt + bias;
                else c0 = f32x4{bias, bias, bias, bias};
                Sc[qt][j] = MFMA16(kf, qf[qt], c0);
            } else {
                Sc[qt][j] = f32x4{0.f, 0.f, 0.f, 0.f};
            }
        }
    }
    const float CS = 0.18898223650461362f * 1.4426950408889634f;
    u32x4 pf[2][9];
#pragma unroll
    for (int qt = 0; qt < 2; ++qt) {
        float m = NEG;
#pragma unroll
        for (int j = qt; j <= qt + 16; ++j) m = fmaxf(fmaxf(m, fmaxf(Sc[qt][j][0], Sc[qt][j][1])), fmaxf(Sc[qt][j][2], Sc[qt][j][3]));
        m = xmax(m);
        const float mc = -m * CS;
#pragma unroll
        for (int j = qt; j <= qt + 16; ++j)
#pragma unroll
            for (int r = 0; r < 4; ++r) Sc[qt][j][r] = __builtin_amdgcn_exp2f(fmaf(Sc[qt][j][r], CS, mc));
#pragma unroll
        for (int b = 0; b < 9; ++b)
            pf[qt][b] = u32x4{pk2(Sc[qt][2 * b][0], Sc[qt][2 * b][1]), pk2(Sc[qt][2 * b][2], Sc[qt][2 * b][3]),
                              pk2(Sc[qt][2 * b + 1][0], Sc[qt][2 * b + 1][1]), pk2(Sc[qt][2 * b + 1][2], Sc[qt][2 * b + 1][3])};
    }
    f32x4 O[2][2];
    O[0][0] = O[0][1] = O[1][0] = O[1][1] = f32x4{0.f, 0.f, 0.f, 0.f};
#pragma unroll
    for (int b = 0; b < 9; ++b)
#pragma unroll
        for (int t = 0; t < 2; ++t) {
            const bf16x8 vf = lds_b128(L + AT_V + ((w + b) * 2 + t) * 1024 + lane * 16);
#pragma unroll
            for (int qt = 0; qt < 2; ++qt) O[qt][t] = MFMA16(vf, __builtin_bit_cast(bf16x8, pf[qt][b]), O[qt][t]);
        }
    const __amdgpu_buffer_rsrc_t ra = __builtin_amdgcn_make_buffer_rsrc(ws, (short)0, ACT_BYTES, 0x00020000);
#pragma unroll
    for (int qt = 0; qt < 2; ++qt) {
        const float l = __shfl(O[qt][0][0], tk);
        const float inv = __builtin_amdgcn_rcpf(l);
        const unsigned dst = (ch * 16 + 2 * w + qt) * ACT_TILE_B + 224 * 256 + tk * 16;
#pragma unroll
        for (int t = 0; t < 2; ++t) {
            const int u = 7 * head + 4 * t + g - 1;
            if (t == 1 || g > 0)
                __builtin_amdgcn_raw_buffer_store_b64(
                    u32x2{pk2(O[qt][t][0] * inv, O[qt][t][1] * inv), pk2(O[qt][t][2] * inv, O[qt][t][3] * inv)}, ra,
                    dst + (u >> 1) * 256 + (u & 1) * 8, 0, 16);
        }
    }
    STAMP(6)
    WAIT_BAR0();
    if (tid == 0) __hip_atomic_store((gu64*)(ws + FLAG_OFF + (size_t)(ch * 32 + head) * 64), tag, RLX_AGENT);
#pragma unroll
    for (int k = 0; k < 10; ++k) { p2_dma(P, P.w1 + k * (32 * 896), k, 0); p2_dma(P, P.w1 + k * (32 * 896), k, 1); }
    STAMP(7)
    return tag;
}

constexpr int LDS_BYTES = 163840;

__global__ __launch_bounds__(512, 2) void k_fused(const char* x, const char* wqkv, const char* wo, const char* wg, const char* wu,
                                                  const char* wd, char* out, char* ws) {
    extern __shared__ __attribute__((aligned(16))) char smem[];
    lds_ptr L = (lds_ptr)smem;
    const unsigned lb = (unsigned)(size_t)L;
    STAMP_DECL STAMP(0)
    const int tid = threadIdx.x, lane = tid & 63;
    const int w = __builtin_amdgcn_readfirstlane(tid >> 6);
    const int xcd = blockIdx.x & 7, idx = blockIdx.x >> 3;
    const int rblk = 4 * (xcd & 1) + (idx & 3), cblk = 4 * (xcd >> 1) + ((idx >> 2) & 3);
    const int th = w & 1, kg = w >> 1;
    const bool has_p2 = idx < 28;
    const int rb = 4 * xcd + (idx & 3), cb = has_p2 ? (idx >> 2) : 0;
    const int a1 = kg ? 14 * (kg - 1) : 56;
    const int a2 = 42 + (kg < 2 ? 3 * kg : 4 * kg - 2);
    P2 P;
    P.n2 = kg < 2 ? 3 : 4;
    P.w1 = (kg ? wd + (size_t)(14 * (kg - 1)) * 32 * 896 : wo) + cb * 128 + th * 16 * 896;
    P.w2 = wd + (size_t)a2 * 32 * 896 + cb * 128 + th * 16 * 896;
    P.wv0 = (lane >> 3) * 896 + (((lane & 7) ^ (((lane >> 4) & 1) << 1)) << 4);
    P.ring = lb + kg * 40960 + th * 2048;
    unsigned long long tag;
    if (idx < 16) tag = att_block(x, wqkv, ws, rblk, cblk, L, P STAMP_PASS);
    else tag = ffn_block(x, wg, wu, ws, rblk, cblk, L, P, has_p2 STAMP_PASS);
    if (!has_p2) {
        if (blockIdx.x == 255 && w == 0) {
#pragma unroll 1
            for (int i = 0; i < 4; ++i) wait_flags(ws, 64 * i, 64, lane, tag);
            if (lane == 0) __hip_atomic_store((gu64*)(ws + SYNC_OFF), tag, RLX_AGENT);
        }
        STAMP_FLUSH(ws, 0, 8)
        return;
    }
    const int g = lane >> 4, q = (lane >> 2) & 3, p = lane & 3, tk = lane & 15;
    const size_t oo = (size_t)(rb * 64 + 32 * th + tk) * 896 + cb * 128 + kg * 32 + g * 8;
    const u32x2 xr0 = *(const u32x2*)(x + oo), xr1 = *(const u32x2*)(x + oo + 16 * 896);
    STAMP(8)
    if (w == 0) wait_flags(ws, (rb >> 2) * 32, 32, lane, tag);
    asm volatile("s_waitcnt vmcnt(0)\n\ts_barrier" ::: "memory");
    STAMP(9)
    const __amdgpu_buffer_rsrc_t ra = __builtin_amdgcn_make_buffer_rsrc(ws, (short)0, ACT_BYTES, 0x00020000);
    const unsigned ab = (rb * 4 + 2 * th) * ACT_TILE_B + lane * 16;
    bf16x8 af[14][2], ag[4][2];
#define ALD(ks, i) __builtin_bit_cast(bf16x8, __builtin_amdgcn_raw_buffer_load_b128(ra, ab + (i) * ACT_TILE_B, (ks) * 1024, 16))
#pragma unroll
    for (int s = 0; s < 14; ++s) { af[s][0] = ALD(a1 + s, 0); af[s][1] = ALD(a1 + s, 1); SB(); }
    const int f = ((q >> 1) & 1) | ((g & 1) << 1);
    int wa[4];
#pragma unroll
    for (int t = 0; t < 4; ++t) wa[t] = kg * 40960 + (8 * g + q) * 128 + ((t ^ f) << 5) + p * 8;
    f32x4 c1[2][4], c2[2][4];
#pragma unroll
    for (int i = 0; i < 2; ++i)
#pragma unroll
        for (int t = 0; t < 4; ++t) c1[i][t] = c2[i][t] = f32x4{0.f, 0.f, 0.f, 0.f};
#define STEP(ACC, FR, SLOT)                                                          \
    {                                                                                \
        bf16x8 wf[4];                                                                \
        _Pragma("unroll") for (int t = 0; t < 4; ++t) wf[t] = lds_tr2(L + wa[t] + (SLOT) * 4096, 512); \
        _Pragma("unroll") for (int t = 0; t < 4; ++t) {                              \
            ACC[0][t] = MFMA16(wf[t], FR[0], ACC[0][t]);                             \
            ACC[1][t] = MFMA16(wf[t], FR[1], ACC[1][t]);                             \
        }                                                                            \
    }
    STAMP(10)
#pragma unroll
    for (int s = 0; s < 4; ++s) STEP(c1, af[s], s)
    LBAR();
#pragma unroll
    for (int s = 10; s < 14; ++s) { p2_dma(P, P.w1 + s * (32 * 896), s - 10, 0); p2_dma(P, P.w1 + s * (32 * 896), s - 10, 1); }
#pragma unroll
    for (int s = 0; s < 4; ++s) {
        const int sc = s < P.n2 ? s : P.n2 - 1;
        ag[s][0] = ALD(a2 + sc, 0); ag[s][1] = ALD(a2 + sc, 1);
    }
#pragma unroll
    for (int s = 4; s < 8; ++s) STEP(c1, af[s], s)
    LBAR();
    STAMP(11)
#pragma unroll
    for (int s = 0; s < 4; ++s) {
        const char* src = P.w2 + (s < P.n2 ? s : P.n2 - 1) * (32 * 896);
        p2_dma(P, src, 4 + s, 0); p2_dma(P, src, 4 + s, 1);
    }
#pragma unroll
    for (int s = 8; s < 10; ++s) STEP(c1, af[s], s)
    asm volatile("s_waitcnt vmcnt(16) lgkmcnt(0)\n\ts_barrier" ::: "memory");
#pragma unroll
    for (int s = 10; s < 14; ++s) STEP(c1, af[s], s - 10)
    STAMP(12)
    WAIT_BAR0();
    STAMP(13)
#pragma unroll
    for (int s = 0; s < 3; ++s) STEP(c2, ag[s], 4 + s)
    if (P.n2 == 4) STEP(c2, ag[3], 7)
    LBAR();
    STAMP(14)
    if (kg) {
#pragma unroll
        for (int i = 0; i < 2; ++i)
#pragma unroll
            for (int t = 0; t < 4; ++t) c2[i][t] += c1[i][t];
    } else {
#pragma unroll
        for (int i = 0; i < 2; ++i)
#pragma unroll
            for (int t = 0; t < 4; ++t)
                *(__attribute__((address_space(3))) f32x4*)(L + 65536 + th * 8192 + (i * 4 + t) * 1024 + lane * 16) = c1[i][t];
    }
#pragma unroll
    for (int i = 0; i < 2; ++i)
#pragma unroll
        for (int t = 0; t < 4; ++t)
            *(__attribute__((address_space(3))) f32x4*)(L + w * 8192 + (i * 4 + t) * 1024 + lane * 16) = c2[i][t];
    LBAR();
#pragma unroll
    for (int i = 0; i < 2; ++i) {
        f32x4 fs = f32x4{0.f, 0.f, 0.f, 0.f};
#pragma unroll
        for (int k = 0; k < 4; ++k) fs += lds_f4(L + (2 * k + th) * 8192 + (i * 4 + kg) * 1024 + lane * 16);
        const f32x4 sv = lds_f4(L + 65536 + th * 8192 + (i * 4 + kg) * 1024 + lane * 16);
        const u32x2 xr = i ? xr1 : xr0;
        float o[4];
#pragma unroll
        for (int r = 0; r < 4; ++r) {
            const float fb = rbf(fs[r]);
            const float gb = rbf(fb * __builtin_amdgcn_rcpf(1.0f + __builtin_amdgcn_exp2f(-fb * 1.4426950408889634f)));
            const float pb = rbf(rbf(sv[r]) * gb);
            const unsigned xw = xr[r >> 1];
            const float xv = __uint_as_float((r & 1) ? (xw & 0xFFFF0000u) : (xw << 16));
            o[r] = xv + pb;
        }
        *(u32x2*)(out + oo + (size_t)i * 16 * 896) = u32x2{pk2(o[0], o[1]), pk2(o[2], o[3])};
    }
    STAMP(15)
    STAMP_FLUSH(ws, 0, 16)
}

extern "C" void kernel_launch(void* const* d_in, const int* in_sizes, int n_in, void* d_out, int out_size, void* d_ws,
                              size_t ws_size, hipStream_t stream) {
    (void)hipFuncSetAttribute((const void*)k_fused, hipFuncAttributeMaxDynamicSharedMemorySize, LDS_BYTES);
    k_fused<<<256, 512, LDS_BYTES, stream>>>((const char*)d_in[0], (const char*)d_in[1], (const char*)d_in[2], (const char*)d_in[4],
                                             (const char*)d_in[5], (const char*)d_in[6], (char*)d_out, (char*)d_ws);
}
```

```cpp
#include <hip/hip_runtime.h>
#include <hip/hip_bf16.h>

typedef short bf16x8 __attribute__((ext_vector_type(8)));
typedef short s16x4 __attribute__((ext_vector_type(4)));
typedef float f32x4 __attribute__((ext_vector_type(4)));
typedef float f32x2 __attribute__((ext_vector_type(2)));
typedef unsigned u32x4 __attribute__((ext_vector_type(4)));
typedef unsigned u32x2 __attribute__((ext_vector_type(2)));
typedef __bf16 bf16x2_t __attribute__((ext_vector_type(2)));
typedef __attribute__((address_space(3))) char* lds_ptr;

#define MFMA16(a, b, c) __builtin_amdgcn_mfma_f32_16x16x32_bf16(a, b, c, 0, 0, 0)
#define WAIT_BAR0() asm volatile("s_waitcnt vmcnt(0) lgkmcnt(0)\n\ts_barrier" ::: "memory")
#define LBAR() asm volatile("s_waitcnt lgkmcnt(0)\n\ts_barrier" ::: "memory")

#define STAMP_ARG
#define STAMP_PASS
#define STAMP_DECL
#define STAMP(i)
#define STAMP_FLUSH(actp, lo, hi)
constexpr int S_ = 2048, D_ = 448, F_ = 1792, QKV_ = 1344;
constexpr int ACT_OCT = 280;
constexpr int ACT_TILE_B = ACT_OCT * 256;

__device__ __forceinline__ void glds16(const void* g, unsigned lds_dst) {
    unsigned sv;
    asm volatile("s_mov_b32 %0, m0\n\ts_mov_b32 m0, %2\n\ts_nop 0\n\tglobal_load_lds_dwordx4 %1, off\n\ts_mov_b32 m0, %0"
                 : "=&s"(sv) : "v"(g), "s"(lds_dst) : "memory");
}
__device__ __forceinline__ void glds16s(const void* sbase, unsigned voff, unsigned lds_dst) {
    unsigned sv;
    asm volatile("s_mov_b32 %0, m0\n\ts_mov_b32 m0, %3\n\ts_nop 0\n\tglobal_load_lds_dwordx4 %1, %2\n\ts_mov_b32 m0, %0"
                 : "=&s"(sv) : "v"(voff), "s"(sbase), "s"(lds_dst) : "memory");
}
__device__ __forceinline__ bf16x8 lds_b128(lds_ptr p) { return *(const __attribute__((address_space(3))) bf16x8*)p; }
__device__ __forceinline__ f32x4 lds_f4(lds_ptr p) { return *(const __attribute__((address_space(3))) f32x4*)p; }
__device__ __forceinline__ s16x4 lds_tr(lds_ptr p) {
    return __builtin_amdgcn_ds_read_tr16_b64_v4i16((__attribute__((address_space(3))) s16x4*)p);
}
__device__ __forceinline__ bf16x8 lds_tr2(lds_ptr p, int second) {
    s16x4 a = lds_tr(p), b = lds_tr(p + second);
    return __builtin_shufflevector(a, b, 0, 1, 2, 3, 4, 5, 6, 7);
}
__device__ __forceinline__ float dot2(unsigned a, unsigned b, float c) {
    return __builtin_amdgcn_fdot2_f32_bf16(__builtin_bit_cast(bf16x2_t, a), __builtin_bit_cast(bf16x2_t, b), c, false);
}
__device__ __forceinline__ float frag_sum(bf16x8 f, float c) {
    u32x4 u = __builtin_bit_cast(u32x4, f);
    c = dot2(u[0], 0x3F803F80u, c); c = dot2(u[1], 0x3F803F80u, c);
    c = dot2(u[2], 0x3F803F80u, c); c = dot2(u[3], 0x3F803F80u, c);
    return c;
}
__device__ __forceinline__ float frag_sq(bf16x8 f, float c) {
    u32x4 u = __builtin_bit_cast(u32x4, f);
    c = dot2(u[0], u[0], c); c = dot2(u[1], u[1], c); c = dot2(u[2], u[2], c); c = dot2(u[3], u[3], c);
    return c;
}
__device__ __forceinline__ float xsum(float x) {
    auto s = __builtin_amdgcn_permlane16_swap(__float_as_uint(x), __float_as_uint(x), false, false);
    x = __uint_as_float(s[0]) + __uint_as_float(s[1]);
    auto t = __builtin_amdgcn_permlane32_swap(__float_as_uint(x), __float_as_uint(x), false, false);
    return __uint_as_float(t[0]) + __uint_as_float(t[1]);
}
__device__ __forceinline__ float xmax(float x) {
    auto s = __builtin_amdgcn_permlane16_swap(__float_as_uint(x), __float_as_uint(x), false, false);
    x = fmaxf(__uint_as_float(s[0]), __uint_as_float(s[1]));
    auto t = __builtin_amdgcn_permlane32_swap(__float_as_uint(x), __float_as_uint(x), false, false);
    return fmaxf(__uint_as_float(t[0]), __uint_as_float(t[1]));
}
__device__ __forceinline__ unsigned pk2(float lo, float hi) {
    bf16x2_t v; v[0] = (__bf16)lo; v[1] = (__bf16)hi;
    return __builtin_bit_cast(unsigned, v);
}
__device__ __forceinline__ unsigned pk2(f32x2 v) { return pk2(v[0], v[1]); }
__device__ __forceinline__ f32x2 lo2(f32x4 v) { return __builtin_shufflevector(v, v, 0, 1); }
__device__ __forceinline__ f32x2 hi2(f32x4 v) { return __builtin_shufflevector(v, v, 2, 3); }
__device__ __forceinline__ f32x2 sp2(float v) { return f32x2{v, v}; }
__device__ __forceinline__ f32x2 fma2(f32x2 a, f32x2 b, f32x2 c) { return __builtin_elementwise_fma(a, b, c); }
__device__ __forceinline__ float rbf(float x) { return (float)(__bf16)x; }
__device__ __forceinline__ void ln_stats(float sum, float sq, float& mean, float& den) {
    float m = sum * (1.0f / D_);
    float v = fmaxf(sq * (1.0f / D_) - m * m, 0.0f);
    mean = rbf(m);
    den = rbf(sqrtf(rbf(rbf(v) + 1e-5f)));
}

typedef __attribute__((address_space(1))) unsigned long long gu64;
#define RLX_AGENT __ATOMIC_RELAXED, __HIP_MEMORY_SCOPE_AGENT
#define SB() __builtin_amdgcn_sched_barrier(0)
#define LDSF(p) (*(__attribute__((address_space(3))) float*)(p))
#define LDSV(T, p) (*(__attribute__((address_space(3))) T*)(p))
constexpr size_t SYNC_OFF = 16u << 20, FLAG_OFF = SYNC_OFF + 4096, STAT_OFF = SYNC_OFF + 65536;
constexpr int ACT_BYTES = 128 * ACT_TILE_B;
constexpr unsigned long long SPIN_TICKS = 20000000ull;

template <int N> __device__ __forceinline__ float ror_add(float x) {
    return x + __uint_as_float(__builtin_amdgcn_update_dpp(0u, __float_as_uint(x), 0x120 + N, 0xF, 0xF, false));
}
__device__ __forceinline__ float wave_sum(float x) { return xsum(ror_add<1>(ror_add<2>(ror_add<4>(ror_add<8>(x))))); }

__device__ __forceinline__ void start_loads(const char* x, char* ws, int row, int lane, unsigned long long& ep, u32x4& rv) {
    const unsigned zero = 0, ro = (lane < 56 ? lane : 55) * 16;
    asm volatile("global_load_dwordx2 %0, %1, %2 sc1" : "=v"(ep) : "v"(zero), "s"(ws + SYNC_OFF) : "memory");
    asm volatile("global_load_dwordx4 %0, %1, %2" : "=v"(rv) : "v"(ro), "s"(x + (size_t)row * 896) : "memory");
}
__device__ __forceinline__ unsigned long long publish_stats(char* ws, int row, int lane, unsigned long long ep, u32x4 rv) {
    const unsigned long long tag = (((unsigned long long)__builtin_amdgcn_readfirstlane((unsigned)(ep >> 32)) << 32) |
                                    (unsigned)__builtin_amdgcn_readfirstlane((unsigned)ep)) + 1ull;
    if (lane >= 56) rv = u32x4{0u, 0u, 0u, 0u};
    const bf16x8 f = __builtin_bit_cast(bf16x8, rv);
    float mean, den;
    ln_stats(wave_sum(frag_sum(f, 0.f)), wave_sum(frag_sq(f, 0.f)), mean, den);
    const unsigned val = (__float_as_uint(mean) & 0xFFFF0000u) | (__float_as_uint(den) >> 16);
    if (lane == 0) __hip_atomic_store((gu64*)(ws + STAT_OFF) + row, ((unsigned long long)(unsigned)tag << 32) | val, RLX_AGENT);
    return tag;
}
__device__ __forceinline__ unsigned fetch_stats(char* ws, int row, unsigned long long tag) {
    const unsigned long long t0 = __builtin_amdgcn_s_memrealtime();
    unsigned long long v;
    for (;;) {
        v = __hip_atomic_load((gu64*)(ws + STAT_OFF) + row, RLX_AGENT);
        if (__all((unsigned)(v >> 32) == (unsigned)tag)) break;
        if (__builtin_amdgcn_s_memrealtime() - t0 > SPIN_TICKS) break;
        __builtin_amdgcn_s_sleep(2);
    }
    return (unsigned)v;
}
__device__ __forceinline__ void wait_flags(char* ws, int first, int n, int lane, unsigned long long tag) {
    const unsigned long long t0 = __builtin_amdgcn_s_memrealtime();
    const int i = first + (lane < n ? lane : n - 1);
    for (;;) {
        const unsigned long long v = __hip_atomic_load((gu64*)(ws + FLAG_OFF + (size_t)i * 64), RLX_AGENT);
        if (__all(v == tag)) break;
        if (__builtin_amdgcn_s_memrealtime() - t0 > SPIN_TICKS) break;
        __builtin_amdgcn_s_sleep(1);
    }
}

struct P2 {
    const char* w1; const char* w2; unsigned wv0, ring; int n2;
};
__device__ __forceinline__ void p2_dma(const P2& P, const char* src, int slot, int half) {
    glds16s(src + half * (8 * 896), half ? (P.wv0 ^ 64) : P.wv0, P.ring + slot * 4096 + half * 1024);
}

constexpr int FF_XS = 32768, FF_UOFF = 15376, FF_WS = 2 * FF_UOFF, FF_W0 = 2 * FF_XS, FF_CS = 163840 - 1024;

__device__ __forceinline__ unsigned long long ffn_block(const char* x, const char* wg, const char* wu, char* ws, int rb, int cb,
                                                        lds_ptr L, const P2& P, bool has_p2 STAMP_ARG) {
    const int tid = threadIdx.x, lane = tid & 63;
    const int w = __builtin_amdgcn_readfirstlane(tid >> 6);
    const int wm = w & 3, wn = w >> 2;
    const unsigned lb = (unsigned)(size_t)L;
    unsigned long long ep; u32x4 rv;
    const int srow = rb * 256 + (16 + cb) * 8 + w;
    start_loads(x, ws, srow, lane, ep, rv);
    const unsigned xo0 = (lane >> 3) * 896 + (((lane & 7) ^ (lane >> 4)) << 4), xo1 = xo0 ^ 64;
    const char* xb = x + (size_t)(rb * 256 + 32 * w) * 896;
    auto dma_x = [&](int c, int j) { glds16s(xb + j * (8 * 896) + c * 128, (j & 1) ? xo1 : xo0, lb + (c & 1) * FF_XS + (32 * w + 8 * j) * 128); };
#pragma unroll
    for (int j = 0; j < 4; ++j) dma_x(0, j);
    unsigned woff[4];
    const char* wsrc[4];
    unsigned wdst[4];
#pragma unroll
    for (int j = 0; j < 4; ++j) {
        int n = w + 8 * j; if (n > 29) n = 29;
        const int mat = n >= 15, nn = n - 15 * mat;
        const int ci = nn * 64 + lane, grp = ci / 120;
        int rem = ci - grp * 120; if (rem >= 112) rem -= 8;
        const int r = rem / 14, chn = rem - r * 14;
        woff[j] = (8 * grp + r) * (F_ * 2) + chn * 16;
        wsrc[j] = (mat ? wu : wg) + cb * 224;
        wdst[j] = lb + FF_W0 + mat * FF_UOFF + nn * 1024;
    }
    auto dma_w = [&](int c, int buf, int j) { if (j < 3 || w < 6) glds16s(wsrc[j] + (size_t)c * (64 * F_ * 2), woff[j], wdst[j] + buf); };
#pragma unroll
    for (int j = 0; j < 4; ++j) dma_w(0, 0, j);
#pragma unroll
    for (int j = 0; j < 4; ++j) dma_w(1, FF_WS, j);
    dma_x(1, 0); dma_x(1, 1);
    asm volatile("s_waitcnt vmcnt(12)" : "+v"(ep), "+v"(rv) :: "memory");
    const unsigned long long tag = publish_stats(ws, srow, lane, ep, rv);
    const int g = lane >> 4, q = (lane >> 2) & 3, p = lane & 3, tk = lane & 15;
    const int xa = (64 * wm + tk) * 128 + ((g ^ ((lane >> 1) & 7)) << 4);
    const int wa = FF_W0 + g * 1920 + q * 224 + (p & 1) * 8 + (p >> 1) * FF_UOFF + wn * 112;
    const int tc1 = (wm + 4 > 6) ? 6 : wm + 4;
    f32x4 acc[4][7];
#pragma unroll
    for (int i = 0; i < 4; ++i)
#pragma unroll
        for (int t = 0; t < 7; ++t) acc[i][t] = f32x4{0.f, 0.f, 0.f, 0.f};
    f32x4 cs0 = f32x4{0.f, 0.f, 0.f, 0.f}, cs1 = cs0;
    const bf16x8 ones = __builtin_bit_cast(bf16x8, u32x4{0x3F803F80u, 0x3F803F80u, 0x3F803F80u, 0x3F803F80u});
    bf16x8 xf[2][4], wf[4], cf;
    if (w < 6) asm volatile("s_waitcnt vmcnt(7)\n\ts_barrier" ::: "memory");
    else asm volatile("s_waitcnt vmcnt(6)\n\ts_barrier" ::: "memory");
    STAMP(1)
#define XRD(B, ks, i) xf[ks][i] = lds_b128((B) + (xa ^ ((ks) * 64)) + (i) * 2048)
#define WRD(B, s) lds_tr2((B) + ((s) / 7) * 7680 + ((s) % 7) * 16, 896)
    {
        lds_ptr W = L + wa;
        XRD(L, 0, 0); XRD(L, 0, 1); XRD(L, 0, 2); XRD(L, 0, 3);
        wf[0] = WRD(W, 0); wf[1] = WRD(W, 1); wf[2] = WRD(W, 2);
    }
    int wb = 0;
#pragma unroll 1
    for (int c = 0; c < 7; ++c) {
        const int wb1 = (wb == 2 * FF_WS) ? 0 : wb + FF_WS, wb2 = (wb == 0) ? 2 * FF_WS : wb - FF_WS;
        lds_ptr X = L + (c & 1) * FF_XS, Xn = L + ((c + 1) & 1) * FF_XS, W = L + wa + wb, Wn = L + wa + wb1;
#pragma unroll
        for (int s = 0; s < 14; ++s) {
            const int ks = s / 7, t = s % 7;
            if (s == 11) {
                if (c < 5) {
                    if (w < 6) asm volatile("s_waitcnt vmcnt(4) lgkmcnt(0)\n\ts_barrier" ::: "memory");
                    else asm volatile("s_waitcnt vmcnt(3) lgkmcnt(0)\n\ts_barrier" ::: "memory");
                } else WAIT_BAR0();
            }
            if (s < 11) wf[(s + 3) % 4] = WRD(W, s + 3); else wf[(s + 3) % 4] = WRD(Wn, s - 11);
            if (s < 4) XRD(X, 1, s);
            if (s == 11) { XRD(Xn, 0, 0); XRD(Xn, 0, 1); }
            if (s == 12) { XRD(Xn, 0, 2); XRD(Xn, 0, 3); }
            if (s == 4 || s == 6 || s == 8 || s == 10) cf = lds_tr2(W + (s >= 8) * 7680 + ((s == 4 || s == 8) ? wm : tc1) * 16, 896);
            if (s < 2) { if (c < 6) dma_x(c + 1, 2 + s); }
            if (s >= 2 && s < 6) { if (c < 5) dma_w(c + 2, wb2, s - 2); }
            if (s >= 12) { if (c < 5) dma_x(c + 2, s - 12); }
#pragma unroll
            for (int i = 0; i < 4; ++i) acc[i][t] = MFMA16(wf[s % 4], xf[ks][i], acc[i][t]);
            if (s == 5 || s == 9) cs0 = MFMA16(cf, ones, cs0);
            if (s == 7 || s == 12) cs1 = MFMA16(cf, ones, cs1);
            SB();
        }
        { bf16x8 a = wf[2], b = wf[3], d = wf[0]; wf[0] = a; wf[1] = b; wf[2] = d; }
        wb = wb1;
    }
    STAMP(4)
    if (tk == 0) {
        LDSV(f32x4, L + FF_CS + (7 * wn + wm) * 64 + g * 16) = cs0;
        if (wm < 3) LDSV(f32x4, L + FF_CS + (7 * wn + wm + 4) * 64 + g * 16) = cs1;
    }
    const unsigned sv = fetch_stats(ws, rb * 256 + 64 * wm + lane, tag);
    LBAR();
    STAMP(5)
    const __amdgpu_buffer_rsrc_t ra = __builtin_amdgcn_make_buffer_rsrc(ws, (short)0, ACT_BYTES, 0x00020000);
    f32x4 csn = lds_f4(L + FF_CS + (7 * wn) * 64 + g * 16);
    const float K1 = -2.3022081986f, K2 = -0.1029432398f;
#pragma unroll
    for (int ip = 0; ip < 2; ++ip) {
        const unsigned sa = __shfl(sv, 32 * ip + tk), sb = __shfl(sv, 32 * ip + 16 + tk);
        const float ma = -__uint_as_float(sa & 0xFFFF0000u), mb = -__uint_as_float(sb & 0xFFFF0000u);
        const float dn = __uint_as_float((lane < 32 ? sa : sb) << 16);
        const float a = __builtin_amdgcn_rcpf(dn), ia2 = dn * dn, c1 = K1 * a, c2 = K2 * a * a * a;
        const unsigned dst = (rb * 16 + wm * 4 + 2 * ip + (lane >> 5)) * ACT_TILE_B + (cb * 14 + wn * 7) * 256 + tk * 16;
        u32x2 hp[7];
#pragma unroll
        for (int t = 0; t < 7; ++t) {
            const f32x4 cs = csn;
            csn = lds_f4(L + FF_CS + (7 * wn + (t + 1) % 7) * 64 + g * 16);
#pragma unroll
            for (int u = 0; u < 2; ++u) {
                const f32x2 c2v = u ? hi2(cs) : lo2(cs);
                const f32x2 va = fma2(sp2(ma), c2v, u ? hi2(acc[2 * ip][t]) : lo2(acc[2 * ip][t]));
                const f32x2 vb = fma2(sp2(mb), c2v, u ? hi2(acc[2 * ip + 1][t]) : lo2(acc[2 * ip + 1][t]));
                auto s0 = __builtin_amdgcn_permlane32_swap(__float_as_uint(va[0]), __float_as_uint(vb[0]), false, false);
                auto s1 = __builtin_amdgcn_permlane32_swap(__float_as_uint(va[1]), __float_as_uint(vb[1]), false, false);
                const f32x2 G = f32x2{__uint_as_float(s0[0]), __uint_as_float(s1[0])}, U = f32x2{__uint_as_float(s0[1]), __uint_as_float(s1[1])};
                const f32x2 z = G * fma2(G * G, sp2(c2), sp2(c1));
                const f32x2 e = f32x2{__builtin_amdgcn_exp2f(z[0]), __builtin_amdgcn_exp2f(z[1])};
                const f32x2 d = fma2(e, sp2(ia2), sp2(ia2));
                hp[t][u] = pk2((G * U) * f32x2{__builtin_amdgcn_rcpf(d[0]), __builtin_amdgcn_rcpf(d[1])});
            }
            if (t & 1) {
                auto s0 = __builtin_amdgcn_permlane16_swap(hp[t - 1][0], hp[t][0], false, false);
                auto s1 = __builtin_amdgcn_permlane16_swap(hp[t - 1][1], hp[t][1], false, false);
                __builtin_amdgcn_raw_buffer_store_b128(u32x4{s0[0], s1[0], s0[1], s1[1]}, ra, dst + (g & 1) * 256, (t - 1) * 256, 16);
            }
            if (t == 6) __builtin_amdgcn_raw_buffer_store_b64(hp[6], ra, dst + (g & 1) * 8, 6 * 256, 16);
            if (has_p2) {
                const int k = ip * 7 + t;
                if (k < 9) { p2_dma(P, P.w1 + k * (32 * 896), k, 0); p2_dma(P, P.w1 + k * (32 * 896), k, 1); }
            }
            SB();
        }
    }
    STAMP(6)
    WAIT_BAR0();
    if (tid == 0) __hip_atomic_store((gu64*)(ws + FLAG_OFF + (size_t)(rb * 32 + 16 + cb) * 64), tag, RLX_AGENT);
    if (has_p2) { p2_dma(P, P.w1 + 9 * (32 * 896), 9, 0); p2_dma(P, P.w1 + 9 * (32 * 896), 9, 1); }
    STAMP(7)
    return tag;
}

constexpr int AT_WM = 28672, AT_X = 3 * AT_WM, AT_CS = AT_X + 65536, AT_ST = AT_CS + 512;
constexpr int AT_K = 0, AT_V = 32768;

__device__ __forceinline__ unsigned long long att_block(const char* x, const char* wqkv, char* ws, int ch, int head, lds_ptr L,
                                                        const P2& P STAMP_ARG) {
    const int tid = threadIdx.x, lane = tid & 63;
    const int w = __builtin_amdgcn_readfirstlane(tid >> 6);
    const unsigned lb = (unsigned)(size_t)L;
    const int g = lane >> 4, q = (lane >> 2) & 3, p = lane & 3, tk = lane & 15;
    unsigned long long ep; u32x4 rv;
    const int srow = ch * 256 + head * 8 + w;
    start_loads(x, ws, srow, lane, ep, rv);
    const unsigned wo = (lane >> 2) * (QKV_ * 2) + (((lane & 3) ^ (((lane >> 5) & 1) << 1)) << 4);
    auto dma_w = [&](int j) {
        const int n = w + 8 * j, rblk = (n * 43) >> 7, mat = n - rblk * 3;
        glds16s(wqkv + rblk * (16 * QKV_ * 2) + mat * 896 + head * 56 - (mat == 2 ? 8 : 0), wo, lb + mat * AT_WM + rblk * 1024);
    };
    const int hrow = (ch ? ch - 1 : 0) * 256 + 32 * w, orow = ch * 256 + 32 * w;
    const unsigned xo0 = (lane >> 3) * 896 + (((lane & 7) ^ (lane >> 4)) << 4), xo1 = xo0 ^ 64;
    const char* xh = x + (size_t)hrow * 896;
    const char* xs = x + (size_t)orow * 896;
    const unsigned xst = lb + AT_X + w * 8192;
    auto dma_x = [&](int c, int i) {
        const char* src = (i < 2 ? xh : xs) + (i & 1) * (16 * 896) + c * 128;
        glds16s(src, xo0, xst + i * 2048);
        glds16s(src + 8 * 896, xo1, xst + i * 2048 + 1024);
    };
#pragma unroll
    for (int j = 0; j < 5; ++j) dma_w(j);
#pragma unroll
    for (int i = 0; i < 4; ++i) dma_x(0, i);
#pragma unroll
    for (int j = 5; j < 10; ++j) dma_w(j);
    if (w < 4) dma_w(10);
    asm volatile("s_waitcnt vmcnt(18)" : "+v"(ep), "+v"(rv) :: "memory");
    const unsigned long long tag = publish_stats(ws, srow, lane, ep, rv);
    const int xa0 = AT_X + w * 8192 + tk * 128 + ((g ^ ((lane >> 1) & 7)) << 4), xa1 = xa0 ^ 64;
    const int wA0 = (8 * g + q) * 64 + ((g & 1) << 5) + p * 8, wA1 = wA0 ^ 32;
    const int cidx = w > 5 ? 5 : w;
    const int wC = (cidx >> 1) * AT_WM + (wA0 ^ ((cidx & 1) << 5));
    f32x4 aK[4][2], aV[4][2], aQ[2][2], cs = f32x4{0.f, 0.f, 0.f, 0.f};
#pragma unroll
    for (int i = 0; i < 4; ++i) { aK[i][0] = aK[i][1] = aV[i][0] = aV[i][1] = f32x4{0.f, 0.f, 0.f, 0.f}; }
    aQ[0][0] = aQ[0][1] = aQ[1][0] = aQ[1][1] = f32x4{0.f, 0.f, 0.f, 0.f};
    const bf16x8 ones = __builtin_bit_cast(bf16x8, u32x4{0x3F803F80u, 0x3F803F80u, 0x3F803F80u, 0x3F803F80u});
    bf16x8 wf[2][3][2], cf[2], xf[2][2];
#define AWRD(B, ks) { _Pragma("unroll") for (int m = 0; m < 3; ++m) { wf[ks][m][0] = lds_tr2((B) + wA0 + m * AT_WM + (ks) * 2048, 256); \
                      wf[ks][m][1] = lds_tr2((B) + wA1 + m * AT_WM + (ks) * 2048, 256); } cf[ks] = lds_tr2((B) + wC + (ks) * 2048, 256); }
#define AXRD(i) { xf[(i) & 1][0] = lds_b128(L + xa0 + (i) * 2048); xf[(i) & 1][1] = lds_b128(L + xa1 + (i) * 2048); }
#define AMM(i, ks) { _Pragma("unroll") for (int t = 0; t < 2; ++t) { \
                       aK[i][t] = MFMA16(wf[ks][1][t], xf[(i) & 1][ks], aK[i][t]); aV[i][t] = MFMA16(xf[(i) & 1][ks], wf[ks][2][t], aV[i][t]); \
                       if (i >= 2) aQ[(i) & 1][t] = MFMA16(wf[ks][0][t], xf[(i) & 1][ks], aQ[(i) & 1][t]); } }
    asm volatile("s_waitcnt vmcnt(12)\n\ts_barrier" ::: "memory");
    STAMP(1)
    AXRD(0) AWRD(L, 0) AWRD(L, 1)
    SB();
#pragma unroll 1
    for (int c = 0; c < 6; ++c) {
        lds_ptr Wn = L + (c + 1) * 4096;
#pragma unroll
        for (int i = 0; i < 4; ++i) {
            asm volatile("s_waitcnt vmcnt(4)" ::: "memory");
            if (i == 3 && c == 2) asm volatile("s_barrier" ::: "memory");
            AXRD((i + 1) & 3)
            AMM(i, 0)
            if (i == 0) cs = MFMA16(cf[0], ones, cs);
            SB();
            if (i == 3) AWRD(Wn, 0)
            AMM(i, 1)
            if (i == 1) cs = MFMA16(cf[1], ones, cs);
            SB();
            if (i == 3) AWRD(Wn, 1)
            dma_x(c + 1, i);
            SB();
        }
    }
    STAMP(3)
#pragma unroll
    for (int i = 0; i < 4; ++i) {
        if (i == 0) asm volatile("s_waitcnt vmcnt(4)" ::: "memory");
        if (i == 1) asm volatile("s_waitcnt vmcnt(2)" ::: "memory");
        if (i == 2) asm volatile("s_waitcnt vmcnt(0)" ::: "memory");
        if (i < 3) AXRD(i + 1)
        AMM(i, 0)
        if (i == 0) cs = MFMA16(cf[0], ones, cs);
        AMM(i, 1)
        if (i == 1) cs = MFMA16(cf[1], ones, cs);
        SB();
    }
    STAMP(4)
    if (tk == 0 && w < 6) LDSV(f32x4, L + AT_CS + w * 64 + g * 16) = cs;
    {
        const unsigned sv = fetch_stats(ws, (lane < 32 ? hrow : orow - 32) + lane, tag);
        LDSF(L + AT_ST + w * 512 + lane * 4) = -__uint_as_float(sv & 0xFFFF0000u);
        LDSF(L + AT_ST + w * 512 + 256 + lane * 4) = __builtin_amdgcn_rcpf(__uint_as_float(sv << 16));
    }
    LBAR();
    bf16x8 qf[2];
    {
        f32x4 cq[2], ck[2];
        float cv[2];
#pragma unroll
        for (int t = 0; t < 2; ++t) {
            cq[t] = lds_f4(L + AT_CS + t * 64 + g * 16);
            ck[t] = lds_f4(L + AT_CS + 128 + t * 64 + g * 16);
            cv[t] = LDSF(L + AT_CS + 256 + t * 64 + tk * 4);
        }
        const bool pad = (lane >= 48);
#pragma unroll
        for (int i = 0; i < 4; ++i) {
            const float nm = LDSF(L + AT_ST + w * 512 + i * 64 + tk * 4), rs = LDSF(L + AT_ST + w * 512 + 256 + i * 64 + tk * 4);
            const float rs1 = pad ? 0.f : rs;
            const int kt = (i >> 1) * 16 + 2 * w + (i & 1);
#define CORR(A, C) u32x4{pk2(fma2(sp2(nm), lo2(C[0]), lo2(A[0])) * sp2(rs)), pk2(fma2(sp2(nm), hi2(C[0]), hi2(A[0])) * sp2(rs)), \
                         pk2(fma2(sp2(nm), lo2(C[1]), lo2(A[1])) * sp2(rs1)), pk2(fma2(sp2(nm), hi2(C[1]), hi2(A[1])) * sp2(rs1))}
            LDSV(u32x4, L + AT_K + kt * 1024 + lane * 16) = CORR(aK[i], ck);
            if (i >= 2) qf[i - 2] = __builtin_bit_cast(bf16x8, CORR(aQ[i - 2], cq));
        }
#pragma unroll
        for (int b = 0; b < 2; ++b) {
            f32x4 s0[2], s1[2];
#pragma unroll
            for (int u = 0; u < 2; ++u) {
                s0[u] = lds_f4(L + AT_ST + w * 512 + (2 * b + u) * 64 + g * 16);
                s1[u] = lds_f4(L + AT_ST + w * 512 + 256 + (2 * b + u) * 64 + g * 16);
            }
#pragma unroll
            for (int t = 0; t < 2; ++t) {
                u32x4 o;
#pragma unroll
                for (int u = 0; u < 2; ++u) {
                    o[2 * u] = pk2(fma2(lo2(s0[u]), sp2(cv[t]), lo2(aV[2 * b + u][t])) * lo2(s1[u]));
                    o[2 * u + 1] = pk2(fma2(hi2(s0[u]), sp2(cv[t]), hi2(aV[2 * b + u][t])) * hi2(s1[u]));
                }
                if (t == 0 && tk == 0) o = u32x4{0x3F803F80u, 0x3F803F80u, 0x3F803F80u, 0x3F803F80u};
                LDSV(u32x4, L + AT_V + ((b * 8 + w) * 2 + t) * 1024 + lane * 16) = o;
            }
        }
    }
    LBAR();
    STAMP(5)
    const float NEG = -1e30f;
    f32x4 mle, mgt;
#pragma unroll
    for (int r = 0; r < 4; ++r) { mle[r] = (4 * g + r <= tk) ? 0.f : NEG; mgt[r] = (4 * g + r > tk) ? 0.f : NEG; }
    const int jmin = ch ? 0 : 16 - 2 * w;
    f32x4 Sc[2][18];
#pragma unroll
    for (int j = 0; j < 18; ++j) {
        const bf16x8 kf = lds_b128(L + AT_K + (2 * w + j) * 1024 + lane * 16);
        const float bias = (j < jmin) ? NEG : 0.f;
#pragma unroll
        for (int qt = 0; qt < 2; ++qt) {
            if (j >= qt && j <= qt + 16) {
                f32x4 c0;
                if (j == qt + 16) c0 = mle;
                else if (j == qt) c0 = mgt + bias;
                else c0 = f32x4{bias, bias, bias, bias};
                Sc[qt][j] = MFMA16(kf, qf[qt], c0);
            } else {
                Sc[qt][j] = f32x4{0.f, 0.f, 0.f, 0.f};
            }
        }
    }
    const float CS = 0.18898223650461362f * 1.4426950408889634f;
    u32x4 pf[2][9];
#pragma unroll
    for (int qt = 0; qt < 2; ++qt) {
        float m = NEG;
#pragma unroll
        for (int j = qt; j <= qt + 16; ++j) m = fmaxf(fmaxf(m, fmaxf(Sc[qt][j][0], Sc[qt][j][1])), fmaxf(Sc[qt][j][2], Sc[qt][j][3]));
        m = xmax(m);
        const float mc = -m * CS;
#pragma unroll
        for (int j = qt; j <= qt + 16; ++j) {
            const f32x2 a = fma2(lo2(Sc[qt][j]), sp2(CS), sp2(mc)), b = fma2(hi2(Sc[qt][j]), sp2(CS), sp2(mc));
            Sc[qt][j] = f32x4{__builtin_amdgcn_exp2f(a[0]), __builtin_amdgcn_exp2f(a[1]), __builtin_amdgcn_exp2f(b[0]), __builtin_amdgcn_exp2f(b[1])};
        }
#pragma unroll
        for (int b = 0; b < 9; ++b)
            pf[qt][b] = u32x4{pk2(Sc[qt][2 * b][0], Sc[qt][2 * b][1]), pk2(Sc[qt][2 * b][2], Sc[qt][2 * b][3]),
                              pk2(Sc[qt][2 * b + 1][0], Sc[qt][2 * b + 1][1]), pk2(Sc[qt][2 * b + 1][2], Sc[qt][2 * b + 1][3])};
    }
    f32x4 O[2][2];
    O[0][0] = O[0][1] = O[1][0] = O[1][1] = f32x4{0.f, 0.f, 0.f, 0.f};
#pragma unroll
    for (int b = 0; b < 9; ++b)
#pragma unroll
        for (int t = 0; t < 2; ++t) {
            const bf16x8 vf = lds_b128(L + AT_V + ((w + b) * 2 + t) * 1024 + lane * 16);
#pragma unroll
            for (int qt = 0; qt < 2; ++qt) O[qt][t] = MFMA16(vf, __builtin_bit_cast(bf16x8, pf[qt][b]), O[qt][t]);
        }
    const __amdgpu_buffer_rsrc_t ra = __builtin_amdgcn_make_buffer_rsrc(ws, (short)0, ACT_BYTES, 0x00020000);
#pragma unroll
    for (int qt = 0; qt < 2; ++qt) {
        const float l = __shfl(O[qt][0][0], tk);
        const float inv = __builtin_amdgcn_rcpf(l);
        const unsigned dst = (ch * 16 + 2 * w + qt) * ACT_TILE_B + 224 * 256 + tk * 16;
#pragma unroll
        for (int t = 0; t < 2; ++t) {
            const int u = 7 * head + 4 * t + g - 1;
            if (t == 1 || g > 0)
                __builtin_amdgcn_raw_buffer_store_b64(
                    u32x2{pk2(O[qt][t][0] * inv, O[qt][t][1] * inv), pk2(O[qt][t][2] * inv, O[qt][t][3] * inv)}, ra,
                    dst + (u >> 1) * 256 + (u & 1) * 8, 0, 16);
        }
    }
    STAMP(6)
    WAIT_BAR0();
    if (tid == 0) __hip_atomic_store((gu64*)(ws + FLAG_OFF + (size_t)(ch * 32 + head) * 64), tag, RLX_AGENT);
#pragma unroll
    for (int k = 0; k < 10; ++k) { p2_dma(P, P.w1 + k * (32 * 896), k, 0); p2_dma(P, P.w1 + k * (32 * 896), k, 1); }
    STAMP(7)
    return tag;
}

constexpr int LDS_BYTES = 163840;

__global__ __launch_bounds__(512, 2) void k_fused(const char* x, const char* wqkv, const char* wo, const char* wg, const char* wu,
                                                  const char* wd, char* out, char* ws) {
    extern __shared__ __attribute__((aligned(16))) char smem[];
    lds_ptr L = (lds_ptr)smem;
    const unsigned lb = (unsigned)(size_t)L;
    STAMP_DECL STAMP(0)
    const int tid = threadIdx.x, lane = tid & 63;
    const int w = __builtin_amdgcn_readfirstlane(tid >> 6);
    const int xcd = blockIdx.x & 7, idx = blockIdx.x >> 3;
    const int rblk = 4 * (xcd & 1) + (idx & 3), cblk = 4 * (xcd >> 1) + ((idx >> 2) & 3);
    const int th = w & 1, kg = w >> 1;
    const bool has_p2 = idx < 28;
    const int rb = 4 * xcd + (idx & 3), cb = has_p2 ? (idx >> 2) : 0;
    const int a1 = kg ? 14 * (kg - 1) : 56;
    const int a2 = 42 + (kg < 2 ? 3 * kg : 4 * kg - 2);
    P2 P;
    P.n2 = kg < 2 ? 3 : 4;
    P.w1 = (kg ? wd + (size_t)(14 * (kg - 1)) * 32 * 896 : wo) + cb * 128 + th * 16 * 896;
    P.w2 = wd + (size_t)a2 * 32 * 896 + cb * 128 + th * 16 * 896;
    P.wv0 = (lane >> 3) * 896 + (((lane & 7) ^ (((lane >> 4) & 1) << 1)) << 4);
    P.ring = lb + kg * 40960 + th * 2048;
    unsigned long long tag;
    if (idx < 16) tag = att_block(x, wqkv, ws, rblk, cblk, L, P STAMP_PASS);
    else tag = ffn_block(x, wg, wu, ws, rblk, cblk, L, P, has_p2 STAMP_PASS);
    if (!has_p2) {
        if (blockIdx.x == 255 && w == 0) {
#pragma unroll 1
            for (int i = 0; i < 4; ++i) wait_flags(ws, 64 * i, 64, lane, tag);
            if (lane == 0) __hip_atomic_store((gu64*)(ws + SYNC_OFF), tag, RLX_AGENT);
        }
        STAMP_FLUSH(ws, 0, 8)
        return;
    }
    const int g = lane >> 4, q = (lane >> 2) & 3, p = lane & 3, tk = lane & 15;
    const size_t oo = (size_t)(rb * 64 + 32 * th + tk) * 896 + cb * 128 + kg * 32 + g * 8;
    const u32x2 xr0 = *(const u32x2*)(x + oo), xr1 = *(const u32x2*)(x + oo + 16 * 896);
    STAMP(8)
    if (w == 0) wait_flags(ws, (rb >> 2) * 32, 32, lane, tag);
    asm volatile("s_waitcnt vmcnt(0)\n\ts_barrier" ::: "memory");
    STAMP(9)
    const __amdgpu_buffer_rsrc_t ra = __builtin_amdgcn_make_buffer_rsrc(ws, (short)0, ACT_BYTES, 0x00020000);
    const unsigned ab = (rb * 4 + 2 * th) * ACT_TILE_B + lane * 16;
    bf16x8 af[14][2], ag[4][2];
#define ALD(ks, i) __builtin_bit_cast(bf16x8, __builtin_amdgcn_raw_buffer_load_b128(ra, ab + (i) * ACT_TILE_B, (ks) * 1024, 16))
#pragma unroll
    for (int s = 0; s < 14; ++s) { af[s][0] = ALD(a1 + s, 0); af[s][1] = ALD(a1 + s, 1); SB(); }
    const int f = ((q >> 1) & 1) | ((g & 1) << 1);
    int wa[4];
#pragma unroll
    for (int t = 0; t < 4; ++t) wa[t] = kg * 40960 + (8 * g + q) * 128 + ((t ^ f) << 5) + p * 8;
    f32x4 c1[2][4], c2[2][4];
#pragma unroll
    for (int i = 0; i < 2; ++i)
#pragma unroll
        for (int t = 0; t < 4; ++t) c1[i][t] = c2[i][t] = f32x4{0.f, 0.f, 0.f, 0.f};
#define STEP(ACC, FR, SLOT)                                                          \
    {                                                                                \
        bf16x8 wf[4];                                                                \
        _Pragma("unroll") for (int t = 0; t < 4; ++t) wf[t] = lds_tr2(L + wa[t] + (SLOT) * 4096, 512); \
        _Pragma("unroll") for (int t = 0; t < 4; ++t) {                              \
            ACC[0][t] = MFMA16(wf[t], FR[0], ACC[0][t]);                             \
            ACC[1][t] = MFMA16(wf[t], FR[1], ACC[1][t]);                             \
        }                                                                            \
    }
    STAMP(10)
#pragma unroll
    for (int s = 0; s < 4; ++s) STEP(c1, af[s], s)
    LBAR();
#pragma unroll
    for (int s = 10; s < 14; ++s) { p2_dma(P, P.w1 + s * (32 * 896), s - 10, 0); p2_dma(P, P.w1 + s * (32 * 896), s - 10, 1); }
#pragma unroll
    for (int s = 0; s < 4; ++s) {
        const int sc = s < P.n2 ? s : P.n2 - 1;
        ag[s][0] = ALD(a2 + sc, 0); ag[s][1] = ALD(a2 + sc, 1);
    }
#pragma unroll
    for (int s = 4; s < 8; ++s) STEP(c1, af[s], s)
    LBAR();
    STAMP(11)
#pragma unroll
    for (int s = 0; s < 4; ++s) {
        const char* src = P.w2 + (s < P.n2 ? s : P.n2 - 1) * (32 * 896);
        p2_dma(P, src, 4 + s, 0); p2_dma(P, src, 4 + s, 1);
    }
#pragma unroll
    for (int s = 8; s < 10; ++s) STEP(c1, af[s], s)
    asm volatile("s_waitcnt vmcnt(16) lgkmcnt(0)\n\ts_barrier" ::: "memory");
#pragma unroll
    for (int s = 10; s < 14; ++s) STEP(c1, af[s], s - 10)
    STAMP(12)
    WAIT_BAR0();
    STAMP(13)
#pragma unroll
    for (int s = 0; s < 3; ++s) STEP(c2, ag[s], 4 + s)
    if (P.n2 == 4) STEP(c2, ag[3], 7)
    LBAR();
    STAMP(14)
    if (kg) {
#pragma unroll
        for (int i = 0; i < 2; ++i)
#pragma unroll
            for (int t = 0; t < 4; ++t) c2[i][t] += c1[i][t];
    } else {
#pragma unroll
        for (int i = 0; i < 2; ++i)
#pragma unroll
            for (int t = 0; t < 4; ++t)
                *(__attribute__((address_space(3))) f32x4*)(L + 65536 + th * 8192 + (i * 4 + t) * 1024 + lane * 16) = c1[i][t];
    }
#pragma unroll
    for (int i = 0; i < 2; ++i)
#pragma unroll
        for (int t = 0; t < 4; ++t)
            *(__attribute__((address_space(3))) f32x4*)(L + w * 8192 + (i * 4 + t) * 1024 + lane * 16) = c2[i][t];
    LBAR();
#pragma unroll
    for (int i = 0; i < 2; ++i) {
        f32x4 fs = f32x4{0.f, 0.f, 0.f, 0.f};
#pragma unroll
        for (int k = 0; k < 4; ++k) fs += lds_f4(L + (2 * k + th) * 8192 + (i * 4 + kg) * 1024 + lane * 16);
        const f32x4 sv = lds_f4(L + 65536 + th * 8192 + (i * 4 + kg) * 1024 + lane * 16);
        const u32x2 xr = i ? xr1 : xr0;
        float o[4];
#pragma unroll
        for (int r = 0; r < 4; ++r) {
            const float fb = rbf(fs[r]);
            const float gb = rbf(fb * __builtin_amdgcn_rcpf(1.0f + __builtin_amdgcn_exp2f(-fb * 1.4426950408889634f)));
            const float pb = rbf(rbf(sv[r]) * gb);
            const unsigned xw = xr[r >> 1];
            const float xv = __uint_as_float((r & 1) ? (xw & 0xFFFF0000u) : (xw << 16));
            o[r] = xv + pb;
        }
        *(u32x2*)(out + oo + (size_t)i * 16 * 896) = u32x2{pk2(o[0], o[1]), pk2(o[2], o[3])};
    }
    STAMP(15)
    STAMP_FLUSH(ws, 0, 16)
}

extern "C" void kernel_launch(void* const* d_in, const int* in_sizes, int n_in, void* d_out, int out_size, void* d_ws,
                              size_t ws_size, hipStream_t stream) {
    (void)hipFuncSetAttribute((const void*)k_fused, hipFuncAttributeMaxDynamicSharedMemorySize, LDS_BYTES);
    k_fused<<<256, 512, LDS_BYTES, stream>>>((const char*)d_in[0], (const char*)d_in[1], (const char*)d_in[2], (const char*)d_in[4],
                                             (const char*)d_in[5], (const char*)d_in[6], (char*)d_out, (char*)d_ws);
}
```

```cpp
#define WARM_CODE
#define WARM_TLB
#include <hip/hip_runtime.h>
#include <hip/hip_bf16.h>

typedef short bf16x8 __attribute__((ext_vector_type(8)));
typedef short s16x4 __attribute__((ext_vector_type(4)));
typedef float f32x4 __attribute__((ext_vector_type(4)));
typedef float f32x2 __attribute__((ext_vector_type(2)));
typedef unsigned u32x4 __attribute__((ext_vector_type(4)));
typedef unsigned u32x2 __attribute__((ext_vector_type(2)));
typedef __bf16 bf16x2_t __attribute__((ext_vector_type(2)));
typedef __attribute__((address_space(3))) char* lds_ptr;

#define MFMA16(a, b, c) __builtin_amdgcn_mfma_f32_16x16x32_bf16(a, b, c, 0, 0, 0)
#define WAIT_BAR0() asm volatile("s_waitcnt vmcnt(0) lgkmcnt(0)\n\ts_barrier" ::: "memory")
#define LBAR() asm volatile("s_waitcnt lgkmcnt(0)\n\ts_barrier" ::: "memory")

#define STAMP_ARG
#define STAMP_PASS
#define STAMP_DECL
#define STAMP(i)
#define STAMP_FLUSH(actp, lo, hi)
constexpr int S_ = 2048, D_ = 448, F_ = 1792, QKV_ = 1344;
constexpr int ACT_OCT = 280;
constexpr int ACT_TILE_B = ACT_OCT * 256 + 256;

__device__ __forceinline__ void glds16(const void* g, unsigned lds_dst) {
    unsigned sv;
    asm volatile("s_mov_b32 %0, m0\n\ts_mov_b32 m0, %2\n\ts_nop 0\n\tglobal_load_lds_dwordx4 %1, off\n\ts_mov_b32 m0, %0"
                 : "=&s"(sv) : "v"(g), "s"(lds_dst) : "memory");
}
__device__ __forceinline__ void glds16s(const void* sbase, unsigned voff, unsigned lds_dst) {
    unsigned sv;
    asm volatile("s_mov_b32 %0, m0\n\ts_mov_b32 m0, %3\n\ts_nop 0\n\tglobal_load_lds_dwordx4 %1, %2\n\ts_mov_b32 m0, %0"
                 : "=&s"(sv) : "v"(voff), "s"(sbase), "s"(lds_dst) : "memory");
}
__device__ __forceinline__ bf16x8 lds_b128(lds_ptr p) { return *(const __attribute__((address_space(3))) bf16x8*)p; }
__device__ __forceinline__ f32x4 lds_f4(lds_ptr p) { return *(const __attribute__((address_space(3))) f32x4*)p; }
__device__ __forceinline__ s16x4 lds_tr(lds_ptr p) {
    return __builtin_amdgcn_ds_read_tr16_b64_v4i16((__attribute__((address_space(3))) s16x4*)p);
}
__device__ __forceinline__ bf16x8 lds_tr2(lds_ptr p, int second) {
    s16x4 a = lds_tr(p), b = lds_tr(p + second);
    return __builtin_shufflevector(a, b, 0, 1, 2, 3, 4, 5, 6, 7);
}
__device__ __forceinline__ float dot2(unsigned a, unsigned b, float c) {
    return __builtin_amdgcn_fdot2_f32_bf16(__builtin_bit_cast(bf16x2_t, a), __builtin_bit_cast(bf16x2_t, b), c, false);
}
__device__ __forceinline__ float frag_sum(bf16x8 f, float c) {
    u32x4 u = __builtin_bit_cast(u32x4, f);
    c = dot2(u[0], 0x3F803F80u, c); c = dot2(u[1], 0x3F803F80u, c);
    c = dot2(u[2], 0x3F803F80u, c); c = dot2(u[3], 0x3F803F80u, c);
    return c;
}
__device__ __forceinline__ float frag_sq(bf16x8 f, float c) {
    u32x4 u = __builtin_bit_cast(u32x4, f);
    c = dot2(u[0], u[0], c); c = dot2(u[1], u[1], c); c = dot2(u[2], u[2], c); c = dot2(u[3], u[3], c);
    return c;
}
__device__ __forceinline__ float xsum(float x) {
    auto s = __builtin_amdgcn_permlane16_swap(__float_as_uint(x), __float_as_uint(x), false, false);
    x = __uint_as_float(s[0]) + __uint_as_float(s[1]);
    auto t = __builtin_amdgcn_permlane32_swap(__float_as_uint(x), __float_as_uint(x), false, false);
    return __uint_as_float(t[0]) + __uint_as_float(t[1]);
}
__device__ __forceinline__ float xmax(float x) {
    auto s = __builtin_amdgcn_permlane16_swap(__float_as_uint(x), __float_as_uint(x), false, false);
    x = fmaxf(__uint_as_float(s[0]), __uint_as_float(s[1]));
    auto t = __builtin_amdgcn_permlane32_swap(__float_as_uint(x), __float_as_uint(x), false, false);
    return fmaxf(__uint_as_float(t[0]), __uint_as_float(t[1]));
}
__device__ __forceinline__ unsigned pk2(float lo, float hi) {
    bf16x2_t v; v[0] = (__bf16)lo; v[1] = (__bf16)hi;
    return __builtin_bit_cast(unsigned, v);
}
__device__ __forceinline__ unsigned pk2(f32x2 v) { return pk2(v[0], v[1]); }
__device__ __forceinline__ f32x2 lo2(f32x4 v) { return __builtin_shufflevector(v, v, 0, 1); }
__device__ __forceinline__ f32x2 hi2(f32x4 v) { return __builtin_shufflevector(v, v, 2, 3); }
__device__ __forceinline__ f32x2 sp2(float v) { return f32x2{v, v}; }
__device__ __forceinline__ f32x2 fma2(f32x2 a, f32x2 b, f32x2 c) { return __builtin_elementwise_fma(a, b, c); }
__device__ __forceinline__ float rbf(float x) { return (float)(__bf16)x; }
__device__ __forceinline__ void ln_stats(float sum, float sq, float& mean, float& den) {
    float m = sum * (1.0f / D_);
    float v = fmaxf(sq * (1.0f / D_) - m * m, 0.0f);
    mean = rbf(m);
    den = rbf(sqrtf(rbf(rbf(v) + 1e-5f)));
}

typedef __attribute__((address_space(1))) unsigned long long gu64;
#define RLX_AGENT __ATOMIC_RELAXED, __HIP_MEMORY_SCOPE_AGENT
#define SB() __builtin_amdgcn_sched_barrier(0)
#define LDSF(p) (*(__attribute__((address_space(3))) float*)(p))
#define LDSV(T, p) (*(__attribute__((address_space(3))) T*)(p))
constexpr size_t SYNC_OFF = 16u << 20, FLAG_OFF = SYNC_OFF + 4096, STAT_OFF = SYNC_OFF + 65536;
constexpr int ACT_BYTES = 128 * ACT_TILE_B;
constexpr unsigned long long SPIN_TICKS = 20000000ull;

template <int N> __device__ __forceinline__ float ror_add(float x) {
    return x + __uint_as_float(__builtin_amdgcn_update_dpp(0u, __float_as_uint(x), 0x120 + N, 0xF, 0xF, false));
}
__device__ __forceinline__ float wave_sum(float x) { return xsum(ror_add<1>(ror_add<2>(ror_add<4>(ror_add<8>(x))))); }

__device__ __forceinline__ void start_loads(const char* x, char* ws, int row, int lane, unsigned long long& ep, u32x4& rv) {
    const unsigned zero = 0, ro = (lane < 56 ? lane : 55) * 16;
    asm volatile("global_load_dwordx2 %0, %1, %2 sc1" : "=v"(ep) : "v"(zero), "s"(ws + SYNC_OFF) : "memory");
    asm volatile("global_load_dwordx4 %0, %1, %2" : "=v"(rv) : "v"(ro), "s"(x + (size_t)row * 896) : "memory");
}
__device__ __forceinline__ unsigned long long publish_stats(char* ws, int row, int lane, unsigned long long ep, u32x4 rv) {
    const unsigned long long tag = (((unsigned long long)__builtin_amdgcn_readfirstlane((unsigned)(ep >> 32)) << 32) |
                                    (unsigned)__builtin_amdgcn_readfirstlane((unsigned)ep)) + 1ull;
    if (lane >= 56) rv = u32x4{0u, 0u, 0u, 0u};
    const bf16x8 f = __builtin_bit_cast(bf16x8, rv);
    float mean, den;
    ln_stats(wave_sum(frag_sum(f, 0.f)), wave_sum(frag_sq(f, 0.f)), mean, den);
    const unsigned val = (__float_as_uint(mean) & 0xFFFF0000u) | (__float_as_uint(den) >> 16);
    if (lane == 0) __hip_atomic_store((gu64*)(ws + STAT_OFF) + row, ((unsigned long long)(unsigned)tag << 32) | val, RLX_AGENT);
    return tag;
}
__device__ __forceinline__ unsigned fetch_stats(char* ws, int row, unsigned long long tag) {
    const unsigned long long t0 = __builtin_amdgcn_s_memrealtime();
    unsigned long long v;
    for (;;) {
        v = __hip_atomic_load((gu64*)(ws + STAT_OFF) + row, RLX_AGENT);
        if (__all((unsigned)(v >> 32) == (unsigned)tag)) break;
        if (__builtin_amdgcn_s_memrealtime() - t0 > SPIN_TICKS) break;
        __builtin_amdgcn_s_sleep(2);
    }
    return (unsigned)v;
}
__device__ __forceinline__ void wait_flags(char* ws, int first, int n, int lane, unsigned long long tag) {
    const unsigned long long t0 = __builtin_amdgcn_s_memrealtime();
    const int i = first + (lane < n ? lane : n - 1);
    for (;;) {
        const unsigned long long v = __hip_atomic_load((gu64*)(ws + FLAG_OFF + (size_t)i * 64), RLX_AGENT);
        if (__all(v == tag)) break;
        if (__builtin_amdgcn_s_memrealtime() - t0 > SPIN_TICKS) break;
        __builtin_amdgcn_s_sleep(1);
    }
}

struct P2 {
    const char* w1; const char* w2; unsigned wv0, ring; int n2;
};
__device__ __forceinline__ void p2_dma(const P2& P, const char* src, int slot, int half) {
    glds16s(src + half * (8 * 896), half ? (P.wv0 ^ 64) : P.wv0, P.ring + slot * 4096 + half * 1024);
}

constexpr int FF_XS = 32768, FF_UOFF = 15376, FF_WS = 2 * FF_UOFF, FF_W0 = 2 * FF_XS, FF_CS = 163840 - 1024;

__device__ __forceinline__ unsigned long long ffn_block(const char* x, const char* wg, const char* wu, char* ws, int rb, int cb,
                                                        lds_ptr L, const P2& P, bool has_p2 STAMP_ARG) {
    const int tid = threadIdx.x, lane = tid & 63;
    const int w = __builtin_amdgcn_readfirstlane(tid >> 6);
    const int wm = w & 3, wn = w >> 2;
    const unsigned lb = (unsigned)(size_t)L;
    unsigned long long ep; u32x4 rv;
    const int srow = rb * 256 + (16 + cb) * 8 + w;
    start_loads(x, ws, srow, lane, ep, rv);
    const unsigned xo0 = (lane >> 3) * 896 + (((lane & 7) ^ (lane >> 4)) << 4), xo1 = xo0 ^ 64;
    const char* xb = x + (size_t)(rb * 256 + 32 * w) * 896;
    auto dma_x = [&](int c, int j) { glds16s(xb + j * (8 * 896) + c * 128, (j & 1) ? xo1 : xo0, lb + (c & 1) * FF_XS + (32 * w + 8 * j) * 128); };
#pragma unroll
    for (int j = 0; j < 4; ++j) dma_x(0, j);
    unsigned woff[4];
    const char* wsrc[4];
    unsigned wdst[4];
#pragma unroll
    for (int j = 0; j < 4; ++j) {
        int n = w + 8 * j; if (n > 29) n = 29;
        const int mat = n >= 15, nn = n - 15 * mat;
        const int ci = nn * 64 + lane, grp = ci / 120;
        int rem = ci - grp * 120; if (rem >= 112) rem -= 8;
        const int r = rem / 14, chn = rem - r * 14;
        woff[j] = (8 * grp + r) * (F_ * 2) + chn * 16;
        wsrc[j] = (mat ? wu : wg) + cb * 224;
        wdst[j] = lb + FF_W0 + mat * FF_UOFF + nn * 1024;
    }
    auto dma_w = [&](int c, int buf, int j) { if (j < 3 || w < 6) glds16s(wsrc[j] + (size_t)c * (64 * F_ * 2), woff[j], wdst[j] + buf); };
#pragma unroll
    for (int j = 0; j < 4; ++j) dma_w(0, 0, j);
#pragma unroll
    for (int j = 0; j < 4; ++j) dma_w(1, FF_WS, j);
    dma_x(1, 0); dma_x(1, 1);
    asm volatile("s_waitcnt vmcnt(12)" : "+v"(ep), "+v"(rv) :: "memory");
    const unsigned long long tag = publish_stats(ws, srow, lane, ep, rv);
    const int g = lane >> 4, q = (lane >> 2) & 3, p = lane & 3, tk = lane & 15;
    const int xa = (64 * wm + tk) * 128 + ((g ^ ((lane >> 1) & 7)) << 4);
    const int wa = FF_W0 + g * 1920 + q * 224 + (p & 1) * 8 + (p >> 1) * FF_UOFF + wn * 112;
    const int tc1 = (wm + 4 > 6) ? 6 : wm + 4;
    f32x4 acc[4][7];
#pragma unroll
    for (int i = 0; i < 4; ++i)
#pragma unroll
        for (int t = 0; t < 7; ++t) acc[i][t] = f32x4{0.f, 0.f, 0.f, 0.f};
    f32x4 cs0 = f32x4{0.f, 0.f, 0.f, 0.f}, cs1 = cs0;
    const bf16x8 ones = __builtin_bit_cast(bf16x8, u32x4{0x3F803F80u, 0x3F803F80u, 0x3F803F80u, 0x3F803F80u});
    bf16x8 xf[2][4], wf[4], cf;
    if (w < 6) asm volatile("s_waitcnt vmcnt(7)\n\ts_barrier" ::: "memory");
    else asm volatile("s_waitcnt vmcnt(6)\n\ts_barrier" ::: "memory");
    STAMP(1)
#define XRD(B, ks, i) xf[ks][i] = lds_b128((B) + (xa ^ ((ks) * 64)) + (i) * 2048)
#define WRD(B, s) lds_tr2((B) + ((s) / 7) * 7680 + ((s) % 7) * 16, 896)
    {
        lds_ptr W = L + wa;
        XRD(L, 0, 0); XRD(L, 0, 1); XRD(L, 0, 2); XRD(L, 0, 3);
        wf[0] = WRD(W, 0); wf[1] = WRD(W, 1); wf[2] = WRD(W, 2);
    }
    int wb = 0;
#pragma unroll 1
    for (int c = 0; c < 7; ++c) {
        const int wb1 = (wb == 2 * FF_WS) ? 0 : wb + FF_WS, wb2 = (wb == 0) ? 2 * FF_WS : wb - FF_WS;
        lds_ptr X = L + (c & 1) * FF_XS, Xn = L + ((c + 1) & 1) * FF_XS, W = L + wa + wb, Wn = L + wa + wb1;
#pragma unroll
        for (int s = 0; s < 14; ++s) {
            const int ks = s / 7, t = s % 7;
            if (s == 11) {
                if (c < 5) {
                    if (w < 6) asm volatile("s_waitcnt vmcnt(4) lgkmcnt(0)\n\ts_barrier" ::: "memory");
                    else asm volatile("s_waitcnt vmcnt(3) lgkmcnt(0)\n\ts_barrier" ::: "memory");
                } else WAIT_BAR0();
            }
            if (s < 11) wf[(s + 3) % 4] = WRD(W, s + 3); else wf[(s + 3) % 4] = WRD(Wn, s - 11);
            if (s < 4) XRD(X, 1, s);
            if (s == 11) { XRD(Xn, 0, 0); XRD(Xn, 0, 1); }
            if (s == 12) { XRD(Xn, 0, 2); XRD(Xn, 0, 3); }
            if (s == 4 || s == 6 || s == 8 || s == 10) cf = lds_tr2(W + (s >= 8) * 7680 + ((s == 4 || s == 8) ? wm : tc1) * 16, 896);
            if (s < 2) { if (c < 6) dma_x(c + 1, 2 + s); }
            if (s >= 2 && s < 6) { if (c < 5) dma_w(c + 2, wb2, s - 2); }
            if (s >= 12) { if (c < 5) dma_x(c + 2, s - 12); }
#pragma unroll
            for (int i = 0; i < 4; ++i) acc[i][t] = MFMA16(wf[s % 4], xf[ks][i], acc[i][t]);
            if (s == 5 || s == 9) cs0 = MFMA16(cf, ones, cs0);
            if (s == 7 || s == 12) cs1 = MFMA16(cf, ones, cs1);
            SB();
        }
        { bf16x8 a = wf[2], b = wf[3], d = wf[0]; wf[0] = a; wf[1] = b; wf[2] = d; }
        wb = wb1;
    }
    STAMP(4)
    if (tk == 0) {
        LDSV(f32x4, L + FF_CS + (7 * wn + wm) * 64 + g * 16) = cs0;
        if (wm < 3) LDSV(f32x4, L + FF_CS + (7 * wn + wm + 4) * 64 + g * 16) = cs1;
    }
    const unsigned sv = fetch_stats(ws, rb * 256 + 64 * wm + lane, tag);
    LBAR();
    STAMP(5)
    const __amdgpu_buffer_rsrc_t ra = __builtin_amdgcn_make_buffer_rsrc(ws, (short)0, ACT_BYTES, 0x00020000);
    f32x4 csn = lds_f4(L + FF_CS + (7 * wn) * 64 + g * 16);
    const float K1 = -2.3022081986f, K2 = -0.1029432398f;
#pragma unroll
    for (int ip = 0; ip < 2; ++ip) {
        const unsigned sa = __shfl(sv, 32 * ip + tk), sb = __shfl(sv, 32 * ip + 16 + tk);
        const float ma = -__uint_as_float(sa & 0xFFFF0000u), mb = -__uint_as_float(sb & 0xFFFF0000u);
        const float dn = __uint_as_float((lane < 32 ? sa : sb) << 16);
        const float a = __builtin_amdgcn_rcpf(dn), ia2 = dn * dn, c1 = K1 * a, c2 = K2 * a * a * a;
        const unsigned dst = (rb * 16 + wm * 4 + 2 * ip + (lane >> 5)) * ACT_TILE_B + (cb * 14 + wn * 7) * 256 + tk * 16;
        u32x2 hp[7];
#pragma unroll
        for (int t = 0; t < 7; ++t) {
            const f32x4 cs = csn;
            csn = lds_f4(L + FF_CS + (7 * wn + (t + 1) % 7) * 64 + g * 16);
#pragma unroll
            for (int u = 0; u < 2; ++u) {
                const f32x2 c2v = u ? hi2(cs) : lo2(cs);
                const f32x2 va = fma2(sp2(ma), c2v, u ? hi2(acc[2 * ip][t]) : lo2(acc[2 * ip][t]));
                const f32x2 vb = fma2(sp2(mb), c2v, u ? hi2(acc[2 * ip + 1][t]) : lo2(acc[2 * ip + 1][t]));
                auto s0 = __builtin_amdgcn_permlane32_swap(__float_as_uint(va[0]), __float_as_uint(vb[0]), false, false);
                auto s1 = __builtin_amdgcn_permlane32_swap(__float_as_uint(va[1]), __float_as_uint(vb[1]), false, false);
                const f32x2 G = f32x2{__uint_as_float(s0[0]), __uint_as_float(s1[0])}, U = f32x2{__uint_as_float(s0[1]), __uint_as_float(s1[1])};
                const f32x2 z = G * fma2(G * G, sp2(c2), sp2(c1));
                const f32x2 e = f32x2{__builtin_amdgcn_exp2f(z[0]), __builtin_amdgcn_exp2f(z[1])};
                const f32x2 d = fma2(e, sp2(ia2), sp2(ia2));
                hp[t][u] = pk2((G * U) * f32x2{__builtin_amdgcn_rcpf(d[0]), __builtin_amdgcn_rcpf(d[1])});
            }
            if (t & 1) {
                auto s0 = __builtin_amdgcn_permlane16_swap(hp[t - 1][0], hp[t][0], false, false);
                auto s1 = __builtin_amdgcn_permlane16_swap(hp[t - 1][1], hp[t][1], false, false);
                __builtin_amdgcn_raw_buffer_store_b128(u32x4{s0[0], s1[0], s0[1], s1[1]}, ra, dst + (g & 1) * 256, (t - 1) * 256, 16);
            }
            if (t == 6) __builtin_amdgcn_raw_buffer_store_b64(hp[6], ra, dst + (g & 1) * 8, 6 * 256, 16);
            if (has_p2) {
                const int k = ip * 7 + t;
                if (k < 9) { p2_dma(P, P.w1 + k * (32 * 896), k, 0); p2_dma(P, P.w1 + k * (32 * 896), k, 1); }
            }
            SB();
        }
    }
    STAMP(6)
    WAIT_BAR0();
    if (tid == 0) __hip_atomic_store((gu64*)(ws + FLAG_OFF + (size_t)(rb * 32 + 16 + cb) * 64), tag, RLX_AGENT);
    if (has_p2) { p2_dma(P, P.w1 + 9 * (32 * 896), 9, 0); p2_dma(P, P.w1 + 9 * (32 * 896), 9, 1); }
    STAMP(7)
    return tag;
}

constexpr int AT_WM = 28672, AT_X = 3 * AT_WM, AT_CS = AT_X + 65536, AT_ST = AT_CS + 512;
constexpr int AT_K = 0, AT_V = 32768;

__device__ __forceinline__ unsigned long long att_block(const char* x, const char* wqkv, char* ws, int ch, int head, lds_ptr L,
                                                        const P2& P STAMP_ARG) {
    const int tid = threadIdx.x, lane = tid & 63;
    const int w = __builtin_amdgcn_readfirstlane(tid >> 6);
    const unsigned lb = (unsigned)(size_t)L;
    const int g = lane >> 4, q = (lane >> 2) & 3, p = lane & 3, tk = lane & 15;
    unsigned long long ep; u32x4 rv;
    const int srow = ch * 256 + head * 8 + w;
    start_loads(x, ws, srow, lane, ep, rv);
    const unsigned wo = (lane >> 2) * (QKV_ * 2) + (((lane & 3) ^ (((lane >> 5) & 1) << 1)) << 4);
    auto dma_w = [&](int j) {
        const int n = w + 8 * j, rblk = (n * 43) >> 7, mat = n - rblk * 3;
        glds16s(wqkv + rblk * (16 * QKV_ * 2) + mat * 896 + head * 56 - (mat == 2 ? 8 : 0), wo, lb + mat * AT_WM + rblk * 1024);
    };
    const int hrow = (ch ? ch - 1 : 0) * 256 + 32 * w, orow = ch * 256 + 32 * w;
    const unsigned xo0 = (lane >> 3) * 896 + (((lane & 7) ^ (lane >> 4)) << 4), xo1 = xo0 ^ 64;
    const char* xh = x + (size_t)hrow * 896;
    const char* xs = x + (size_t)orow * 896;
    const unsigned xst = lb + AT_X + w * 8192;
    auto dma_x = [&](int c, int i) {
        const char* src = (i < 2 ? xh : xs) + (i & 1) * (16 * 896) + c * 128;
        glds16s(src, xo0, xst + i * 2048);
        glds16s(src + 8 * 896, xo1, xst + i * 2048 + 1024);
    };
#pragma unroll
    for (int j = 0; j < 5; ++j) dma_w(j);
#pragma unroll
    for (int i = 0; i < 4; ++i) dma_x(0, i);
#pragma unroll
    for (int j = 5; j < 10; ++j) dma_w(j);
    if (w < 4) dma_w(10);
    asm volatile("s_waitcnt vmcnt(18)" : "+v"(ep), "+v"(rv) :: "memory");
    const unsigned long long tag = publish_stats(ws, srow, lane, ep, rv);
    const int xa0 = AT_X + w * 8192 + tk * 128 + ((g ^ ((lane >> 1) & 7)) << 4), xa1 = xa0 ^ 64;
    const int wA0 = (8 * g + q) * 64 + ((g & 1) << 5) + p * 8, wA1 = wA0 ^ 32;
    const int cidx = w > 5 ? 5 : w;
    const int wC = (cidx >> 1) * AT_WM + (wA0 ^ ((cidx & 1) << 5));
    f32x4 aK[4][2], aV[4][2], aQ[2][2], cs = f32x4{0.f, 0.f, 0.f, 0.f};
#pragma unroll
    for (int i = 0; i < 4; ++i) { aK[i][0] = aK[i][1] = aV[i][0] = aV[i][1] = f32x4{0.f, 0.f, 0.f, 0.f}; }
    aQ[0][0] = aQ[0][1] = aQ[1][0] = aQ[1][1] = f32x4{0.f, 0.f, 0.f, 0.f};
    const bf16x8 ones = __builtin_bit_cast(bf16x8, u32x4{0x3F803F80u, 0x3F803F80u, 0x3F803F80u, 0x3F803F80u});
    bf16x8 wf[2][3][2], cf[2], xf[2][2];
#define AWRD(B, ks) { _Pragma("unroll") for (int m = 0; m < 3; ++m) { wf[ks][m][0] = lds_tr2((B) + wA0 + m * AT_WM + (ks) * 2048, 256); \
                      wf[ks][m][1] = lds_tr2((B) + wA1 + m * AT_WM + (ks) * 2048, 256); } cf[ks] = lds_tr2((B) + wC + (ks) * 2048, 256); }
#define AXRD(i) { xf[(i) & 1][0] = lds_b128(L + xa0 + (i) * 2048); xf[(i) & 1][1] = lds_b128(L + xa1 + (i) * 2048); }
#define AMM(i, ks) { _Pragma("unroll") for (int t = 0; t < 2; ++t) { \
                       aK[i][t] = MFMA16(wf[ks][1][t], xf[(i) & 1][ks], aK[i][t]); aV[i][t] = MFMA16(xf[(i) & 1][ks], wf[ks][2][t], aV[i][t]); \
                       if (i >= 2) aQ[(i) & 1][t] = MFMA16(wf[ks][0][t], xf[(i) & 1][ks], aQ[(i) & 1][t]); } }
    asm volatile("s_waitcnt vmcnt(12)\n\ts_barrier" ::: "memory");
    STAMP(1)
    AXRD(0) AWRD(L, 0) AWRD(L, 1)
    SB();
#pragma unroll 1
    for (int c = 0; c < 6; ++c) {
        lds_ptr Wn = L + (c + 1) * 4096;
#pragma unroll
        for (int i = 0; i < 4; ++i) {
            asm volatile("s_waitcnt vmcnt(4)" ::: "memory");
            if (i == 3 && c == 2) asm volatile("s_barrier" ::: "memory");
            AXRD((i + 1) & 3)
            AMM(i, 0)
            if (i == 0) cs = MFMA16(cf[0], ones, cs);
            SB();
            if (i == 3) AWRD(Wn, 0)
            AMM(i, 1)
            if (i == 1) cs = MFMA16(cf[1], ones, cs);
            SB();
            if (i == 3) AWRD(Wn, 1)
            dma_x(c + 1, i);
            SB();
        }
    }
    STAMP(3)
#pragma unroll
    for (int i = 0; i < 4; ++i) {
        if (i == 0) asm volatile("s_waitcnt vmcnt(4)" ::: "memory");
        if (i == 1) asm volatile("s_waitcnt vmcnt(2)" ::: "memory");
        if (i == 2) asm volatile("s_waitcnt vmcnt(0)" ::: "memory");
        if (i < 3) AXRD(i + 1)
        AMM(i, 0)
        if (i == 0) cs = MFMA16(cf[0], ones, cs);
        AMM(i, 1)
        if (i == 1) cs = MFMA16(cf[1], ones, cs);
        SB();
    }
    STAMP(4)
    if (tk == 0 && w < 6) LDSV(f32x4, L + AT_CS + w * 64 + g * 16) = cs;
    {
        const unsigned sv = fetch_stats(ws, (lane < 32 ? hrow : orow - 32) + lane, tag);
        LDSF(L + AT_ST + w * 512 + lane * 4) = -__uint_as_float(sv & 0xFFFF0000u);
        LDSF(L + AT_ST + w * 512 + 256 + lane * 4) = __builtin_amdgcn_rcpf(__uint_as_float(sv << 16));
    }
    LBAR();
    bf16x8 qf[2];
    {
        f32x4 cq[2], ck[2];
        float cv[2];
#pragma unroll
        for (int t = 0; t < 2; ++t) {
            cq[t] = lds_f4(L + AT_CS + t * 64 + g * 16);
            ck[t] = lds_f4(L + AT_CS + 128 + t * 64 + g * 16);
            cv[t] = LDSF(L + AT_CS + 256 + t * 64 + tk * 4);
        }
        const bool pad = (lane >= 48);
#pragma unroll
        for (int i = 0; i < 4; ++i) {
            const float nm = LDSF(L + AT_ST + w * 512 + i * 64 + tk * 4), rs = LDSF(L + AT_ST + w * 512 + 256 + i * 64 + tk * 4);
            const float rs1 = pad ? 0.f : rs;
            const int kt = (i >> 1) * 16 + 2 * w + (i & 1);
#define CORR(A, C) u32x4{pk2(fma2(sp2(nm), lo2(C[0]), lo2(A[0])) * sp2(rs)), pk2(fma2(sp2(nm), hi2(C[0]), hi2(A[0])) * sp2(rs)), \
                         pk2(fma2(sp2(nm), lo2(C[1]), lo2(A[1])) * sp2(rs1)), pk2(fma2(sp2(nm), hi2(C[1]), hi2(A[1])) * sp2(rs1))}
            LDSV(u32x4, L + AT_K + kt * 1024 + lane * 16) = CORR(aK[i], ck);
            if (i >= 2) qf[i - 2] = __builtin_bit_cast(bf16x8, CORR(aQ[i - 2], cq));
        }
#pragma unroll
        for (int b = 0; b < 2; ++b) {
            f32x4 s0[2], s1[2];
#pragma unroll
            for (int u = 0; u < 2; ++u) {
                s0[u] = lds_f4(L + AT_ST + w * 512 + (2 * b + u) * 64 + g * 16);
                s1[u] = lds_f4(L + AT_ST + w * 512 + 256 + (2 * b + u) * 64 + g * 16);
            }
#pragma unroll
            for (int t = 0; t < 2; ++t) {
                u32x4 o;
#pragma unroll
                for (int u = 0; u < 2; ++u) {
                    o[2 * u] = pk2(fma2(lo2(s0[u]), sp2(cv[t]), lo2(aV[2 * b + u][t])) * lo2(s1[u]));
                    o[2 * u + 1] = pk2(fma2(hi2(s0[u]), sp2(cv[t]), hi2(aV[2 * b + u][t])) * hi2(s1[u]));
                }
                if (t == 0 && tk == 0) o = u32x4{0x3F803F80u, 0x3F803F80u, 0x3F803F80u, 0x3F803F80u};
                LDSV(u32x4, L + AT_V + ((b * 8 + w) * 2 + t) * 1024 + lane * 16) = o;
            }
        }
    }
    LBAR();
    STAMP(5)
    const float NEG = -1e30f;
    f32x4 mle, mgt;
#pragma unroll
    for (int r = 0; r < 4; ++r) { mle[r] = (4 * g + r <= tk) ? 0.f : NEG; mgt[r] = (4 * g + r > tk) ? 0.f : NEG; }
    const int jmin = ch ? 0 : 16 - 2 * w;
    f32x4 Sc[2][18];
#pragma unroll
    for (int j = 0; j < 18; ++j) {
        const bf16x8 kf = lds_b128(L + AT_K + (2 * w + j) * 1024 + lane * 16);
        const float bias = (j < jmin) ? NEG : 0.f;
#pragma unroll
        for (int qt = 0; qt < 2; ++qt) {
            if (j >= qt && j <= qt + 16) {
                f32x4 c0;
                if (j == qt + 16) c0 = mle;
                else if (j == qt) c0 = mgt + bias;
                else c0 = f32x4{bias, bias, bias, bias};
                Sc[qt][j] = MFMA16(kf, qf[qt], c0);
            } else {
                Sc[qt][j] = f32x4{0.f, 0.f, 0.f, 0.f};
            }
        }
    }
    const float CS = 0.18898223650461362f * 1.4426950408889634f;
    u32x4 pf[2][9];
#pragma unroll
    for (int qt = 0; qt < 2; ++qt) {
        float m = NEG;
#pragma unroll
        for (int j = qt; j <= qt + 16; ++j) m = fmaxf(fmaxf(m, fmaxf(Sc[qt][j][0], Sc[qt][j][1])), fmaxf(Sc[qt][j][2], Sc[qt][j][3]));
        m = xmax(m);
        const float mc = -m * CS;
#pragma unroll
        for (int j = qt; j <= qt + 16; ++j) {
            const f32x2 a = fma2(lo2(Sc[qt][j]), sp2(CS), sp2(mc)), b = fma2(hi2(Sc[qt][j]), sp2(CS), sp2(mc));
            Sc[qt][j] = f32x4{__builtin_amdgcn_exp2f(a[0]), __builtin_amdgcn_exp2f(a[1]), __builtin_amdgcn_exp2f(b[0]), __builtin_amdgcn_exp2f(b[1])};
        }
#pragma unroll
        for (int b = 0; b < 9; ++b)
            pf[qt][b] = u32x4{pk2(Sc[qt][2 * b][0], Sc[qt][2 * b][1]), pk2(Sc[qt][2 * b][2], Sc[qt][2 * b][3]),
                              pk2(Sc[qt][2 * b + 1][0], Sc[qt][2 * b + 1][1]), pk2(Sc[qt][2 * b + 1][2], Sc[qt][2 * b + 1][3])};
    }
    f32x4 O[2][2];
    O[0][0] = O[0][1] = O[1][0] = O[1][1] = f32x4{0.f, 0.f, 0.f, 0.f};
#pragma unroll
    for (int b = 0; b < 9; ++b)
#pragma unroll
        for (int t = 0; t < 2; ++t) {
            const bf16x8 vf = lds_b128(L + AT_V + ((w + b) * 2 + t) * 1024 + lane * 16);
#pragma unroll
            for (int qt = 0; qt < 2; ++qt) O[qt][t] = MFMA16(vf, __builtin_bit_cast(bf16x8, pf[qt][b]), O[qt][t]);
        }
    const __amdgpu_buffer_rsrc_t ra = __builtin_amdgcn_make_buffer_rsrc(ws, (short)0, ACT_BYTES, 0x00020000);
#pragma unroll
    for (int qt = 0; qt < 2; ++qt) {
        const float l = __shfl(O[qt][0][0], tk);
        const float inv = __builtin_amdgcn_rcpf(l);
        const unsigned dst = (ch * 16 + 2 * w + qt) * ACT_TILE_B + 224 * 256 + tk * 16;
#pragma unroll
        for (int t = 0; t < 2; ++t) {
            const int u = 7 * head + 4 * t + g - 1;
            if (t == 1 || g > 0)
                __builtin_amdgcn_raw_buffer_store_b64(
                    u32x2{pk2(O[qt][t][0] * inv, O[qt][t][1] * inv), pk2(O[qt][t][2] * inv, O[qt][t][3] * inv)}, ra,
                    dst + (u >> 1) * 256 + (u & 1) * 8, 0, 16);
        }
    }
    STAMP(6)
    WAIT_BAR0();
    if (tid == 0) __hip_atomic_store((gu64*)(ws + FLAG_OFF + (size_t)(ch * 32 + head) * 64), tag, RLX_AGENT);
#pragma unroll
    for (int k = 0; k < 10; ++k) { p2_dma(P, P.w1 + k * (32 * 896), k, 0); p2_dma(P, P.w1 + k * (32 * 896), k, 1); }
    STAMP(7)
    return tag;
}

constexpr int LDS_BYTES = 163840;

__global__ __launch_bounds__(512, 2) void k_fused(const char* x, const char* wqkv, const char* wo, const char* wg, const char* wu,
                                                  const char* wd, char* out, char* ws) {
    extern __shared__ __attribute__((aligned(16))) char smem[];
    lds_ptr L = (lds_ptr)smem;
    const unsigned lb = (unsigned)(size_t)L;
    STAMP_DECL STAMP(0)
    const int tid = threadIdx.x, lane = tid & 63;
    const int w = __builtin_amdgcn_readfirstlane(tid >> 6);
    const int xcd = blockIdx.x & 7, idx = blockIdx.x >> 3;
    const int rblk = 4 * (xcd & 1) + (idx & 3), cblk = 4 * (xcd >> 1) + ((idx >> 2) & 3);
    if (w == 7) {
        unsigned long long pc;
        asm volatile("s_getpc_b64 %0" : "=s"(pc));
        glds16s((const char*)(pc & ~255ull), idx * 1024 + lane * 16, lb + 162816);
    }
    if (w == 6) {
        const char* a = lane < 8 ? ws + (size_t)(lane * 16 + 15) * ACT_TILE_B + ACT_OCT * 256
                      : lane < 10 ? wd + (lane - 8) * (F_ * 896 - 64) : lane < 12 ? wo + (lane - 10) * (D_ * 896 - 64)
                      : lane < 14 ? (const char*)out + (lane - 12) * (S_ * 896 - 64) : ws + FLAG_OFF - 64;
        glds16(a, lb + 162816);
    }
    const int th = w & 1, kg = w >> 1;
    const bool has_p2 = idx < 28;
    const int rb = 4 * xcd + (idx & 3), cb = has_p2 ? (idx >> 2) : 0;
    const int a1 = kg ? 14 * (kg - 1) : 56;
    const int a2 = 42 + (kg < 2 ? 3 * kg : 4 * kg - 2);
    P2 P;
    P.n2 = kg < 2 ? 3 : 4;
    P.w1 = (kg ? wd + (size_t)(14 * (kg - 1)) * 32 * 896 : wo) + cb * 128 + th * 16 * 896;
    P.w2 = wd + (size_t)a2 * 32 * 896 + cb * 128 + th * 16 * 896;
    P.wv0 = (lane >> 3) * 896 + (((lane & 7) ^ (((lane >> 4) & 1) << 1)) << 4);
    P.ring = lb + kg * 40960 + th * 2048;
    unsigned long long tag;
    if (idx < 16) tag = att_block(x, wqkv, ws, rblk, cblk, L, P STAMP_PASS);
    else tag = ffn_block(x, wg, wu, ws, rblk, cblk, L, P, has_p2 STAMP_PASS);
    if (!has_p2) {
        if (blockIdx.x == 255 && w == 0) {
#pragma unroll 1
            for (int i = 0; i < 4; ++i) wait_flags(ws, 64 * i, 64, lane, tag);
            if (lane == 0) __hip_atomic_store((gu64*)(ws + SYNC_OFF), tag, RLX_AGENT);
        }
        STAMP_FLUSH(ws, 0, 8)
        return;
    }
    const int g = lane >> 4, q = (lane >> 2) & 3, p = lane & 3, tk = lane & 15;
    const size_t oo = (size_t)(rb * 64 + 32 * th + tk) * 896 + cb * 128 + kg * 32 + g * 8;
    const u32x2 xr0 = *(const u32x2*)(x + oo), xr1 = *(const u32x2*)(x + oo + 16 * 896);
    STAMP(8)
    if (w == 0) wait_flags(ws, (rb >> 2) * 32, 32, lane, tag);
    asm volatile("s_waitcnt vmcnt(0)\n\ts_barrier" ::: "memory");
    STAMP(9)
    const __amdgpu_buffer_rsrc_t ra = __builtin_amdgcn_make_buffer_rsrc(ws, (short)0, ACT_BYTES, 0x00020000);
    const unsigned ab = (rb * 4 + 2 * th) * ACT_TILE_B + lane * 16;
    bf16x8 af[14][2], ag[4][2];
#define ALD(ks, i) __builtin_bit_cast(bf16x8, __builtin_amdgcn_raw_buffer_load_b128(ra, ab + (i) * ACT_TILE_B, (ks) * 1024, 16))
#pragma unroll
    for (int s = 0; s < 14; ++s) { af[s][0] = ALD(a1 + s, 0); af[s][1] = ALD(a1 + s, 1); SB(); }
    const int f = ((q >> 1) & 1) | ((g & 1) << 1);
    int wa[4];
#pragma unroll
    for (int t = 0; t < 4; ++t) wa[t] = kg * 40960 + (8 * g + q) * 128 + ((t ^ f) << 5) + p * 8;
    f32x4 c1[2][4], c2[2][4];
#pragma unroll
    for (int i = 0; i < 2; ++i)
#pragma unroll
        for (int t = 0; t < 4; ++t) c1[i][t] = c2[i][t] = f32x4{0.f, 0.f, 0.f, 0.f};
#define STEP(ACC, FR, SLOT)                                                          \
    {                                                                                \
        bf16x8 wf[4];                                                                \
        _Pragma("unroll") for (int t = 0; t < 4; ++t) wf[t] = lds_tr2(L + wa[t] + (SLOT) * 4096, 512); \
        _Pragma("unroll") for (int t = 0; t < 4; ++t) {                              \
            ACC[0][t] = MFMA16(wf[t], FR[0], ACC[0][t]);                             \
            ACC[1][t] = MFMA16(wf[t], FR[1], ACC[1][t]);                             \
        }                                                                            \
    }
    STAMP(10)
#pragma unroll
    for (int s = 0; s < 4; ++s) STEP(c1, af[s], s)
    LBAR();
#pragma unroll
    for (int s = 10; s < 14; ++s) { p2_dma(P, P.w1 + s * (32 * 896), s - 10, 0); p2_dma(P, P.w1 + s * (32 * 896), s - 10, 1); }
#pragma unroll
    for (int s = 0; s < 4; ++s) {
        const int sc = s < P.n2 ? s : P.n2 - 1;
        ag[s][0] = ALD(a2 + sc, 0); ag[s][1] = ALD(a2 + sc, 1);
    }
#pragma unroll
    for (int s = 4; s < 8; ++s) STEP(c1, af[s], s)
    LBAR();
    STAMP(11)
#pragma unroll
    for (int s = 0; s < 4; ++s) {
        const char* src = P.w2 + (s < P.n2 ? s : P.n2 - 1) * (32 * 896);
        p2_dma(P, src, 4 + s, 0); p2_dma(P, src, 4 + s, 1);
    }
#pragma unroll
    for (int s = 8; s < 10; ++s) STEP(c1, af[s], s)
    asm volatile("s_waitcnt vmcnt(16) lgkmcnt(0)\n\ts_barrier" ::: "memory");
#pragma unroll
    for (int s = 10; s < 14; ++s) STEP(c1, af[s], s - 10)
    STAMP(12)
    WAIT_BAR0();
    STAMP(13)
#pragma unroll
    for (int s = 0; s < 3; ++s) STEP(c2, ag[s], 4 + s)
    if (P.n2 == 4) STEP(c2, ag[3], 7)
    LBAR();
    STAMP(14)
    if (kg) {
#pragma unroll
        for (int i = 0; i < 2; ++i)
#pragma unroll
            for (int t = 0; t < 4; ++t) c2[i][t] += c1[i][t];
    } else {
#pragma unroll
        for (int i = 0; i < 2; ++i)
#pragma unroll
            for (int t = 0; t < 4; ++t)
                *(__attribute__((address_space(3))) f32x4*)(L + 65536 + th * 8192 + (i * 4 + t) * 1024 + lane * 16) = c1[i][t];
    }
#pragma unroll
    for (int i = 0; i < 2; ++i)
#pragma unroll
        for (int t = 0; t < 4; ++t)
            *(__attribute__((address_space(3))) f32x4*)(L + w * 8192 + (i * 4 + t) * 1024 + lane * 16) = c2[i][t];
    LBAR();
#pragma unroll
    for (int i = 0; i < 2; ++i) {
        f32x4 fs = f32x4{0.f, 0.f, 0.f, 0.f};
#pragma unroll
        for (int k = 0; k < 4; ++k) fs += lds_f4(L + (2 * k + th) * 8192 + (i * 4 + kg) * 1024 + lane * 16);
        const f32x4 sv = lds_f4(L + 65536 + th * 8192 + (i * 4 + kg) * 1024 + lane * 16);
        const u32x2 xr = i ? xr1 : xr0;
        float o[4];
#pragma unroll
        for (int r = 0; r < 4; ++r) {
            const float fb = rbf(fs[r]);
            const float gb = rbf(fb * __builtin_amdgcn_rcpf(1.0f + __builtin_amdgcn_exp2f(-fb * 1.4426950408889634f)));
            const float pb = rbf(rbf(sv[r]) * gb);
            const unsigned xw = xr[r >> 1];
            const float xv = __uint_as_float((r & 1) ? (xw & 0xFFFF0000u) : (xw << 16));
            o[r] = xv + pb;
        }
        *(u32x2*)(out + oo + (size_t)i * 16 * 896) = u32x2{pk2(o[0], o[1]), pk2(o[2], o[3])};
    }
    STAMP(15)
    STAMP_FLUSH(ws, 0, 16)
}

extern "C" void kernel_launch(void* const* d_in, const int* in_sizes, int n_in, void* d_out, int out_size, void* d_ws,
                              size_t ws_size, hipStream_t stream) {
    (void)hipFuncSetAttribute((const void*)k_fused, hipFuncAttributeMaxDynamicSharedMemorySize, LDS_BYTES);
    k_fused<<<256, 512, LDS_BYTES, stream>>>((const char*)d_in[0], (const char*)d_in[1], (const char*)d_in[2], (const char*)d_in[4],
                                             (const char*)d_in[5], (const char*)d_in[6], (char*)d_out, (char*)d_ws);
}
```

```cpp
#include <hip/hip_runtime.h>
#include <hip/hip_bf16.h>

typedef short bf16x8 __attribute__((ext_vector_type(8)));
typedef short s16x4 __attribute__((ext_vector_type(4)));
typedef float f32x4 __attribute__((ext_vector_type(4)));
typedef float f32x2 __attribute__((ext_vector_type(2)));
typedef unsigned u32x4 __attribute__((ext_vector_type(4)));
typedef unsigned u32x2 __attribute__((ext_vector_type(2)));
typedef __bf16 bf16x2_t __attribute__((ext_vector_type(2)));
typedef __attribute__((address_space(3))) char* lds_ptr;

#define MFMA16(a, b, c) __builtin_amdgcn_mfma_f32_16x16x32_bf16(a, b, c, 0, 0, 0)
#define WAIT_BAR0() asm volatile("s_waitcnt vmcnt(0) lgkmcnt(0)\n\ts_barrier" ::: "memory")
#define LBAR() asm volatile("s_waitcnt lgkmcnt(0)\n\ts_barrier" ::: "memory")

#define STAMP_ARG
#define STAMP_PASS
#define STAMP_DECL
#define STAMP(i)
#define STAMP_FLUSH(actp, lo, hi)
constexpr int S_ = 2048, D_ = 448, F_ = 1792, QKV_ = 1344;
constexpr int ACT_OCT = 280;
constexpr int ACT_TILE_B = ACT_OCT * 256 + 256;

__device__ __forceinline__ void glds16(const void* g, unsigned lds_dst) {
    unsigned sv;
    asm volatile("s_mov_b32 %0, m0\n\ts_mov_b32 m0, %2\n\ts_nop 0\n\tglobal_load_lds_dwordx4 %1, off\n\ts_mov_b32 m0, %0"
                 : "=&s"(sv) : "v"(g), "s"(lds_dst) : "memory");
}
__device__ __forceinline__ void glds16s(const void* sbase, unsigned voff, unsigned lds_dst) {
    unsigned sv;
    asm volatile("s_mov_b32 %0, m0\n\ts_mov_b32 m0, %3\n\ts_nop 0\n\tglobal_load_lds_dwordx4 %1, %2\n\ts_mov_b32 m0, %0"
                 : "=&s"(sv) : "v"(voff), "s"(sbase), "s"(lds_dst) : "memory");
}
__device__ __forceinline__ bf16x8 lds_b128(lds_ptr p) { return *(const __attribute__((address_space(3))) bf16x8*)p; }
__device__ __forceinline__ f32x4 lds_f4(lds_ptr p) { return *(const __attribute__((address_space(3))) f32x4*)p; }
__device__ __forceinline__ s16x4 lds_tr(lds_ptr p) {
    return __builtin_amdgcn_ds_read_tr16_b64_v4i16((__attribute__((address_space(3))) s16x4*)p);
}
__device__ __forceinline__ bf16x8 lds_tr2(lds_ptr p, int second) {
    s16x4 a = lds_tr(p), b = lds_tr(p + second);
    return __builtin_shufflevector(a, b, 0, 1, 2, 3, 4, 5, 6, 7);
}
__device__ __forceinline__ float dot2(unsigned a, unsigned b, float c) {
    return __builtin_amdgcn_fdot2_f32_bf16(__builtin_bit_cast(bf16x2_t, a), __builtin_bit_cast(bf16x2_t, b), c, false);
}
__device__ __forceinline__ float frag_sum(bf16x8 f, float c) {
    u32x4 u = __builtin_bit_cast(u32x4, f);
    c = dot2(u[0], 0x3F803F80u, c); c = dot2(u[1], 0x3F803F80u, c);
    c = dot2(u[2], 0x3F803F80u, c); c = dot2(u[3], 0x3F803F80u, c);
    return c;
}
__device__ __forceinline__ float frag_sq(bf16x8 f, float c) {
    u32x4 u = __builtin_bit_cast(u32x4, f);
    c = dot2(u[0], u[0], c); c = dot2(u[1], u[1], c); c = dot2(u[2], u[2], c); c = dot2(u[3], u[3], c);
    return c;
}
__device__ __forceinline__ float xsum(float x) {
    auto s = __builtin_amdgcn_permlane16_swap(__float_as_uint(x), __float_as_uint(x), false, false);
    x = __uint_as_float(s[0]) + __uint_as_float(s[1]);
    auto t = __builtin_amdgcn_permlane32_swap(__float_as_uint(x), __float_as_uint(x), false, false);
    return __uint_as_float(t[0]) + __uint_as_float(t[1]);
}
__device__ __forceinline__ float xmax(float x) {
    auto s = __builtin_amdgcn_permlane16_swap(__float_as_uint(x), __float_as_uint(x), false, false);
    x = fmaxf(__uint_as_float(s[0]), __uint_as_float(s[1]));
    auto t = __builtin_amdgcn_permlane32_swap(__float_as_uint(x), __float_as_uint(x), false, false);
    return fmaxf(__uint_as_float(t[0]), __uint_as_float(t[1]));
}
__device__ __forceinline__ unsigned pk2(float lo, float hi) {
    bf16x2_t v; v[0] = (__bf16)lo; v[1] = (__bf16)hi;
    return __builtin_bit_cast(unsigned, v);
}
__device__ __forceinline__ unsigned pk2(f32x2 v) { return pk2(v[0], v[1]); }
__device__ __forceinline__ f32x2 lo2(f32x4 v) { return __builtin_shufflevector(v, v, 0, 1); }
__device__ __forceinline__ f32x2 hi2(f32x4 v) { return __builtin_shufflevector(v, v, 2, 3); }
__device__ __forceinline__ f32x2 sp2(float v) { return f32x2{v, v}; }
__device__ __forceinline__ f32x2 fma2(f32x2 a, f32x2 b, f32x2 c) { return __builtin_elementwise_fma(a, b, c); }
__device__ __forceinline__ float rbf(float x) { return (float)(__bf16)x; }
__device__ __forceinline__ void ln_stats(float sum, float sq, float& mean, float& den) {
    float m = sum * (1.0f / D_);
    float v = fmaxf(sq * (1.0f / D_) - m * m, 0.0f);
    mean = rbf(m);
    den = rbf(sqrtf(rbf(rbf(v) + 1e-5f)));
}

typedef __attribute__((address_space(1))) unsigned long long gu64;
#define RLX_AGENT __ATOMIC_RELAXED, __HIP_MEMORY_SCOPE_AGENT
#define SB() __builtin_amdgcn_sched_barrier(0)
#define LDSF(p) (*(__attribute__((address_space(3))) float*)(p))
#define LDSV(T, p) (*(__attribute__((address_space(3))) T*)(p))
constexpr size_t SYNC_OFF = 16u << 20, FLAG_OFF = SYNC_OFF + 4096, STAT_OFF = SYNC_OFF + 65536;
constexpr int ACT_BYTES = 128 * ACT_TILE_B;
constexpr unsigned long long SPIN_TICKS = 20000000ull;

template <int N> __device__ __forceinline__ float ror_add(float x) {
    return x + __uint_as_float(__builtin_amdgcn_update_dpp(0u, __float_as_uint(x), 0x120 + N, 0xF, 0xF, false));
}
__device__ __forceinline__ float wave_sum(float x) { return xsum(ror_add<1>(ror_add<2>(ror_add<4>(ror_add<8>(x))))); }

__device__ __forceinline__ void start_loads(const char* x, char* ws, int row, int lane, unsigned long long& ep, u32x4& rv) {
    const unsigned zero = 0, ro = (lane < 56 ? lane : 55) * 16;
    asm volatile("global_load_dwordx2 %0, %1, %2 sc1" : "=v"(ep) : "v"(zero), "s"(ws + SYNC_OFF) : "memory");
    asm volatile("global_load_dwordx4 %0, %1, %2" : "=v"(rv) : "v"(ro), "s"(x + (size_t)row * 896) : "memory");
}
__device__ __forceinline__ unsigned long long publish_stats(char* ws, int row, int lane, unsigned long long ep, u32x4 rv) {
    const unsigned long long tag = (((unsigned long long)__builtin_amdgcn_readfirstlane((unsigned)(ep >> 32)) << 32) |
                                    (unsigned)__builtin_amdgcn_readfirstlane((unsigned)ep)) + 1ull;
    if (lane >= 56) rv = u32x4{0u, 0u, 0u, 0u};
    const bf16x8 f = __builtin_bit_cast(bf16x8, rv);
    float mean, den;
    ln_stats(wave_sum(frag_sum(f, 0.f)), wave_sum(frag_sq(f, 0.f)), mean, den);
    const unsigned val = (__float_as_uint(mean) & 0xFFFF0000u) | (__float_as_uint(den) >> 16);
    if (lane == 0) __hip_atomic_store((gu64*)(ws + STAT_OFF) + row, ((unsigned long long)(unsigned)tag << 32) | val, RLX_AGENT);
    return tag;
}
__device__ __forceinline__ void stats_request(char* ws, int row, unsigned long long& v) {
    asm volatile("global_load_dwordx2 %0, %1, %2 sc1" : "=v"(v) : "v"(row * 8), "s"(ws + STAT_OFF) : "memory");
}
__device__ __forceinline__ unsigned stats_check(char* ws, int row, unsigned long long tag, unsigned long long v) {
    const unsigned long long t0 = __builtin_amdgcn_s_memrealtime();
    while (!__all((unsigned)(v >> 32) == (unsigned)tag)) {
        if (__builtin_amdgcn_s_memrealtime() - t0 > SPIN_TICKS) break;
        __builtin_amdgcn_s_sleep(2);
        v = __hip_atomic_load((gu64*)(ws + STAT_OFF) + row, RLX_AGENT);
    }
    return (unsigned)v;
}
__device__ __forceinline__ void wait_flags(char* ws, int first, int n, int lane, unsigned long long tag) {
    const unsigned long long t0 = __builtin_amdgcn_s_memrealtime();
    const int i = first + (lane < n ? lane : n - 1);
    for (;;) {
        const unsigned long long v = __hip_atomic_load((gu64*)(ws + FLAG_OFF + (size_t)i * 64), RLX_AGENT);
        if (__all(v == tag)) break;
        if (__builtin_amdgcn_s_memrealtime() - t0 > SPIN_TICKS) break;
        __builtin_amdgcn_s_sleep(1);
    }
}

struct P2 {
    const char* w1; const char* w2; unsigned ring; int n2, th, kg;
    unsigned wv0; int wa[4], own;
};
__device__ __forceinline__ void p2_lane(P2& P) {
    int lane = threadIdx.x & 63;
    asm volatile("" : "+v"(lane));
    const int g = lane >> 4, q = (lane >> 2) & 3, p = lane & 3, f = ((q >> 1) & 1) | ((g & 1) << 1);
    P.wv0 = (lane >> 3) * 896 + (((lane & 7) ^ (((lane >> 4) & 1) << 1)) << 4);
#pragma unroll
    for (int t = 0; t < 4; ++t) P.wa[t] = P.kg * 4096 + (8 * g + q) * 128 + ((t ^ f) << 5) + p * 8;
    P.own = P.th * 2048 + (g >> 1) * (16384 - 2048);
}
__device__ __forceinline__ void p2_dma(const P2& P, int u, int slot, int half) {
    const char* src = u < 14 ? P.w1 + u * (32 * 896) : P.w2 + (u - 14 < P.n2 ? u - 14 : P.n2 - 1) * (32 * 896);
    glds16s(src + half * (8 * 896), half ? (P.wv0 ^ 64) : P.wv0, P.ring + slot * 16384 + half * 1024);
}
__device__ __forceinline__ void p2_dma2(const P2& P, int u, int slot) { p2_dma(P, u, slot, 0); p2_dma(P, u, slot, 1); }
#define P2_WRD(t, SLOT) lds_tr2(L + P.wa[t] + ((SLOT) >> 2) * 65536 + ((SLOT) & 3) * 16384, 512)
__device__ __forceinline__ void p2_dma_own(const P2& P, int u, int sl, int j) {
    glds16s(P.w1 + (u * 32 + j * 8 - P.th * 16) * 896, (j & 1) ? (P.wv0 ^ 64) : P.wv0, P.ring + (sl + (j >> 1)) * 16384 + (j & 1) * 1024);
}
__device__ __forceinline__ void p2_dma_own4(const P2& P, int u, int sl) {
    p2_dma_own(P, u, sl, 0); p2_dma_own(P, u, sl, 1); p2_dma_own(P, u, sl, 2); p2_dma_own(P, u, sl, 3);
}
__device__ __forceinline__ void p2_to_regs(const P2& P, lds_ptr L, bf16x8 (&wr)[4], int sl) {
#pragma unroll
    for (int t = 0; t < 4; ++t) wr[t] = lds_tr2(L + P.wa[t] + P.own + (sl >> 2) * 65536 + (sl & 3) * 16384, 512);
}
#define WAITL0() asm volatile("s_waitcnt lgkmcnt(0)" ::: "memory")

constexpr int FF_XS = 32768, FF_UOFF = 15376, FF_WS = 2 * FF_UOFF, FF_W0 = 2 * FF_XS, FF_CS = 163840 - 1024;

__device__ __forceinline__ unsigned long long ffn_block(const char* x, const char* wg, const char* wu, char* ws, int rb, int cb,
                                                        lds_ptr L, P2& P, bf16x8 (&wr)[5][4] STAMP_ARG) {
    const int tid = threadIdx.x, lane = tid & 63;
    const int w = __builtin_amdgcn_readfirstlane(tid >> 6);
    const int wm = w & 3, wn = w >> 2;
    const unsigned lb = (unsigned)(size_t)L;
    unsigned long long ep; u32x4 rv;
    const int srow = rb * 256 + (16 + cb) * 8 + w;
    start_loads(x, ws, srow, lane, ep, rv);
    const unsigned xo0 = (lane >> 3) * 896 + (((lane & 7) ^ (lane >> 4)) << 4), xo1 = xo0 ^ 64;
    const char* xb = x + (size_t)(rb * 256 + 64 * wm) * 896;
    auto dma_x = [&](int c, int j) { glds16s(xb + j * (8 * 896) + c * 128, (j & 1) ? xo1 : xo0, lb + (c & 1) * FF_XS + (64 * wm + 8 * j) * 128); };
    unsigned woff[8];
    auto dma_w1 = [&](int c, int buf, int j) {
        const int n = (j == 7 && wm > 1) ? 29 : wm + 4 * j, mat = n >= 15, nn = n - 15 * mat;
        glds16s((mat ? wu : wg) + cb * 224 + (size_t)c * (64 * F_ * 2), woff[j], lb + FF_W0 + mat * FF_UOFF + nn * 1024 + buf);
    };
    if (wn == 0) {
#pragma unroll
        for (int j = 0; j < 8; ++j) dma_x(0, j);
#pragma unroll
        for (int j = 0; j < 3; ++j) dma_x(1, j);
    }
#pragma unroll
    for (int j = 0; j < 8; ++j) {
        int n = wm + 4 * j; if (n > 29) n = 29;
        const int nn = n >= 15 ? n - 15 : n;
        const int ci = nn * 64 + lane, grp = ci / 120;
        int rem = ci - grp * 120; if (rem >= 112) rem -= 8;
        const int r = rem / 14, chn = rem - r * 14;
        woff[j] = (8 * grp + r) * (F_ * 2) + chn * 16;
        if (wn == 1) dma_w1(0, 0, j);
    }
    if (wn == 1) {
#pragma unroll
        for (int j = 0; j < 8; ++j) dma_w1(1, FF_WS, j);
    }
    asm volatile("s_waitcnt vmcnt(11)" : "+v"(ep), "+v"(rv) :: "memory");
    const unsigned long long tag = publish_stats(ws, srow, lane, ep, rv);
    const int g = lane >> 4, q = (lane >> 2) & 3, p = lane & 3, tk = lane & 15;
    const int xa = (64 * wm + tk) * 128 + ((g ^ ((lane >> 1) & 7)) << 4);
    const int wa = FF_W0 + g * 1920 + q * 224 + (p & 1) * 8 + (p >> 1) * FF_UOFF + wn * 112;
    const int tc1 = (wm + 4 > 6) ? 6 : wm + 4;
    f32x4 acc[4][7];
#pragma unroll
    for (int i = 0; i < 4; ++i)
#pragma unroll
        for (int t = 0; t < 7; ++t) acc[i][t] = f32x4{0.f, 0.f, 0.f, 0.f};
    f32x4 cs0 = f32x4{0.f, 0.f, 0.f, 0.f}, cs1 = cs0;
    const bf16x8 ones = __builtin_bit_cast(bf16x8, u32x4{0x3F803F80u, 0x3F803F80u, 0x3F803F80u, 0x3F803F80u});
    bf16x8 xf[2][4], wf[4], cf;
    if (wn == 0) asm volatile("s_waitcnt vmcnt(4)\n\ts_barrier" ::: "memory");
    else asm volatile("s_waitcnt vmcnt(9)\n\ts_barrier" ::: "memory");
    STAMP(1)
#define XRD(B, ks, i) xf[ks][i] = lds_b128((B) + (xa ^ ((ks) * 64)) + (i) * 2048)
#define WRD(B, s) lds_tr2((B) + ((s) / 7) * 7680 + ((s) % 7) * 16, 896)
    {
        lds_ptr W = L + wa;
        XRD(L, 0, 0); XRD(L, 0, 1); XRD(L, 0, 2); XRD(L, 0, 3);
        wf[0] = WRD(W, 0); wf[1] = WRD(W, 1); wf[2] = WRD(W, 2);
    }
    unsigned long long sreq;
    int wb = 0;
#define FCHUNK(c, ROLE, MODE)                                                                                          \
    {                                                                                                                  \
        const int wb1 = (wb == 2 * FF_WS) ? 0 : wb + FF_WS, wb2 = (wb == 0) ? 2 * FF_WS : wb - FF_WS;                  \
        lds_ptr X = L + ((c) & 1) * FF_XS, Xn = L + (((c) + 1) & 1) * FF_XS, W = L + wa + wb, Wn = L + wa + wb1;       \
        _Pragma("unroll") for (int s = 0; s < 14; ++s) {                                                               \
            const int ks = s / 7, t = s % 7;                                                                           \
            if (s == 11 && MODE < 2) {                                                                                 \
                if (ROLE == 1 && MODE == 0) asm volatile("s_waitcnt vmcnt(8) lgkmcnt(0)\n\ts_barrier" ::: "memory");  \
                else WAIT_BAR0();                                                                                      \
            }                                                                                                          \
            if (s < 11) wf[(s + 3) % 4] = WRD(W, s + 3); else if (MODE < 2) wf[(s + 3) % 4] = WRD(Wn, s - 11);        \
            if (s < 4) XRD(X, 1, s);                                                                                   \
            if (s == 11 && MODE < 2) { XRD(Xn, 0, 0); XRD(Xn, 0, 1); }                                                 \
            if (s == 12 && MODE < 2) { XRD(Xn, 0, 2); XRD(Xn, 0, 3); }                                                 \
            if (s == 4 || s == 6 || s == 8 || s == 10) cf = lds_tr2(W + (s >= 8) * 7680 + ((s == 4 || s == 8) ? wm : tc1) * 16, 896); \
            if (ROLE == 0 && MODE < 2 && s < 5) dma_x((c) + 1, 3 + s);                                                 \
            if (ROLE > 0 && MODE == 0 && s >= 3 && s < 11) dma_w1((c) + 2, wb2, s - 3);                         \
            if (ROLE == 0 && MODE == 0 && s >= 11) dma_x((c) + 2, s - 11);                                             \
            if (MODE == 3 && s >= 2 && s < 10) p2_dma_own(P, (s - 2) >> 2, s < 6 ? 2 : 6, (s - 2) & 3);                \
            _Pragma("unroll") for (int i = 0; i < 4; ++i) acc[i][t] = MFMA16(wf[s % 4], xf[ks][i], acc[i][t]);        \
            if (s == 5 || s == 9) cs0 = MFMA16(cf, ones, cs0);                                                         \
            if (s == 7 || s == 12) cs1 = MFMA16(cf, ones, cs1);                                                        \
            SB();                                                                                                      \
        }                                                                                                              \
        { bf16x8 a_ = wf[2], b_ = wf[3], d_ = wf[0]; wf[0] = a_; wf[1] = b_; wf[2] = d_; }                             \
        wb = wb1;                                                                                                      \
    }
    if (wn == 0) {
#pragma unroll 1
        for (int c = 0; c < 5; ++c) FCHUNK(c, 0, 0)
        stats_request(ws, rb * 256 + 64 * wm + lane, sreq);
        FCHUNK(5, 0, 1)
    } else {
#pragma unroll 1
        for (int c = 0; c < 5; ++c) FCHUNK(c, 1, 0)
        stats_request(ws, rb * 256 + 64 * wm + lane, sreq);
        FCHUNK(5, 1, 1)
    }
    STAMP(3)
    p2_lane(P);
    FCHUNK(6, 0, 3)
    STAMP(4)
    if (tk == 0) {
        LDSV(f32x4, L + FF_CS + (7 * wn + wm) * 64 + g * 16) = cs0;
        if (wm < 3) LDSV(f32x4, L + FF_CS + (7 * wn + wm + 4) * 64 + g * 16) = cs1;
    }
    asm volatile("" : "+v"(sreq));
    const unsigned sv = stats_check(ws, rb * 256 + 64 * wm + lane, tag, sreq);
    LBAR();
    STAMP(5)
    const __amdgpu_buffer_rsrc_t ra = __builtin_amdgcn_make_buffer_rsrc(ws, (short)0, ACT_BYTES, 0x00020000);
    f32x4 csr[7];
#pragma unroll
    for (int t = 0; t < 7; ++t) csr[t] = lds_f4(L + FF_CS + (7 * wn + t) * 64 + g * 16);
    LBAR();
    const float K1 = -2.3022081986f, K2 = -0.1029432398f;
#pragma unroll
    for (int ip = 0; ip < 2; ++ip) {
        const unsigned sa = __shfl(sv, 32 * ip + tk), sb = __shfl(sv, 32 * ip + 16 + tk);
        const float ma = -__uint_as_float(sa & 0xFFFF0000u), mb = -__uint_as_float(sb & 0xFFFF0000u);
        const float dn = __uint_as_float((lane < 32 ? sa : sb) << 16);
        const float a = __builtin_amdgcn_rcpf(dn), ia2 = dn * dn, c1 = K1 * a, c2 = K2 * a * a * a;
        const unsigned dst = (rb * 16 + wm * 4 + 2 * ip + (lane >> 5)) * ACT_TILE_B + (cb * 14 + wn * 7) * 256 + tk * 16;
        u32x2 hp[7];
#pragma unroll
        for (int t = 0; t < 7; ++t) {
            const f32x4 cs = csr[t];
#pragma unroll
            for (int u = 0; u < 2; ++u) {
                const f32x2 c2v = u ? hi2(cs) : lo2(cs);
                const f32x2 va = fma2(sp2(ma), c2v, u ? hi2(acc[2 * ip][t]) : lo2(acc[2 * ip][t]));
                const f32x2 vb = fma2(sp2(mb), c2v, u ? hi2(acc[2 * ip + 1][t]) : lo2(acc[2 * ip + 1][t]));
                auto s0 = __builtin_amdgcn_permlane32_swap(__float_as_uint(va[0]), __float_as_uint(vb[0]), false, false);
                auto s1 = __builtin_amdgcn_permlane32_swap(__float_as_uint(va[1]), __float_as_uint(vb[1]), false, false);
                const f32x2 G = f32x2{__uint_as_float(s0[0]), __uint_as_float(s1[0])}, U = f32x2{__uint_as_float(s0[1]), __uint_as_float(s1[1])};
                const f32x2 z = G * fma2(G * G, sp2(c2), sp2(c1));
                const f32x2 e = f32x2{__builtin_amdgcn_exp2f(z[0]), __builtin_amdgcn_exp2f(z[1])};
                const f32x2 d = fma2(e, sp2(ia2), sp2(ia2));
                hp[t][u] = pk2((G * U) * f32x2{__builtin_amdgcn_rcpf(d[0]), __builtin_amdgcn_rcpf(d[1])});
            }
            if (t & 1) {
                auto s0 = __builtin_amdgcn_permlane16_swap(hp[t - 1][0], hp[t][0], false, false);
                auto s1 = __builtin_amdgcn_permlane16_swap(hp[t - 1][1], hp[t][1], false, false);
                __builtin_amdgcn_raw_buffer_store_b128(u32x4{s0[0], s1[0], s0[1], s1[1]}, ra, dst + (g & 1) * 256, (t - 1) * 256, 16);
            }
            if (t == 6) __builtin_amdgcn_raw_buffer_store_b64(hp[6], ra, dst + (g & 1) * 8, 6 * 256, 16);
            {
                if (ip == 0) {
                    if (t < 6) { p2_dma_own(P, 2 + (t >> 1), 4 * (t >> 1), 2 * (t & 1)); p2_dma_own(P, 2 + (t >> 1), 4 * (t >> 1), 2 * (t & 1) + 1); }
                } else if (t < 5) p2_dma2(P, 5 + t, t);
            }
            SB();
        }
        if (ip == 0) {
            asm volatile("s_waitcnt vmcnt(1)" ::: "memory");
            p2_to_regs(P, L, wr[0], 2); p2_to_regs(P, L, wr[1], 6); p2_to_regs(P, L, wr[2], 0); p2_to_regs(P, L, wr[3], 4); p2_to_regs(P, L, wr[4], 8);
            WAITL0();
            SB();
        }
    }
    STAMP(6)
    WAIT_BAR0();
    if (tid == 0) __hip_atomic_store((gu64*)(ws + FLAG_OFF + (size_t)(rb * 32 + 16 + cb) * 64), tag, RLX_AGENT);
#pragma unroll
    for (int k = 5; k < 10; ++k) p2_dma2(P, 5 + k, k);
    STAMP(7)
    return tag;
}

constexpr int AT_WM = 4096, AT_WS = 3 * AT_WM, AT_W = 131072;
constexpr int AT_K = 98304, AT_V = AT_K + 32768;

__device__ __forceinline__ unsigned long long att_block(const char* x, const char* wqkv, char* ws, int ch, int head, lds_ptr L,
                                                        P2& P, bf16x8 (&wr)[5][4] STAMP_ARG) {
    const int tid = threadIdx.x, lane = tid & 63;
    const int w = __builtin_amdgcn_readfirstlane(tid >> 6);
    const unsigned lb = (unsigned)(size_t)L;
    const int g = lane >> 4, q = (lane >> 2) & 3, p = lane & 3, tk = lane & 15;
    unsigned long long ep; u32x4 rv;
    const int srow = ch * 256 + head * 8 + w;
    start_loads(x, ws, srow, lane, ep, rv);
    const unsigned wo = (lane >> 2) * (QKV_ * 2) + (((lane & 3) ^ (((lane >> 5) & 1) << 1)) << 4);
    const char* wsrc0 = wqkv + (w & 3) * (16 * QKV_ * 2) + (w >> 2) * 896 + head * 56;
    const char* wsrc1 = wqkv + (w & 3) * (16 * QKV_ * 2) + 2 * 896 + head * 56 - 8;
    auto dma_w = [&](int c) {
        glds16s(wsrc0 + c * (64 * QKV_ * 2), wo, lb + AT_W + (c & 1) * AT_WS + w * 1024);
        if (w < 4) glds16s(wsrc1 + c * (64 * QKV_ * 2), wo, lb + AT_W + (c & 1) * AT_WS + 8192 + w * 1024);
    };
    const int hrow = (ch ? ch - 1 : 0) * 256 + 32 * w, orow = ch * 256 + 32 * w;
    const unsigned xo0 = (lane >> 3) * 896 + (((lane & 7) ^ (lane >> 4)) << 4), xo1 = xo0 ^ 64;
    const char* xh = x + (size_t)hrow * 896;
    const char* xs = x + (size_t)orow * 896;
    const unsigned xst = lb + w * 8192;
    auto dma_x = [&](int c, int i) {
        const char* src = (i < 2 ? xh : xs) + (i & 1) * (16 * 896) + c * 128;
        glds16s(src, xo0, xst + (c & 1) * 65536 + i * 2048);
        glds16s(src + 8 * 896, xo1, xst + (c & 1) * 65536 + i * 2048 + 1024);
    };
    dma_w(0);
#pragma unroll
    for (int i = 0; i < 4; ++i) dma_x(0, i);
    dma_w(1);
#pragma unroll
    for (int i = 0; i < 4; ++i) dma_x(1, i);
    asm volatile("s_waitcnt vmcnt(18)" : "+v"(ep), "+v"(rv) :: "memory");
    const unsigned long long tag = publish_stats(ws, srow, lane, ep, rv);
    const int xa0 = w * 8192 + tk * 128 + ((g ^ ((lane >> 1) & 7)) << 4), xa1 = xa0 ^ 64;
    const int wA0 = AT_W + (8 * g + q) * 64 + ((g & 1) << 5) + p * 8, wA1 = wA0 ^ 32;
    const int cidx = w > 5 ? 5 : w;
    const int wC = (cidx >> 1) * AT_WM + (wA0 ^ ((cidx & 1) << 5));
    f32x4 aK[4][2], aV[4][2], aQ[2][2], cs = f32x4{0.f, 0.f, 0.f, 0.f};
    const bf16x8 ones = __builtin_bit_cast(bf16x8, u32x4{0x3F803F80u, 0x3F803F80u, 0x3F803F80u, 0x3F803F80u});
#pragma unroll
    for (int i = 0; i < 4; ++i) { aK[i][0] = aK[i][1] = aV[i][0] = aV[i][1] = f32x4{0.f, 0.f, 0.f, 0.f}; }
    aQ[0][0] = aQ[0][1] = aQ[1][0] = aQ[1][1] = f32x4{0.f, 0.f, 0.f, 0.f};
    bf16x8 wf[2][3][2], cf[2], xf[2][2];
#define AWRD(B, ks) { _Pragma("unroll") for (int m = 0; m < 3; ++m) { wf[ks][m][0] = lds_tr2((B) + wA0 + m * AT_WM + (ks) * 2048, 256); \
                      wf[ks][m][1] = lds_tr2((B) + wA1 + m * AT_WM + (ks) * 2048, 256); } cf[ks] = lds_tr2((B) + wC + (ks) * 2048, 256); }
#define AXRD(B, i) { xf[(i) & 1][0] = lds_b128((B) + xa0 + (i) * 2048); xf[(i) & 1][1] = lds_b128((B) + xa1 + (i) * 2048); }
#define AMM(i, ks) { _Pragma("unroll") for (int t = 0; t < 2; ++t) { \
                       aK[i][t] = MFMA16(wf[ks][1][t], xf[(i) & 1][ks], aK[i][t]); aV[i][t] = MFMA16(xf[(i) & 1][ks], wf[ks][2][t], aV[i][t]); \
                       if (i >= 2) aQ[(i) & 1][t] = MFMA16(wf[ks][0][t], xf[(i) & 1][ks], aQ[(i) & 1][t]); } }
    asm volatile("s_waitcnt vmcnt(16)\n\ts_barrier" ::: "memory");
    STAMP(1)
    AXRD(L, 0) AWRD(L, 0) AWRD(L, 1)
    SB();
    auto dma_w2 = [&](int c, bool both) {
        glds16s(wsrc0 + c * (64 * QKV_ * 2), wo, lb + AT_W + (c & 1) * AT_WS + w * 1024);
        if (both) glds16s(wsrc1 + c * (64 * QKV_ * 2), wo, lb + AT_W + (c & 1) * AT_WS + 8192 + w * 1024);
    };
    const int strow = (lane < 32 ? hrow : orow - 32) + lane;
    unsigned long long sreq;
#define WAITV(n) asm volatile("s_waitcnt vmcnt(%0)" :: "i"(n) : "memory")
#define ACHUNK(c, MODE, grp0, FIRST)                                                                                   \
    {                                                                                                     \
        lds_ptr X = L + ((c) & 1) * 65536, Xn = L + (((c) + 1) & 1) * 65536, Wn = L + (((c) + 1) & 1) * AT_WS; \
        _Pragma("unroll") for (int i = 0; i < 4; ++i) {                                                   \
            if (i < 3) { if (MODE == 0) WAITV(8); else if (MODE == 1) WAITV(i == 0 ? 8 : 4); else WAITV(0); } \
            else if (MODE == 0) asm volatile("s_waitcnt vmcnt(4)\n\ts_barrier" ::: "memory");          \
            else if (MODE == 1) asm volatile("s_waitcnt vmcnt(4)\n\ts_barrier" ::: "memory");          \
            if (i < 3) AXRD(X, i + 1) else if (MODE < 2) AXRD(Xn, 0)                                      \
            if (i == 3 && MODE == 0) dma_w2((c) + 2, grp0);                                                      \
            AMM(i, 0)                                                                                     \
            if (i == 0) cs = MFMA16(cf[0], ones, cs);                                                     \
            SB();                                                                                         \
            if (i == 3 && MODE < 2) AWRD(Wn, 0)                                                           \
            AMM(i, 1)                                                                                     \
            if (i == 1) cs = MFMA16(cf[1], ones, cs);                                                     \
            SB();                                                                                         \
            if (i == 3 && MODE < 2) AWRD(Wn, 1)                                                           \
            if (MODE == 0 && (i & 1) && grp0) { dma_x((c) + 2, i - 1); dma_x((c) + 2, i); }               \
            if (MODE == 0 && i == 2 && !grp0) { dma_x((c) + 2, 0); dma_x((c) + 2, 1); }                   \
            if (MODE < 2 && i == 0 && !grp0 && !FIRST) { dma_x((c) + 1, 2); dma_x((c) + 1, 3); }         \
            SB();                                                                                         \
        }                                                                                                 \
    }
    if (w < 4) {
#pragma unroll 1
        for (int c = 0; c < 5; ++c) ACHUNK(c, 0, true, false)
        stats_request(ws, strow, sreq);
        ACHUNK(5, 1, true, false)
    } else {
        ACHUNK(0, 0, false, true)
#pragma unroll 1
        for (int c = 1; c < 5; ++c) ACHUNK(c, 0, false, false)
        stats_request(ws, strow, sreq);
        ACHUNK(5, 1, false, false)
    }
    STAMP(3)
    ACHUNK(6, 2, true, false)
    asm volatile("" : "+v"(sreq));
    STAMP(4)
    lds_ptr ST = L + w * 8192;
    if (tk == 0) LDSV(f32x4, ST + 512 + g * 16) = cs;
    {
        const unsigned sv = stats_check(ws, strow, tag, sreq);
        LDSF(ST + lane * 4) = -__uint_as_float(sv & 0xFFFF0000u);
        LDSF(ST + 256 + lane * 4) = __builtin_amdgcn_rcpf(__uint_as_float(sv << 16));
    }
    LBAR();
    bf16x8 qf[2];
    {
        f32x4 cq[2], ck[2];
        float cv[2];
#pragma unroll
        for (int t = 0; t < 2; ++t) {
            cq[t] = lds_f4(L + 512 + t * 8192 + g * 16);
            ck[t] = lds_f4(L + 512 + (2 + t) * 8192 + g * 16);
            cv[t] = LDSF(L + 512 + (4 + t) * 8192 + tk * 4);
        }
        const bool pad = (lane >= 48);
        const unsigned kpad = ch == 0 ? 0xF149u : 0u;
#pragma unroll
        for (int i = 0; i < 4; ++i) {
            const float nm = LDSF(ST + i * 64 + tk * 4), rs = LDSF(ST + 256 + i * 64 + tk * 4);
            const int kt = (i >> 1) * 16 + 2 * w + (i & 1);
#define CORR(A, C) u32x4{pk2(fma2(sp2(nm), lo2(C[0]), lo2(A[0])) * sp2(rs)), pk2(fma2(sp2(nm), hi2(C[0]), hi2(A[0])) * sp2(rs)), \
                         pk2(fma2(sp2(nm), lo2(C[1]), lo2(A[1])) * sp2(rs)), pk2(fma2(sp2(nm), hi2(C[1]), hi2(A[1])) * sp2(rs))}
            u32x4 kv = CORR(aK[i], ck);
            kv[2] = pad ? (i < 2 ? kpad : 0u) : kv[2];
            kv[3] = pad ? 0u : kv[3];
            LDSV(u32x4, L + AT_K + kt * 1024 + lane * 16) = kv;
            if (i >= 2) {
                u32x4 qv = CORR(aQ[i - 2], cq);
                qv[2] = pad ? 0x3F80u : qv[2];
                qv[3] = pad ? 0u : qv[3];
                qf[i - 2] = __builtin_bit_cast(bf16x8, qv);
            }
        }
#pragma unroll
        for (int b = 0; b < 2; ++b) {
            f32x4 s0[2], s1[2];
#pragma unroll
            for (int u = 0; u < 2; ++u) {
                s0[u] = lds_f4(ST + (2 * b + u) * 64 + g * 16);
                s1[u] = lds_f4(ST + 256 + (2 * b + u) * 64 + g * 16);
            }
#pragma unroll
            for (int t = 0; t < 2; ++t) {
                u32x4 o;
#pragma unroll
                for (int u = 0; u < 2; ++u) {
                    o[2 * u] = pk2(fma2(lo2(s0[u]), sp2(cv[t]), lo2(aV[2 * b + u][t])) * lo2(s1[u]));
                    o[2 * u + 1] = pk2(fma2(hi2(s0[u]), sp2(cv[t]), hi2(aV[2 * b + u][t])) * hi2(s1[u]));
                }
                if (t == 0 && tk == 0) o = u32x4{0x3F803F80u, 0x3F803F80u, 0x3F803F80u, 0x3F803F80u};
                LDSV(u32x4, L + AT_V + ((b * 8 + w) * 2 + t) * 1024 + lane * 16) = o;
            }
        }
    }
    LBAR();
    STAMP(5)
    p2_lane(P);
    const float NEG = -1e30f;
    f32x4 mle, mgt;
#pragma unroll
    for (int r = 0; r < 4; ++r) { mle[r] = (4 * g + r <= tk) ? 0.f : NEG; mgt[r] = (4 * g + r > tk) ? 0.f : NEG; }
    f32x4 Sc[2][18];
#pragma unroll
    for (int j = 0; j < 18; ++j) {
        const bf16x8 kf = lds_b128(L + AT_K + (2 * w + j) * 1024 + lane * 16);
#pragma unroll
        for (int qt = 0; qt < 2; ++qt) {
            if (j >= qt && j <= qt + 16) {
                f32x4 c0;
                if (j == qt + 16) c0 = mle;
                else if (j == qt) c0 = mgt;
                else c0 = f32x4{0.f, 0.f, 0.f, 0.f};
                Sc[qt][j] = MFMA16(kf, qf[qt], c0);
            } else {
                Sc[qt][j] = f32x4{0.f, 0.f, 0.f, 0.f};
            }
        }
        if (j < 12) p2_dma_own(P, j >> 2, 2 * (j >> 2), j & 3);
    }
    const float CS = 0.18898223650461362f * 1.4426950408889634f;
    u32x4 pf[2][9];
#pragma unroll
    for (int qt = 0; qt < 2; ++qt) {
        float m = NEG;
#pragma unroll
        for (int j = qt; j <= qt + 16; ++j) m = fmaxf(fmaxf(m, fmaxf(Sc[qt][j][0], Sc[qt][j][1])), fmaxf(Sc[qt][j][2], Sc[qt][j][3]));
        m = xmax(m);
        const float mc = -m * CS;
#pragma unroll
        for (int j = qt; j <= qt + 16; ++j) {
            const f32x2 a = fma2(lo2(Sc[qt][j]), sp2(CS), sp2(mc)), b = fma2(hi2(Sc[qt][j]), sp2(CS), sp2(mc));
            Sc[qt][j] = f32x4{__builtin_amdgcn_exp2f(a[0]), __builtin_amdgcn_exp2f(a[1]), __builtin_amdgcn_exp2f(b[0]), __builtin_amdgcn_exp2f(b[1])};
            if (qt == 1 && j >= 2 && j < 14) {
                if (j < 10) p2_dma_own(P, 3 + ((j - 2) >> 2), 2 * ((j - 2) >> 2), (j - 2) & 3);
                else p2_dma(P, 9 + ((j - 10) >> 1), 4 + ((j - 10) >> 1), j & 1);
                SB();
            }
        }
#pragma unroll
        for (int b = 0; b < 9; ++b)
            pf[qt][b] = u32x4{pk2(Sc[qt][2 * b][0], Sc[qt][2 * b][1]), pk2(Sc[qt][2 * b][2], Sc[qt][2 * b][3]),
                              pk2(Sc[qt][2 * b + 1][0], Sc[qt][2 * b + 1][1]), pk2(Sc[qt][2 * b + 1][2], Sc[qt][2 * b + 1][3])};
        if (qt == 0) {
            SB();
            asm volatile("s_waitcnt vmcnt(0)" ::: "memory");
            p2_to_regs(P, L, wr[0], 0); p2_to_regs(P, L, wr[1], 2); p2_to_regs(P, L, wr[2], 4);
            WAITL0();
            SB();
        }
    }
    f32x4 O[2][2];
    O[0][0] = O[0][1] = O[1][0] = O[1][1] = f32x4{0.f, 0.f, 0.f, 0.f};
#pragma unroll
    for (int b = 0; b < 9; ++b)
#pragma unroll
        for (int t = 0; t < 2; ++t) {
            const bf16x8 vf = lds_b128(L + AT_V + ((w + b) * 2 + t) * 1024 + lane * 16);
#pragma unroll
            for (int qt = 0; qt < 2; ++qt) O[qt][t] = MFMA16(vf, __builtin_bit_cast(bf16x8, pf[qt][b]), O[qt][t]);
        }
    const __amdgpu_buffer_rsrc_t ra = __builtin_amdgcn_make_buffer_rsrc(ws, (short)0, ACT_BYTES, 0x00020000);
#pragma unroll
    for (int qt = 0; qt < 2; ++qt) {
        const float l = __shfl(O[qt][0][0], tk);
        const float inv = __builtin_amdgcn_rcpf(l);
        const unsigned dst = (ch * 16 + 2 * w + qt) * ACT_TILE_B + 224 * 256 + tk * 16;
#pragma unroll
        for (int t = 0; t < 2; ++t) {
            const int u = 7 * head + 4 * t + g - 1;
            if (t == 1 || g > 0)
                __builtin_amdgcn_raw_buffer_store_b64(
                    u32x2{pk2(O[qt][t][0] * inv, O[qt][t][1] * inv), pk2(O[qt][t][2] * inv, O[qt][t][3] * inv)}, ra,
                    dst + (u >> 1) * 256 + (u & 1) * 8, 0, 16);
        }
    }
    STAMP(6)
    WAIT_BAR0();
    if (tid == 0) __hip_atomic_store((gu64*)(ws + FLAG_OFF + (size_t)(ch * 32 + head) * 64), tag, RLX_AGENT);
    p2_to_regs(P, L, wr[3], 0); p2_to_regs(P, L, wr[4], 2);
    WAITL0();
#pragma unroll
    for (int k = 0; k < 10; ++k)
        if (k < 4 || k > 5) p2_dma2(P, 5 + k, k);
    STAMP(7)
    return tag;
}

constexpr int LDS_BYTES = 163840;

__global__ __launch_bounds__(512, 2) void k_fused(const char* x, const char* wqkv, const char* wo, const char* wg, const char* wu,
                                                  const char* wd, char* out, char* ws) {
    extern __shared__ __attribute__((aligned(16))) char smem[];
    lds_ptr L = (lds_ptr)smem;
    const unsigned lb = (unsigned)(size_t)L;
    STAMP_DECL STAMP(0)
    const int tid = threadIdx.x, lane = tid & 63;
    const int w = __builtin_amdgcn_readfirstlane(tid >> 6);
    const int xcd = blockIdx.x & 7, idx = blockIdx.x >> 3;
    const int rblk = 4 * (xcd & 1) + (idx & 3), cblk = 4 * (xcd >> 1) + ((idx >> 2) & 3);
    if (w == 7) {
        unsigned long long pc;
        asm volatile("s_getpc_b64 %0" : "=s"(pc));
        glds16s((const char*)(pc & ~255ull), idx * 1024 + lane * 16, lb + 162816);
        if (idx < 17) glds16s((const char*)(pc & ~255ull), 32768 + idx * 1024 + lane * 16, lb + 162816);
    }
    const int th = w & 1, kg = w >> 1;
    const bool has_p2 = idx < 28;
    const int rb = 4 * xcd + (idx & 3), cb = has_p2 ? (idx >> 2) : 0;
    const int a1 = kg ? 14 * (kg - 1) : 56;
    const int a2 = 42 + (kg < 2 ? 3 * kg : 4 * kg - 2);
    P2 P;
    P.n2 = kg < 2 ? 3 : 4;
    P.w1 = (kg ? wd + (size_t)(14 * (kg - 1)) * 32 * 896 : wo) + cb * 128 + th * 16 * 896;
    P.w2 = wd + (size_t)a2 * 32 * 896 + cb * 128 + th * 16 * 896;
    P.ring = lb + kg * 4096 + th * 2048;
    P.th = th; P.kg = kg;
    bf16x8 wr[5][4];
    unsigned long long tag;
    if (idx < 16) tag = att_block(x, wqkv, ws, rblk, cblk, L, P, wr STAMP_PASS);
    else tag = ffn_block(x, wg, wu, ws, rblk, cblk, L, P, wr STAMP_PASS);
    if (!has_p2) {
        asm volatile("s_waitcnt vmcnt(0)" ::: "memory");
        if (blockIdx.x == 255 && w == 0) {
#pragma unroll 1
            for (int i = 0; i < 4; ++i) wait_flags(ws, 64 * i, 64, lane, tag);
            if (lane == 0) __hip_atomic_store((gu64*)(ws + SYNC_OFF), tag, RLX_AGENT);
        }
        STAMP_FLUSH(ws, 0, 8)
        return;
    }
    int ln = lane;
    asm volatile("" : "+v"(ln));
    const int g = ln >> 4, tk = ln & 15;
    const size_t oo = (size_t)(rb * 64 + 32 * th + tk) * 896 + cb * 128 + kg * 32 + g * 8;
    const u32x2 xr0 = *(const u32x2*)(x + oo), xr1 = *(const u32x2*)(x + oo + 16 * 896);
    STAMP(8)
    if (w == 0) wait_flags(ws, (rb >> 2) * 32, 32, lane, tag);
    asm volatile("s_barrier" ::: "memory");
    STAMP(9)
    const __amdgpu_buffer_rsrc_t ra = __builtin_amdgcn_make_buffer_rsrc(ws, (short)0, ACT_BYTES, 0x00020000);
    const unsigned ab = (rb * 4 + 2 * th) * ACT_TILE_B + lane * 16;
    bf16x8 af[18][2];
#define ALD(u) { const int ks_ = (u) < 14 ? a1 + (u) : a2 + ((u) - 14 < P.n2 ? (u) - 14 : P.n2 - 1);                                      \
                 af[u][0] = __builtin_bit_cast(bf16x8, __builtin_amdgcn_raw_buffer_load_b128(ra, ab, ks_ * 1024, 16));                    \
                 af[u][1] = __builtin_bit_cast(bf16x8, __builtin_amdgcn_raw_buffer_load_b128(ra, ab + ACT_TILE_B, ks_ * 1024, 16)); SB(); }
#pragma unroll
    for (int u = 0; u < 5; ++u) ALD(u)
    f32x4 c1[2][4], c2[2][4];
#pragma unroll
    for (int i = 0; i < 2; ++i)
#pragma unroll
        for (int t = 0; t < 4; ++t) c1[i][t] = c2[i][t] = f32x4{0.f, 0.f, 0.f, 0.f};
#define MM8(ACC, WF, u) { _Pragma("unroll") for (int t = 0; t < 4; ++t) {            \
            ACC[0][t] = MFMA16(WF[t], af[u][0], ACC[0][t]);                          \
            ACC[1][t] = MFMA16(WF[t], af[u][1], ACC[1][t]); } }
#define STEP(ACC, u, SLOT) { bf16x8 wf[4]; _Pragma("unroll") for (int t = 0; t < 4; ++t) wf[t] = P2_WRD(t, SLOT); MM8(ACC, wf, u) }
    STAMP(10)
#pragma unroll
    for (int u = 0; u < 5; ++u) {
        MM8(c1, wr[u], u)
        SB();
        ALD(u + 5)
    }
    asm volatile("s_barrier" ::: "memory");
    STAMP(11)
#pragma unroll
    for (int u = 5; u < 8; ++u) { STEP(c1, u, u - 5) SB(); ALD(u + 5) }
    LBAR();
#pragma unroll
    for (int u = 8; u < 14; ++u) {
        STEP(c1, u, u - 5)
        SB();
        if (u < 13) ALD(u + 5)
        if (u < 11) p2_dma2(P, u + 7, u - 8);
    }
    STAMP(12)
    STEP(c2, 14, 9)
    SB();
    WAIT_BAR0();
    STAMP(13)
    STEP(c2, 15, 0) STEP(c2, 16, 1)
    if (P.n2 == 4) STEP(c2, 17, 2)
    if (kg == 0) {
#pragma unroll
        for (int i = 0; i < 2; ++i)
#pragma unroll
            for (int t = 0; t < 4; ++t) LDSV(f32x4, L + 131072 + th * 8192 + (i * 4 + t) * 1024 + lane * 16) = c1[i][t];
    } else {
#pragma unroll
        for (int i = 0; i < 2; ++i)
#pragma unroll
            for (int t = 0; t < 4; ++t) c2[i][t] += c1[i][t];
    }
    STAMP(14)
    f32x4 fs[2];
#define PUT(K)                                                                                             \
    {                                                                                                      \
        _Pragma("unroll") for (int i = 0; i < 2; ++i) {                                                    \
            _Pragma("unroll") for (int t = 0; t < 4; ++t)                                                  \
                if (t != K) LDSV(f32x4, L + 49152 + w * 8192 + (i * 4 + t) * 1024 + lane * 16) = c2[i][t]; \
            fs[i] = c2[i][K];                                                                              \
        }                                                                                                  \
    }
    if (kg == 0) PUT(0) else if (kg == 1) PUT(1) else if (kg == 2) PUT(2) else PUT(3)
    LBAR();
#pragma unroll
    for (int i = 0; i < 2; ++i) {
#pragma unroll
        for (int k = 1; k < 4; ++k) fs[i] += lds_f4(L + 49152 + (2 * ((kg + k) & 3) + th) * 8192 + (i * 4 + kg) * 1024 + lane * 16);
        const f32x4 sv = lds_f4(L + 131072 + th * 8192 + (i * 4 + kg) * 1024 + lane * 16);
        const u32x2 xr = i ? xr1 : xr0;
        float o[4];
#pragma unroll
        for (int r = 0; r < 4; ++r) {
            const float fb = rbf(fs[i][r]);
            const float gb = rbf(fb * __builtin_amdgcn_rcpf(1.0f + __builtin_amdgcn_exp2f(-fb * 1.4426950408889634f)));
            const float pb = rbf(rbf(sv[r]) * gb);
            const unsigned xw = xr[r >> 1];
            const float xv = __uint_as_float((r & 1) ? (xw & 0xFFFF0000u) : (xw << 16));
            o[r] = xv + pb;
        }
        *(u32x2*)(out + oo + (size_t)i * 16 * 896) = u32x2{pk2(o[0], o[1]), pk2(o[2], o[3])};
    }
    STAMP(15)
    STAMP_FLUSH(ws, 0, 16)
}

extern "C" void kernel_launch(void* const* d_in, const int* in_sizes, int n_in, void* d_out, int out_size, void* d_ws,
                              size_t ws_size, hipStream_t stream) {
    (void)hipFuncSetAttribute((const void*)k_fused, hipFuncAttributeMaxDynamicSharedMemorySize, LDS_BYTES);
    k_fused<<<256, 512, LDS_BYTES, stream>>>((const char*)d_in[0], (const char*)d_in[1], (const char*)d_in[2], (const char*)d_in[4],
                                             (const char*)d_in[5], (const char*)d_in[6], (char*)d_out, (char*)d_ws);
}
```

```cpp
#include <hip/hip_runtime.h>
#include <hip/hip_bf16.h>

typedef short bf16x8 __attribute__((ext_vector_type(8)));
typedef short s16x4 __attribute__((ext_vector_type(4)));
typedef float f32x4 __attribute__((ext_vector_type(4)));
typedef float f32x2 __attribute__((ext_vector_type(2)));
typedef unsigned u32x4 __attribute__((ext_vector_type(4)));
typedef unsigned u32x2 __attribute__((ext_vector_type(2)));
typedef __bf16 bf16x2_t __attribute__((ext_vector_type(2)));
typedef __attribute__((address_space(3))) char* lds_ptr;

#define MFMA16(a, b, c) __builtin_amdgcn_mfma_f32_16x16x32_bf16(a, b, c, 0, 0, 0)
#define WAIT_BAR0() asm volatile("s_waitcnt vmcnt(0) lgkmcnt(0)\n\ts_barrier" ::: "memory")
#define LBAR() asm volatile("s_waitcnt lgkmcnt(0)\n\ts_barrier" ::: "memory")

#define STAMP_ARG
#define STAMP_PASS
#define STAMP_DECL
#define STAMP(i)
#define STAMP_FLUSH(actp, lo, hi)
constexpr int S_ = 2048, D_ = 448, F_ = 1792, QKV_ = 1344;
constexpr int ACT_OCT = 280;
constexpr int ACT_TILE_B = ACT_OCT * 256 + 256;

__device__ __forceinline__ void glds16(const void* g, unsigned lds_dst) {
    unsigned sv;
    asm volatile("s_mov_b32 %0, m0\n\ts_mov_b32 m0, %2\n\ts_nop 0\n\tglobal_load_lds_dwordx4 %1, off\n\ts_mov_b32 m0, %0"
                 : "=&s"(sv) : "v"(g), "s"(lds_dst) : "memory");
}
__device__ __forceinline__ void glds16s(const void* sbase, unsigned voff, unsigned lds_dst) {
    unsigned sv;
    asm volatile("s_mov_b32 %0, m0\n\ts_mov_b32 m0, %3\n\ts_nop 0\n\tglobal_load_lds_dwordx4 %1, %2\n\ts_mov_b32 m0, %0"
                 : "=&s"(sv) : "v"(voff), "s"(sbase), "s"(lds_dst) : "memory");
}
__device__ __forceinline__ bf16x8 lds_b128(lds_ptr p) { return *(const __attribute__((address_space(3))) bf16x8*)p; }
__device__ __forceinline__ f32x4 lds_f4(lds_ptr p) { return *(const __attribute__((address_space(3))) f32x4*)p; }
__device__ __forceinline__ s16x4 lds_tr(lds_ptr p) {
    return __builtin_amdgcn_ds_read_tr16_b64_v4i16((__attribute__((address_space(3))) s16x4*)p);
}
__device__ __forceinline__ bf16x8 lds_tr2(lds_ptr p, int second) {
    s16x4 a = lds_tr(p), b = lds_tr(p + second);
    return __builtin_shufflevector(a, b, 0, 1, 2, 3, 4, 5, 6, 7);
}
__device__ __forceinline__ float dot2(unsigned a, unsigned b, float c) {
    return __builtin_amdgcn_fdot2_f32_bf16(__builtin_bit_cast(bf16x2_t, a), __builtin_bit_cast(bf16x2_t, b), c, false);
}
__device__ __forceinline__ float frag_sum(bf16x8 f, float c) {
    u32x4 u = __builtin_bit_cast(u32x4, f);
    c = dot2(u[0], 0x3F803F80u, c); c = dot2(u[1], 0x3F803F80u, c);
    c = dot2(u[2], 0x3F803F80u, c); c = dot2(u[3], 0x3F803F80u, c);
    return c;
}
__device__ __forceinline__ float frag_sq(bf16x8 f, float c) {
    u32x4 u = __builtin_bit_cast(u32x4, f);
    c = dot2(u[0], u[0], c); c = dot2(u[1], u[1], c); c = dot2(u[2], u[2], c); c = dot2(u[3], u[3], c);
    return c;
}
__device__ __forceinline__ float xsum(float x) {
    auto s = __builtin_amdgcn_permlane16_swap(__float_as_uint(x), __float_as_uint(x), false, false);
    x = __uint_as_float(s[0]) + __uint_as_float(s[1]);
    auto t = __builtin_amdgcn_permlane32_swap(__float_as_uint(x), __float_as_uint(x), false, false);
    return __uint_as_float(t[0]) + __uint_as_float(t[1]);
}
__device__ __forceinline__ float xmax(float x) {
    auto s = __builtin_amdgcn_permlane16_swap(__float_as_uint(x), __float_as_uint(x), false, false);
    x = fmaxf(__uint_as_float(s[0]), __uint_as_float(s[1]));
    auto t = __builtin_amdgcn_permlane32_swap(__float_as_uint(x), __float_as_uint(x), false, false);
    return fmaxf(__uint_as_float(t[0]), __uint_as_float(t[1]));
}
__device__ __forceinline__ unsigned pk2(float lo, float hi) {
    bf16x2_t v; v[0] = (__bf16)lo; v[1] = (__bf16)hi;
    return __builtin_bit_cast(unsigned, v);
}
__device__ __forceinline__ unsigned pk2(f32x2 v) { return pk2(v[0], v[1]); }
__device__ __forceinline__ f32x2 lo2(f32x4 v) { return __builtin_shufflevector(v, v, 0, 1); }
__device__ __forceinline__ f32x2 hi2(f32x4 v) { return __builtin_shufflevector(v, v, 2, 3); }
__device__ __forceinline__ f32x2 sp2(float v) { return f32x2{v, v}; }
__device__ __forceinline__ f32x2 fma2(f32x2 a, f32x2 b, f32x2 c) { return __builtin_elementwise_fma(a, b, c); }
__device__ __forceinline__ float rbf(float x) { return (float)(__bf16)x; }
__device__ __forceinline__ void ln_stats(float sum, float sq, float& mean, float& den) {
    float m = sum * (1.0f / D_);
    float v = fmaxf(sq * (1.0f / D_) - m * m, 0.0f);
    mean = rbf(m);
    den = rbf(sqrtf(rbf(rbf(v) + 1e-5f)));
}

typedef __attribute__((address_space(1))) unsigned long long gu64;
#define RLX_AGENT __ATOMIC_RELAXED, __HIP_MEMORY_SCOPE_AGENT
#define SB() __builtin_amdgcn_sched_barrier(0)
#define LDSF(p) (*(__attribute__((address_space(3))) float*)(p))
#define LDSV(T, p) (*(__attribute__((address_space(3))) T*)(p))
constexpr size_t SYNC_OFF = 16u << 20, FLAG_OFF = SYNC_OFF + 4096, STAT_OFF = SYNC_OFF + 65536, KV_OFF = 20u << 20;
constexpr int ACT_BYTES = 128 * ACT_TILE_B;
constexpr unsigned long long SPIN_TICKS = 20000000ull;

template <int N> __device__ __forceinline__ float ror_add(float x) {
    return x + __uint_as_float(__builtin_amdgcn_update_dpp(0u, __float_as_uint(x), 0x120 + N, 0xF, 0xF, false));
}
__device__ __forceinline__ float wave_sum(float x) { return xsum(ror_add<1>(ror_add<2>(ror_add<4>(ror_add<8>(x))))); }

__device__ __forceinline__ void start_loads(const char* x, char* ws, int row, int lane, unsigned long long& ep, u32x4& rv) {
    const unsigned zero = 0, ro = (lane < 56 ? lane : 55) * 16;
    asm volatile("global_load_dwordx2 %0, %1, %2 sc1" : "=v"(ep) : "v"(zero), "s"(ws + SYNC_OFF) : "memory");
    asm volatile("global_load_dwordx4 %0, %1, %2" : "=v"(rv) : "v"(ro), "s"(x + (size_t)row * 896) : "memory");
}
__device__ __forceinline__ unsigned long long publish_stats(char* ws, int row, int lane, unsigned long long ep, u32x4 rv) {
    const unsigned long long tag = (((unsigned long long)__builtin_amdgcn_readfirstlane((unsigned)(ep >> 32)) << 32) |
                                    (unsigned)__builtin_amdgcn_readfirstlane((unsigned)ep)) + 1ull;
    if (lane >= 56) rv = u32x4{0u, 0u, 0u, 0u};
    const bf16x8 f = __builtin_bit_cast(bf16x8, rv);
    float mean, den;
    ln_stats(wave_sum(frag_sum(f, 0.f)), wave_sum(frag_sq(f, 0.f)), mean, den);
    const unsigned val = (__float_as_uint(mean) & 0xFFFF0000u) | (__float_as_uint(den) >> 16);
    if (lane == 0) __hip_atomic_store((gu64*)(ws + STAT_OFF) + row, ((unsigned long long)(unsigned)tag << 32) | val, RLX_AGENT);
    return tag;
}
__device__ __forceinline__ void stats_request(char* ws, int row, unsigned long long& v) {
    asm volatile("global_load_dwordx2 %0, %1, %2 sc1" : "=v"(v) : "v"(row * 8), "s"(ws + STAT_OFF) : "memory");
}
__device__ __forceinline__ unsigned stats_check(char* ws, int row, unsigned long long tag, unsigned long long v) {
    const unsigned long long t0 = __builtin_amdgcn_s_memrealtime();
    while (!__all((unsigned)(v >> 32) == (unsigned)tag)) {
        if (__builtin_amdgcn_s_memrealtime() - t0 > SPIN_TICKS) break;
        __builtin_amdgcn_s_sleep(2);
        v = __hip_atomic_load((gu64*)(ws + STAT_OFF) + row, RLX_AGENT);
    }
    return (unsigned)v;
}
__device__ __forceinline__ void wait_flags(char* ws, int first, int n, int lane, unsigned long long tag) {
    const unsigned long long t0 = __builtin_amdgcn_s_memrealtime();
    const int i = first + (lane < n ? lane : n - 1);
    for (;;) {
        const unsigned long long v = __hip_atomic_load((gu64*)(ws + FLAG_OFF + (size_t)i * 64), RLX_AGENT);
        if (__all(v == tag)) break;
        if (__builtin_amdgcn_s_memrealtime() - t0 > SPIN_TICKS) break;
        __builtin_amdgcn_s_sleep(1);
    }
}

struct P2 {
    const char* wd; const char* wo;
    const char* w1; const char* w2; unsigned ring; int n2, th, kg;
    unsigned wv0; int wa[4], own;
};
__device__ __forceinline__ void p2_lane(P2& P) {
    int lane = threadIdx.x & 63;
    asm volatile("" : "+v"(lane));
    const int g = lane >> 4, q = (lane >> 2) & 3, p = lane & 3, f = ((q >> 1) & 1) | ((g & 1) << 1);
    P.wv0 = (lane >> 3) * 896 + (((lane & 7) ^ (((lane >> 4) & 1) << 1)) << 4);
#pragma unroll
    for (int t = 0; t < 4; ++t) P.wa[t] = P.kg * 4096 + (8 * g + q) * 128 + ((t ^ f) << 5) + p * 8;
    P.own = P.th * 2048 + (g >> 1) * (16384 - 2048);
}
__device__ __forceinline__ void p2_dma(const P2& P, int u, int slot, int half) {
    const char* src = u < 14 ? P.w1 + u * (32 * 896) : P.w2 + (u - 14 < P.n2 ? u - 14 : P.n2 - 1) * (32 * 896);
    glds16s(src + half * (8 * 896), half ? (P.wv0 ^ 64) : P.wv0, P.ring + slot * 16384 + half * 1024);
}
__device__ __forceinline__ void p2_dma2(const P2& P, int u, int slot) { p2_dma(P, u, slot, 0); p2_dma(P, u, slot, 1); }
#define P2_WRD(t, SLOT) lds_tr2(L + P.wa[t] + ((SLOT) >> 2) * 65536 + ((SLOT) & 3) * 16384, 512)
__device__ __forceinline__ void p2_dma_own(const P2& P, int u, int sl, int j) {
    glds16s(P.w1 + (u * 32 + j * 8 - P.th * 16) * 896, (j & 1) ? (P.wv0 ^ 64) : P.wv0, P.ring + (sl + (j >> 1)) * 16384 + (j & 1) * 1024);
}
__device__ __forceinline__ void p2_dma_own4(const P2& P, int u, int sl) {
    p2_dma_own(P, u, sl, 0); p2_dma_own(P, u, sl, 1); p2_dma_own(P, u, sl, 2); p2_dma_own(P, u, sl, 3);
}
__device__ __forceinline__ void p2_to_regs(const P2& P, lds_ptr L, bf16x8 (&wr)[4], int sl) {
#pragma unroll
    for (int t = 0; t < 4; ++t) wr[t] = lds_tr2(L + P.wa[t] + P.own + (sl >> 2) * 65536 + (sl & 3) * 16384, 512);
}
__device__ __forceinline__ void p2_touch(const P2& P, int w, int lane, unsigned lb) {
    const unsigned o1 = blockIdx.x * 6272 + w * 1024 + lane * 16, o2 = blockIdx.x * 1568 + (w & 1) * 1024 + lane * 16;
    if (w < 7) glds16s(P.wd, o1 < F_ * 896 - 16 ? o1 : F_ * 896 - 16, lb + 162816);
    if (w >= 6) glds16s(P.wo, o2 < D_ * 896 - 16 ? o2 : D_ * 896 - 16, lb + 162816);
}
#define WAITL0() asm volatile("s_waitcnt lgkmcnt(0)" ::: "memory")

constexpr int FF_XS = 32768, FF_UOFF = 15376, FF_WS = 2 * FF_UOFF, FF_W0 = 2 * FF_XS, FF_CS = 163840 - 1024;

__device__ __forceinline__ unsigned long long ffn_block(const char* x, const char* wg, const char* wu, char* ws, int rb, int cb,
                                                        lds_ptr L, P2& P, bf16x8 (&wr)[5][4] STAMP_ARG) {
    const int tid = threadIdx.x, lane = tid & 63;
    const int w = __builtin_amdgcn_readfirstlane(tid >> 6);
    const int wm = w & 3, wn = w >> 2;
    const unsigned lb = (unsigned)(size_t)L;
    unsigned long long ep; u32x4 rv;
    const int srow = rb * 256 + (16 + cb) * 8 + w;
    start_loads(x, ws, srow, lane, ep, rv);
    const unsigned xo0 = (lane >> 3) * 896 + (((lane & 7) ^ (lane >> 4)) << 4), xo1 = xo0 ^ 64;
    const char* xb = x + (size_t)(rb * 256 + 64 * wm) * 896;
    auto dma_x = [&](int c, int j) { glds16s(xb + j * (8 * 896) + c * 128, (j & 1) ? xo1 : xo0, lb + (c & 1) * FF_XS + (64 * wm + 8 * j) * 128); };
    unsigned woff[8];
    auto dma_w1 = [&](int c, int buf, int j) {
        const int n = (j == 7 && wm > 1) ? 29 : wm + 4 * j, mat = n >= 15, nn = n - 15 * mat;
        glds16s((mat ? wu : wg) + cb * 224 + (size_t)c * (64 * F_ * 2), woff[j], lb + FF_W0 + mat * FF_UOFF + nn * 1024 + buf);
    };
    if (wn == 0) {
#pragma unroll
        for (int j = 0; j < 8; ++j) dma_x(0, j);
#pragma unroll
        for (int j = 0; j < 3; ++j) dma_x(1, j);
    }
#pragma unroll
    for (int j = 0; j < 8; ++j) {
        int n = wm + 4 * j; if (n > 29) n = 29;
        const int nn = n >= 15 ? n - 15 : n;
        const int ci = nn * 64 + lane, grp = ci / 120;
        int rem = ci - grp * 120; if (rem >= 112) rem -= 8;
        const int r = rem / 14, chn = rem - r * 14;
        woff[j] = (8 * grp + r) * (F_ * 2) + chn * 16;
        if (wn == 1) dma_w1(0, 0, j);
    }
    if (wn == 1) {
#pragma unroll
        for (int j = 0; j < 8; ++j) dma_w1(1, FF_WS, j);
    }
    p2_touch(P, w, lane, lb);
    asm volatile("s_waitcnt vmcnt(12)" : "+v"(ep), "+v"(rv) :: "memory");
    const unsigned long long tag = publish_stats(ws, srow, lane, ep, rv);
    const int g = lane >> 4, q = (lane >> 2) & 3, p = lane & 3, tk = lane & 15;
    const int xa = (64 * wm + tk) * 128 + ((g ^ ((lane >> 1) & 7)) << 4);
    const int wa = FF_W0 + g * 1920 + q * 224 + (p & 1) * 8 + (p >> 1) * FF_UOFF + wn * 112;
    const int tc1 = (wm + 4 > 6) ? 6 : wm + 4;
    f32x4 acc[4][7];
#pragma unroll
    for (int i = 0; i < 4; ++i)
#pragma unroll
        for (int t = 0; t < 7; ++t) acc[i][t] = f32x4{0.f, 0.f, 0.f, 0.f};
    f32x4 cs0 = f32x4{0.f, 0.f, 0.f, 0.f}, cs1 = cs0;
    const bf16x8 ones = __builtin_bit_cast(bf16x8, u32x4{0x3F803F80u, 0x3F803F80u, 0x3F803F80u, 0x3F803F80u});
    bf16x8 xf[2][4], wf[4], cf;
    if (wn == 0) asm volatile("s_waitcnt vmcnt(5)\n\ts_barrier" ::: "memory");
    else asm volatile("s_waitcnt vmcnt(10)\n\ts_barrier" ::: "memory");
    STAMP(1)
#define XRD(B, ks, i) xf[ks][i] = lds_b128((B) + (xa ^ ((ks) * 64)) + (i) * 2048)
#define WRD(B, s) lds_tr2((B) + ((s) / 7) * 7680 + ((s) % 7) * 16, 896)
    {
        lds_ptr W = L + wa;
        XRD(L, 0, 0); XRD(L, 0, 1); XRD(L, 0, 2); XRD(L, 0, 3);
        wf[0] = WRD(W, 0); wf[1] = WRD(W, 1); wf[2] = WRD(W, 2);
    }
    unsigned long long sreq;
    int wb = 0;
#define FCHUNK(c, ROLE, MODE)                                                                                          \
    {                                                                                                                  \
        const int wb1 = (wb == 2 * FF_WS) ? 0 : wb + FF_WS, wb2 = (wb == 0) ? 2 * FF_WS : wb - FF_WS;                  \
        lds_ptr X = L + ((c) & 1) * FF_XS, Xn = L + (((c) + 1) & 1) * FF_XS, W = L + wa + wb, Wn = L + wa + wb1;       \
        _Pragma("unroll") for (int s = 0; s < 14; ++s) {                                                               \
            const int ks = s / 7, t = s % 7;                                                                           \
            if (s == 11 && MODE < 2) {                                                                                 \
                if (ROLE == 1 && MODE == 0) asm volatile("s_waitcnt vmcnt(8) lgkmcnt(0)\n\ts_barrier" ::: "memory");  \
                else WAIT_BAR0();                                                                                      \
            }                                                                                                          \
            if (s < 11) wf[(s + 3) % 4] = WRD(W, s + 3); else if (MODE < 2) wf[(s + 3) % 4] = WRD(Wn, s - 11);        \
            if (s < 4) XRD(X, 1, s);                                                                                   \
            if (s == 11 && MODE < 2) { XRD(Xn, 0, 0); XRD(Xn, 0, 1); }                                                 \
            if (s == 12 && MODE < 2) { XRD(Xn, 0, 2); XRD(Xn, 0, 3); }                                                 \
            if (s == 4 || s == 6 || s == 8 || s == 10) cf = lds_tr2(W + (s >= 8) * 7680 + ((s == 4 || s == 8) ? wm : tc1) * 16, 896); \
            if (ROLE == 0 && MODE < 2 && s < 5) dma_x((c) + 1, 3 + s);                                                 \
            if (ROLE > 0 && MODE == 0 && s >= 3 && s < 11) dma_w1((c) + 2, wb2, s - 3);                         \
            if (ROLE == 0 && MODE == 0 && s >= 11) dma_x((c) + 2, s - 11);                                             \
            if (MODE == 3 && s >= 2 && s < 10) p2_dma_own(P, (s - 2) >> 2, s < 6 ? 2 : 6, (s - 2) & 3);                \
            _Pragma("unroll") for (int i = 0; i < 4; ++i) acc[i][t] = MFMA16(wf[s % 4], xf[ks][i], acc[i][t]);        \
            if (s == 5 || s == 9) cs0 = MFMA16(cf, ones, cs0);                                                         \
            if (s == 7 || s == 12) cs1 = MFMA16(cf, ones, cs1);                                                        \
            SB();                                                                                                      \
        }                                                                                                              \
        { bf16x8 a_ = wf[2], b_ = wf[3], d_ = wf[0]; wf[0] = a_; wf[1] = b_; wf[2] = d_; }                             \
        wb = wb1;                                                                                                      \
    }
    if (wn == 0) {
#pragma unroll 1
        for (int c = 0; c < 5; ++c) FCHUNK(c, 0, 0)
        stats_request(ws, rb * 256 + 64 * wm + lane, sreq);
        FCHUNK(5, 0, 1)
    } else {
#pragma unroll 1
        for (int c = 0; c < 5; ++c) FCHUNK(c, 1, 0)
        stats_request(ws, rb * 256 + 64 * wm + lane, sreq);
        FCHUNK(5, 1, 1)
    }
    STAMP(3)
    p2_lane(P);
    FCHUNK(6, 0, 3)
    STAMP(4)
    if (tk == 0) {
        LDSV(f32x4, L + FF_CS + (7 * wn + wm) * 64 + g * 16) = cs0;
        if (wm < 3) LDSV(f32x4, L + FF_CS + (7 * wn + wm + 4) * 64 + g * 16) = cs1;
    }
    asm volatile("" : "+v"(sreq));
    const unsigned sv = stats_check(ws, rb * 256 + 64 * wm + lane, tag, sreq);
    LBAR();
    STAMP(5)
    const __amdgpu_buffer_rsrc_t ra = __builtin_amdgcn_make_buffer_rsrc(ws, (short)0, ACT_BYTES, 0x00020000);
    f32x4 csr[7];
#pragma unroll
    for (int t = 0; t < 7; ++t) csr[t] = lds_f4(L + FF_CS + (7 * wn + t) * 64 + g * 16);
    LBAR();
    const float K1 = -2.3022081986f, K2 = -0.1029432398f;
#pragma unroll
    for (int ip = 0; ip < 2; ++ip) {
        const unsigned sa = __shfl(sv, 32 * ip + tk), sb = __shfl(sv, 32 * ip + 16 + tk);
        const float ma = -__uint_as_float(sa & 0xFFFF0000u), mb = -__uint_as_float(sb & 0xFFFF0000u);
        const float dn = __uint_as_float((lane < 32 ? sa : sb) << 16);
        const float a = __builtin_amdgcn_rcpf(dn), ia2 = dn * dn, c1 = K1 * a, c2 = K2 * a * a * a;
        const unsigned dst = (rb * 16 + wm * 4 + 2 * ip + (lane >> 5)) * ACT_TILE_B + (cb * 14 + wn * 7) * 256 + tk * 16;
        u32x2 hp[7];
#pragma unroll
        for (int t = 0; t < 7; ++t) {
            const f32x4 cs = csr[t];
#pragma unroll
            for (int u = 0; u < 2; ++u) {
                const f32x2 c2v = u ? hi2(cs) : lo2(cs);
                const f32x2 va = fma2(sp2(ma), c2v, u ? hi2(acc[2 * ip][t]) : lo2(acc[2 * ip][t]));
                const f32x2 vb = fma2(sp2(mb), c2v, u ? hi2(acc[2 * ip + 1][t]) : lo2(acc[2 * ip + 1][t]));
                auto s0 = __builtin_amdgcn_permlane32_swap(__float_as_uint(va[0]), __float_as_uint(vb[0]), false, false);
                auto s1 = __builtin_amdgcn_permlane32_swap(__float_as_uint(va[1]), __float_as_uint(vb[1]), false, false);
                const f32x2 G = f32x2{__uint_as_float(s0[0]), __uint_as_float(s1[0])}, U = f32x2{__uint_as_float(s0[1]), __uint_as_float(s1[1])};
                const f32x2 z = G * fma2(G * G, sp2(c2), sp2(c1));
                const f32x2 e = f32x2{__builtin_amdgcn_exp2f(z[0]), __builtin_amdgcn_exp2f(z[1])};
                const f32x2 d = fma2(e, sp2(ia2), sp2(ia2));
                hp[t][u] = pk2((G * U) * f32x2{__builtin_amdgcn_rcpf(d[0]), __builtin_amdgcn_rcpf(d[1])});
            }
            if (t & 1) {
                auto s0 = __builtin_amdgcn_permlane16_swap(hp[t - 1][0], hp[t][0], false, false);
                auto s1 = __builtin_amdgcn_permlane16_swap(hp[t - 1][1], hp[t][1], false, false);
                __builtin_amdgcn_raw_buffer_store_b128(u32x4{s0[0], s1[0], s0[1], s1[1]}, ra, dst + (g & 1) * 256, (t - 1) * 256, 16);
            }
            if (t == 6) __builtin_amdgcn_raw_buffer_store_b64(hp[6], ra, dst + (g & 1) * 8, 6 * 256, 16);
            {
                if (ip == 0) {
                    if (t < 6) { p2_dma_own(P, 2 + (t >> 1), 4 * (t >> 1), 2 * (t & 1)); p2_dma_own(P, 2 + (t >> 1), 4 * (t >> 1), 2 * (t & 1) + 1); }
                } else if (t < 5) p2_dma2(P, 5 + t, t);
            }
            SB();
        }
        if (ip == 0) {
            asm volatile("s_waitcnt vmcnt(1)" ::: "memory");
            p2_to_regs(P, L, wr[0], 2); p2_to_regs(P, L, wr[1], 6); p2_to_regs(P, L, wr[2], 0); p2_to_regs(P, L, wr[3], 4); p2_to_regs(P, L, wr[4], 8);
            WAITL0();
            SB();
        }
    }
    STAMP(6)
    WAIT_BAR0();
    if (tid == 0) __hip_atomic_store((gu64*)(ws + FLAG_OFF + (size_t)(rb * 32 + 16 + cb) * 64), tag, RLX_AGENT);
#pragma unroll
    for (int k = 5; k < 10; ++k) p2_dma2(P, 5 + k, k);
    STAMP(7)
    return tag;
}

constexpr int AT_WM = 4096, AT_WS = 3 * AT_WM, AT_W = 131072;
constexpr int AT_K = 98304, AT_V = AT_K + 32768;

__device__ __forceinline__ unsigned long long att_block(const char* x, const char* wqkv, char* ws, int ch, int head, lds_ptr L,
                                                        P2& P, bf16x8 (&wr)[5][4] STAMP_ARG) {
    const int tid = threadIdx.x, lane = tid & 63;
    const int w = __builtin_amdgcn_readfirstlane(tid >> 6);
    const unsigned lb = (unsigned)(size_t)L;
    const int g = lane >> 4, q = (lane >> 2) & 3, p = lane & 3, tk = lane & 15;
    unsigned long long ep; u32x4 rv;
    const int srow = ch * 256 + head * 8 + w;
    start_loads(x, ws, srow, lane, ep, rv);
    const unsigned wo = (lane >> 2) * (QKV_ * 2) + (((lane & 3) ^ (((lane >> 5) & 1) << 1)) << 4);
    const char* wsrc0 = wqkv + (w & 3) * (16 * QKV_ * 2) + (w >> 2) * 896 + head * 56;
    const char* wsrc1 = wqkv + (w & 3) * (16 * QKV_ * 2) + 2 * 896 + head * 56 - 8;
    auto dma_w = [&](int c) {
        glds16s(wsrc0 + c * (64 * QKV_ * 2), wo, lb + AT_W + (c & 1) * AT_WS + w * 1024);
        if (w < 4) glds16s(wsrc1 + c * (64 * QKV_ * 2), wo, lb + AT_W + (c & 1) * AT_WS + 8192 + w * 1024);
    };
    const int orow = ch * 256 + 32 * w;
    const unsigned xo0 = (lane >> 3) * 896 + (((lane & 7) ^ (lane >> 4)) << 4), xo1 = xo0 ^ 64;
    const char* xs = x + (size_t)orow * 896;
    auto dma_x = [&](int c, int i) {
        const char* src = xs + i * (16 * 896) + c * 128;
        glds16s(src, xo0, lb + (c & 3) * 32768 + w * 4096 + i * 2048);
        glds16s(src + 8 * 896, xo1, lb + (c & 3) * 32768 + w * 4096 + i * 2048 + 1024);
    };
    dma_w(0); dma_x(0, 0); dma_x(0, 1);
    dma_w(1); dma_x(1, 0); dma_x(1, 1); dma_x(2, 0); dma_x(2, 1);
    p2_touch(P, w, lane, lb);
    asm volatile("s_waitcnt vmcnt(15)" : "+v"(ep), "+v"(rv) :: "memory");
    const unsigned long long tag = publish_stats(ws, srow, lane, ep, rv);
    const int xa0 = w * 4096 + tk * 128 + ((g ^ ((lane >> 1) & 7)) << 4), xa1 = xa0 ^ 64;
    const int wA0 = AT_W + (8 * g + q) * 64 + ((g & 1) << 5) + p * 8, wA1 = wA0 ^ 32;
    const int cidx = w > 5 ? 5 : w;
    const int wC = (cidx >> 1) * AT_WM + (wA0 ^ ((cidx & 1) << 5));
    f32x4 aK[2][2], aV[2][2], aQ[2][2], cs = f32x4{0.f, 0.f, 0.f, 0.f};
#pragma unroll
    for (int i = 0; i < 2; ++i) aK[i][0] = aK[i][1] = aV[i][0] = aV[i][1] = aQ[i][0] = aQ[i][1] = f32x4{0.f, 0.f, 0.f, 0.f};
    const bf16x8 ones = __builtin_bit_cast(bf16x8, u32x4{0x3F803F80u, 0x3F803F80u, 0x3F803F80u, 0x3F803F80u});
    bf16x8 wf[2][3][2], cf[2], xf[2][2];
#define AWRD(B, ks) { _Pragma("unroll") for (int m = 0; m < 3; ++m) { wf[ks][m][0] = lds_tr2((B) + wA0 + m * AT_WM + (ks) * 2048, 256); \
                      wf[ks][m][1] = lds_tr2((B) + wA1 + m * AT_WM + (ks) * 2048, 256); } cf[ks] = lds_tr2((B) + wC + (ks) * 2048, 256); }
#define AXRD(c, i) { xf[i][0] = lds_b128(L + xa0 + ((c) & 3) * 32768 + (i) * 2048); xf[i][1] = lds_b128(L + xa1 + ((c) & 3) * 32768 + (i) * 2048); }
#define AMM(i, ks) { _Pragma("unroll") for (int t = 0; t < 2; ++t) { \
                       aK[i][t] = MFMA16(wf[ks][1][t], xf[i][ks], aK[i][t]); aV[i][t] = MFMA16(xf[i][ks], wf[ks][2][t], aV[i][t]); \
                       aQ[i][t] = MFMA16(wf[ks][0][t], xf[i][ks], aQ[i][t]); } }
    asm volatile("s_waitcnt vmcnt(11)\n\ts_barrier" ::: "memory");
    STAMP(1)
    AXRD(0, 0) AWRD(L, 0) AWRD(L, 1)
    SB();
    const int strow = orow + (lane & 31);
    unsigned long long sreq;
#pragma unroll
    for (int c = 0; c < 7; ++c) {
        AXRD(c, 1)
        AMM(0, 0)
        cs = MFMA16(cf[0], ones, cs);
        AMM(0, 1)
        cs = MFMA16(cf[1], ones, cs);
        SB();
        if (c + 3 < 7) dma_x(c + 3, 0);
        if (c == 4) stats_request(ws, strow, sreq);
        if (c < 6) {
            asm volatile("s_waitcnt vmcnt(%0)\n\ts_barrier" :: "i"(c == 0 ? 8 : c < 4 ? 4 : c == 4 ? 3 : 0) : "memory");
            AXRD(c + 1, 0)
            if (c + 2 < 7) dma_w(c + 2);
        }
        AMM(1, 0)
        SB();
        if (c < 6) AWRD(L + ((c + 1) & 1) * AT_WS, 0)
        AMM(1, 1)
        SB();
        if (c < 6) AWRD(L + ((c + 1) & 1) * AT_WS, 1)
        if (c + 3 < 7) dma_x(c + 3, 1);
        SB();
    }
    asm volatile("" : "+v"(sreq));
    STAMP(4)
    lds_ptr ST = L + w * 4096;
    if (tk == 0) LDSV(f32x4, ST + 512 + g * 16) = cs;
    {
        const unsigned sv = stats_check(ws, strow, tag, sreq);
        LDSF(ST + lane * 4) = -__uint_as_float(sv & 0xFFFF0000u);
        LDSF(ST + 256 + lane * 4) = __builtin_amdgcn_rcpf(__uint_as_float(sv << 16));
    }
    LBAR();
    bf16x8 qf[2];
    const bool pad = (lane >= 48);
    const __amdgpu_buffer_rsrc_t rk = __builtin_amdgcn_make_buffer_rsrc(ws + KV_OFF, (short)0, 128 * 32768, 0x00020000);
    {
        const unsigned kvo = (ch * 16 + head) * 32768 + w * 2048 + lane * 16;
        f32x4 cq[2], ck[2];
        float cv[2];
#pragma unroll
        for (int t = 0; t < 2; ++t) {
            cq[t] = lds_f4(L + 512 + t * 4096 + g * 16);
            ck[t] = lds_f4(L + 512 + (2 + t) * 4096 + g * 16);
            cv[t] = LDSF(L + 512 + (4 + t) * 4096 + tk * 4);
        }
#define CORR(A, C) u32x4{pk2(fma2(sp2(nm), lo2(C[0]), lo2(A[0])) * sp2(rs)), pk2(fma2(sp2(nm), hi2(C[0]), hi2(A[0])) * sp2(rs)), \
                         pk2(fma2(sp2(nm), lo2(C[1]), lo2(A[1])) * sp2(rs)), pk2(fma2(sp2(nm), hi2(C[1]), hi2(A[1])) * sp2(rs))}
#pragma unroll
        for (int i = 0; i < 2; ++i) {
            const float nm = LDSF(ST + i * 64 + tk * 4), rs = LDSF(ST + 256 + i * 64 + tk * 4);
            u32x4 kv = CORR(aK[i], ck);
            kv[2] = pad ? 0u : kv[2];
            kv[3] = pad ? 0u : kv[3];
            __builtin_amdgcn_raw_buffer_store_b128(kv, rk, kvo, i * 1024, 16);
            LDSV(u32x4, L + AT_K + (16 + 2 * w + i) * 1024 + lane * 16) = kv;
        }
        f32x4 s0[2], s1[2];
#pragma unroll
        for (int u = 0; u < 2; ++u) {
            s0[u] = lds_f4(ST + u * 64 + g * 16);
            s1[u] = lds_f4(ST + 256 + u * 64 + g * 16);
        }
#pragma unroll
        for (int t = 0; t < 2; ++t) {
            u32x4 o;
#pragma unroll
            for (int u = 0; u < 2; ++u) {
                o[2 * u] = pk2(fma2(lo2(s0[u]), sp2(cv[t]), lo2(aV[u][t])) * lo2(s1[u]));
                o[2 * u + 1] = pk2(fma2(hi2(s0[u]), sp2(cv[t]), hi2(aV[u][t])) * hi2(s1[u]));
            }
            if (t == 0 && tk == 0) o = u32x4{0x3F803F80u, 0x3F803F80u, 0x3F803F80u, 0x3F803F80u};
            __builtin_amdgcn_raw_buffer_store_b128(o, rk, kvo, 16384 + t * 1024, 16);
            LDSV(u32x4, L + AT_V + ((8 + w) * 2 + t) * 1024 + lane * 16) = o;
        }
#pragma unroll
        for (int i = 0; i < 2; ++i) {
            const float nm = LDSF(ST + i * 64 + tk * 4), rs = LDSF(ST + 256 + i * 64 + tk * 4);
            u32x4 qv = CORR(aQ[i], cq);
            qv[2] = pad ? 0x3F80u : qv[2];
            qv[3] = pad ? 0u : qv[3];
            qf[i] = __builtin_bit_cast(bf16x8, qv);
        }
    }
    WAIT_BAR0();
    if (tid == 0) __hip_atomic_store((gu64*)(ws + FLAG_OFF + (size_t)(256 + ch * 16 + head) * 64), tag, RLX_AGENT);
    STAMP(3)
    p2_lane(P);
    p2_dma_own4(P, 0, 0); p2_dma_own4(P, 1, 2); p2_dma_own4(P, 2, 4);
    {
        u32x4 hk[2], hv[2];
        if (ch) {
            wait_flags(ws, 256 + (ch - 1) * 16 + head, 1, lane, tag);
            const unsigned kvo = ((ch - 1) * 16 + head) * 32768 + w * 2048 + lane * 16;
#pragma unroll
            for (int i = 0; i < 2; ++i) {
                hk[i] = __builtin_amdgcn_raw_buffer_load_b128(rk, kvo, i * 1024, 16);
                hv[i] = __builtin_amdgcn_raw_buffer_load_b128(rk, kvo, 16384 + i * 1024, 16);
            }
        } else {
            hk[0] = hk[1] = u32x4{0u, 0u, pad ? 0xF149u : 0u, 0u};
            hv[0] = hv[1] = u32x4{0u, 0u, 0u, 0u};
        }
#pragma unroll
        for (int i = 0; i < 2; ++i) {
            LDSV(u32x4, L + AT_K + (2 * w + i) * 1024 + lane * 16) = hk[i];
            LDSV(u32x4, L + AT_V + (w * 2 + i) * 1024 + lane * 16) = hv[i];
        }
    }
    LBAR();
    STAMP(5)
    const float NEG = -1e30f;
    f32x4 mle, mgt;
#pragma unroll
    for (int r = 0; r < 4; ++r) { mle[r] = (4 * g + r <= tk) ? 0.f : NEG; mgt[r] = (4 * g + r > tk) ? 0.f : NEG; }
    f32x4 Sc[2][18];
#pragma unroll
    for (int j = 0; j < 18; ++j) {
        const bf16x8 kf = lds_b128(L + AT_K + (2 * w + j) * 1024 + lane * 16);
#pragma unroll
        for (int qt = 0; qt < 2; ++qt) {
            if (j >= qt && j <= qt + 16) {
                f32x4 c0;
                if (j == qt + 16) c0 = mle;
                else if (j == qt) c0 = mgt;
                else c0 = f32x4{0.f, 0.f, 0.f, 0.f};
                Sc[qt][j] = MFMA16(kf, qf[qt], c0);
            } else {
                Sc[qt][j] = f32x4{0.f, 0.f, 0.f, 0.f};
            }
        }
    }
    SB();
    LBAR();
    const float CS = 0.18898223650461362f * 1.4426950408889634f;
    u32x4 pf[2][9];
#pragma unroll
    for (int qt = 0; qt < 2; ++qt) {
        float m = NEG;
#pragma unroll
        for (int j = qt; j <= qt + 16; ++j) m = fmaxf(fmaxf(m, fmaxf(Sc[qt][j][0], Sc[qt][j][1])), fmaxf(Sc[qt][j][2], Sc[qt][j][3]));
        m = xmax(m);
        const float mc = -m * CS;
#pragma unroll
        for (int j = qt; j <= qt + 16; ++j) {
            const f32x2 a = fma2(lo2(Sc[qt][j]), sp2(CS), sp2(mc)), b = fma2(hi2(Sc[qt][j]), sp2(CS), sp2(mc));
            Sc[qt][j] = f32x4{__builtin_amdgcn_exp2f(a[0]), __builtin_amdgcn_exp2f(a[1]), __builtin_amdgcn_exp2f(b[0]), __builtin_amdgcn_exp2f(b[1])};
            if (qt == 1 && j >= 2) {
                if (j < 10) p2_dma_own(P, 3 + ((j - 2) >> 2), 2 * ((j - 2) >> 2), (j - 2) & 3);
                else p2_dma(P, 9 + ((j - 10) >> 1), 4 + ((j - 10) >> 1), j & 1);
                SB();
            }
        }
#pragma unroll
        for (int b = 0; b < 9; ++b)
            pf[qt][b] = u32x4{pk2(Sc[qt][2 * b][0], Sc[qt][2 * b][1]), pk2(Sc[qt][2 * b][2], Sc[qt][2 * b][3]),
                              pk2(Sc[qt][2 * b + 1][0], Sc[qt][2 * b + 1][1]), pk2(Sc[qt][2 * b + 1][2], Sc[qt][2 * b + 1][3])};
        if (qt == 0) {
            SB();
            asm volatile("s_waitcnt vmcnt(0)" ::: "memory");
            p2_to_regs(P, L, wr[0], 0); p2_to_regs(P, L, wr[1], 2); p2_to_regs(P, L, wr[2], 4);
            WAITL0();
            SB();
        }
    }
    f32x4 O[2][2];
    O[0][0] = O[0][1] = O[1][0] = O[1][1] = f32x4{0.f, 0.f, 0.f, 0.f};
#pragma unroll
    for (int b = 0; b < 9; ++b)
#pragma unroll
        for (int t = 0; t < 2; ++t) {
            const bf16x8 vf = lds_b128(L + AT_V + ((w + b) * 2 + t) * 1024 + lane * 16);
#pragma unroll
            for (int qt = 0; qt < 2; ++qt) O[qt][t] = MFMA16(vf, __builtin_bit_cast(bf16x8, pf[qt][b]), O[qt][t]);
        }
    const __amdgpu_buffer_rsrc_t ra = __builtin_amdgcn_make_buffer_rsrc(ws, (short)0, ACT_BYTES, 0x00020000);
#pragma unroll
    for (int qt = 0; qt < 2; ++qt) {
        const float l = __shfl(O[qt][0][0], tk);
        const float inv = __builtin_amdgcn_rcpf(l);
        const unsigned dst = (ch * 16 + 2 * w + qt) * ACT_TILE_B + 224 * 256 + tk * 16;
#pragma unroll
        for (int t = 0; t < 2; ++t) {
            const int u = 7 * head + 4 * t + g - 1;
            if (t == 1 || g > 0)
                __builtin_amdgcn_raw_buffer_store_b64(
                    u32x2{pk2(O[qt][t][0] * inv, O[qt][t][1] * inv), pk2(O[qt][t][2] * inv, O[qt][t][3] * inv)}, ra,
                    dst + (u >> 1) * 256 + (u & 1) * 8, 0, 16);
        }
    }
    STAMP(6)
    WAIT_BAR0();
    if (tid == 0) __hip_atomic_store((gu64*)(ws + FLAG_OFF + (size_t)(ch * 32 + head) * 64), tag, RLX_AGENT);
    p2_to_regs(P, L, wr[3], 0); p2_to_regs(P, L, wr[4], 2);
    WAITL0();
#pragma unroll
    for (int k = 0; k < 4; ++k) p2_dma2(P, 5 + k, k);
    STAMP(7)
    return tag;
}

constexpr int LDS_BYTES = 163840;

__global__ __launch_bounds__(512, 2) void k_fused(const char* x, const char* wqkv, const char* wo, const char* wg, const char* wu,
                                                  const char* wd, char* out, char* ws) {
    extern __shared__ __attribute__((aligned(16))) char smem[];
    lds_ptr L = (lds_ptr)smem;
    const unsigned lb = (unsigned)(size_t)L;
    STAMP_DECL STAMP(0)
    const int tid = threadIdx.x, lane = tid & 63;
    const int w = __builtin_amdgcn_readfirstlane(tid >> 6);
    const int xcd = blockIdx.x & 7, idx = blockIdx.x >> 3;
    const int rblk = 4 * (xcd & 1) + (idx & 3), cblk = 4 * (xcd >> 1) + ((idx >> 2) & 3);
    if (w == 7) {
        unsigned long long pc;
        asm volatile("s_getpc_b64 %0" : "=s"(pc));
        glds16s((const char*)(pc & ~255ull), idx * 1024 + lane * 16, lb + 162816);
        if (idx < 12) glds16s((const char*)(pc & ~255ull), 32768 + idx * 1024 + lane * 16, lb + 162816);
    }
    const int th = w & 1, kg = w >> 1;
    const bool has_p2 = idx >= 4;
    const int rb = 4 * xcd + (idx & 3), cb = has_p2 ? (idx >> 2) - 1 : 0;
    const int a1 = kg ? 14 * (kg - 1) : 56;
    const int a2 = 42 + (kg < 2 ? 3 * kg : 4 * kg - 2);
    P2 P;
    P.n2 = kg < 2 ? 3 : 4;
    P.w1 = (kg ? wd + (size_t)(14 * (kg - 1)) * 32 * 896 : wo) + cb * 128 + th * 16 * 896;
    P.w2 = wd + (size_t)a2 * 32 * 896 + cb * 128 + th * 16 * 896;
    P.ring = lb + kg * 4096 + th * 2048;
    P.th = th; P.kg = kg; P.wd = wd; P.wo = wo;
    bf16x8 wr[5][4];
    unsigned long long tag;
    if (idx < 16) tag = att_block(x, wqkv, ws, rblk, cblk, L, P, wr STAMP_PASS);
    else tag = ffn_block(x, wg, wu, ws, rblk, cblk, L, P, wr STAMP_PASS);
    if (!has_p2) {
        asm volatile("s_waitcnt vmcnt(0)" ::: "memory");
        if (blockIdx.x == 0 && w == 0) {
#pragma unroll 1
            for (int i = 0; i < 4; ++i) wait_flags(ws, 64 * i, 64, lane, tag);
            if (lane == 0) __hip_atomic_store((gu64*)(ws + SYNC_OFF), tag, RLX_AGENT);
        }
        STAMP_FLUSH(ws, 0, 8)
        return;
    }
    int ln = lane;
    asm volatile("" : "+v"(ln));
    const int g = ln >> 4, tk = ln & 15;
    const size_t oo = (size_t)(rb * 64 + 32 * th + tk) * 896 + cb * 128 + kg * 32 + g * 8;
    const u32x2 xr0 = *(const u32x2*)(x + oo), xr1 = *(const u32x2*)(x + oo + 16 * 896);
    STAMP(8)
    if (w == 0) wait_flags(ws, (rb >> 2) * 32, 32, lane, tag);
    asm volatile("s_barrier" ::: "memory");
    STAMP(9)
    const __amdgpu_buffer_rsrc_t ra = __builtin_amdgcn_make_buffer_rsrc(ws, (short)0, ACT_BYTES, 0x00020000);
    const unsigned ab = (rb * 4 + 2 * th) * ACT_TILE_B + lane * 16;
    bf16x8 af[18][2];
#define ALD(u) { const int ks_ = (u) < 14 ? a1 + (u) : a2 + ((u) - 14 < P.n2 ? (u) - 14 : P.n2 - 1);                                      \
                 af[u][0] = __builtin_bit_cast(bf16x8, __builtin_amdgcn_raw_buffer_load_b128(ra, ab, ks_ * 1024, 16));                    \
                 af[u][1] = __builtin_bit_cast(bf16x8, __builtin_amdgcn_raw_buffer_load_b128(ra, ab + ACT_TILE_B, ks_ * 1024, 16)); SB(); }
#pragma unroll
    for (int u = 0; u < 5; ++u) ALD(u)
    f32x4 c1[2][4], c2[2][4];
#pragma unroll
    for (int i = 0; i < 2; ++i)
#pragma unroll
        for (int t = 0; t < 4; ++t) c1[i][t] = c2[i][t] = f32x4{0.f, 0.f, 0.f, 0.f};
#define MM8(ACC, WF, u) { _Pragma("unroll") for (int t = 0; t < 4; ++t) {            \
            ACC[0][t] = MFMA16(WF[t], af[u][0], ACC[0][t]);                          \
            ACC[1][t] = MFMA16(WF[t], af[u][1], ACC[1][t]); } }
#define STEP(ACC, u, SLOT) { bf16x8 wf[4]; _Pragma("unroll") for (int t = 0; t < 4; ++t) wf[t] = P2_WRD(t, SLOT); MM8(ACC, wf, u) }
    STAMP(10)
#pragma unroll
    for (int u = 0; u < 5; ++u) {
        MM8(c1, wr[u], u)
        SB();
        if (u < 2 && idx < 16) p2_dma2(P, 13 + u, 8 + u);
        ALD(u + 5)
    }
    asm volatile("s_barrier" ::: "memory");
    STAMP(11)
#pragma unroll
    for (int u = 5; u < 8; ++u) { STEP(c1, u, u - 5) SB(); ALD(u + 5) }
    LBAR();
#pragma unroll
    for (int u = 8; u < 14; ++u) {
        STEP(c1, u, u - 5)
        SB();
        if (u < 13) ALD(u + 5)
        if (u < 11) p2_dma2(P, u + 7, u - 8);
    }
    STAMP(12)
    STEP(c2, 14, 9)
    SB();
    WAIT_BAR0();
    STAMP(13)
    STEP(c2, 15, 0) STEP(c2, 16, 1)
    if (P.n2 == 4) STEP(c2, 17, 2)
    if (kg == 0) {
#pragma unroll
        for (int i = 0; i < 2; ++i)
#pragma unroll
            for (int t = 0; t < 4; ++t) LDSV(f32x4, L + 131072 + th * 8192 + (i * 4 + t) * 1024 + lane * 16) = c1[i][t];
    } else {
#pragma unroll
        for (int i = 0; i < 2; ++i)
#pragma unroll
            for (int t = 0; t < 4; ++t) c2[i][t] += c1[i][t];
    }
    STAMP(14)
    f32x4 fs[2];
#define PUT(K)                                                                                             \
    {                                                                                                      \
        _Pragma("unroll") for (int i = 0; i < 2; ++i) {                                                    \
            _Pragma("unroll") for (int t = 0; t < 4; ++t)                                                  \
                if (t != K) LDSV(f32x4, L + 49152 + w * 8192 + (i * 4 + t) * 1024 + lane * 16) = c2[i][t]; \
            fs[i] = c2[i][K];                                                                              \
        }                                                                                                  \
    }
    if (kg == 0) PUT(0) else if (kg == 1) PUT(1) else if (kg == 2) PUT(2) else PUT(3)
    LBAR();
#pragma unroll
    for (int i = 0; i < 2; ++i) {
#pragma unroll
        for (int k = 1; k < 4; ++k) fs[i] += lds_f4(L + 49152 + (2 * ((kg + k) & 3) + th) * 8192 + (i * 4 + kg) * 1024 + lane * 16);
        const f32x4 sv = lds_f4(L + 131072 + th * 8192 + (i * 4 + kg) * 1024 + lane * 16);
        const u32x2 xr = i ? xr1 : xr0;
        float o[4];
#pragma unroll
        for (int r = 0; r < 4; ++r) {
            const float fb = rbf(fs[i][r]);
            const float gb = rbf(fb * __builtin_amdgcn_rcpf(1.0f + __builtin_amdgcn_exp2f(-fb * 1.4426950408889634f)));
            const float pb = rbf(rbf(sv[r]) * gb);
            const unsigned xw = xr[r >> 1];
            const float xv = __uint_as_float((r & 1) ? (xw & 0xFFFF0000u) : (xw << 16));
            o[r] = xv + pb;
        }
        *(u32x2*)(out + oo + (size_t)i * 16 * 896) = u32x2{pk2(o[0], o[1]), pk2(o[2], o[3])};
    }
    STAMP(15)
    STAMP_FLUSH(ws, 0, 16)
}

extern "C" void kernel_launch(void* const* d_in, const int* in_sizes, int n_in, void* d_out, int out_size, void* d_ws,
                              size_t ws_size, hipStream_t stream) {
    (void)hipFuncSetAttribute((const void*)k_fused, hipFuncAttributeMaxDynamicSharedMemorySize, LDS_BYTES);
    k_fused<<<256, 512, LDS_BYTES, stream>>>((const char*)d_in[0], (const char*)d_in[1], (const char*)d_in[2], (const char*)d_in[4],
                                             (const char*)d_in[5], (const char*)d_in[6], (char*)d_out, (char*)d_ws);
}
```

```cpp
#include <hip/hip_runtime.h>
#include <hip/hip_bf16.h>

typedef short bf16x8 __attribute__((ext_vector_type(8)));
typedef short s16x4 __attribute__((ext_vector_type(4)));
typedef float f32x4 __attribute__((ext_vector_type(4)));
typedef float f32x2 __attribute__((ext_vector_type(2)));
typedef unsigned u32x4 __attribute__((ext_vector_type(4)));
typedef unsigned u32x2 __attribute__((ext_vector_type(2)));
typedef __bf16 bf16x2_t __attribute__((ext_vector_type(2)));
typedef __attribute__((address_space(3))) char* lds_ptr;

#define MFMA16(a, b, c) __builtin_amdgcn_mfma_f32_16x16x32_bf16(a, b, c, 0, 0, 0)
#define WAIT_BAR0() asm volatile("s_waitcnt vmcnt(0) lgkmcnt(0)\n\ts_barrier" ::: "memory")
#define LBAR() asm volatile("s_waitcnt lgkmcnt(0)\n\ts_barrier" ::: "memory")

#define STAMP_ARG
#define STAMP_PASS
#define STAMP_DECL
#define STAMP(i)
#define STAMP_FLUSH(actp, lo, hi)
constexpr int S_ = 2048, D_ = 448, F_ = 1792, QKV_ = 1344;
constexpr int ACT_OCT = 280;
constexpr int ACT_TILE_B = ACT_OCT * 256 + 256;

__device__ __forceinline__ void glds16(const void* g, unsigned lds_dst) {
    unsigned sv;
    asm volatile("s_mov_b32 %0, m0\n\ts_mov_b32 m0, %2\n\ts_nop 0\n\tglobal_load_lds_dwordx4 %1, off\n\ts_mov_b32 m0, %0"
                 : "=&s"(sv) : "v"(g), "s"(lds_dst) : "memory");
}
__device__ __forceinline__ void glds16s(const void* sbase, unsigned voff, unsigned lds_dst) {
    unsigned sv;
    asm volatile("s_mov_b32 %0, m0\n\ts_mov_b32 m0, %3\n\ts_nop 0\n\tglobal_load_lds_dwordx4 %1, %2\n\ts_mov_b32 m0, %0"
                 : "=&s"(sv) : "v"(voff), "s"(sbase), "s"(lds_dst) : "memory");
}
__device__ __forceinline__ bf16x8 lds_b128(lds_ptr p) { return *(const __attribute__((address_space(3))) bf16x8*)p; }
__device__ __forceinline__ f32x4 lds_f4(lds_ptr p) { return *(const __attribute__((address_space(3))) f32x4*)p; }
__device__ __forceinline__ s16x4 lds_tr(lds_ptr p) {
    return __builtin_amdgcn_ds_read_tr16_b64_v4i16((__attribute__((address_space(3))) s16x4*)p);
}
__device__ __forceinline__ bf16x8 lds_tr2(lds_ptr p, int second) {
    s16x4 a = lds_tr(p), b = lds_tr(p + second);
    return __builtin_shufflevector(a, b, 0, 1, 2, 3, 4, 5, 6, 7);
}
__device__ __forceinline__ float dot2(unsigned a, unsigned b, float c) {
    return __builtin_amdgcn_fdot2_f32_bf16(__builtin_bit_cast(bf16x2_t, a), __builtin_bit_cast(bf16x2_t, b), c, false);
}
__device__ __forceinline__ float frag_sum(bf16x8 f, float c) {
    u32x4 u = __builtin_bit_cast(u32x4, f);
    c = dot2(u[0], 0x3F803F80u, c); c = dot2(u[1], 0x3F803F80u, c);
    c = dot2(u[2], 0x3F803F80u, c); c = dot2(u[3], 0x3F803F80u, c);
    return c;
}
__device__ __forceinline__ float frag_sq(bf16x8 f, float c) {
    u32x4 u = __builtin_bit_cast(u32x4, f);
    c = dot2(u[0], u[0], c); c = dot2(u[1], u[1], c); c = dot2(u[2], u[2], c); c = dot2(u[3], u[3], c);
    return c;
}
__device__ __forceinline__ float xsum(float x) {
    auto s = __builtin_amdgcn_permlane16_swap(__float_as_uint(x), __float_as_uint(x), false, false);
    x = __uint_as_float(s[0]) + __uint_as_float(s[1]);
    auto t = __builtin_amdgcn_permlane32_swap(__float_as_uint(x), __float_as_uint(x), false, false);
    return __uint_as_float(t[0]) + __uint_as_float(t[1]);
}
__device__ __forceinline__ float max3f(float a, float b, float c) { return __builtin_fmaxf(__builtin_fmaxf(a, b), c); }
__device__ __forceinline__ float xmax(float x) {
    auto s = __builtin_amdgcn_permlane16_swap(__float_as_uint(x), __float_as_uint(x), false, false);
    x = fmaxf(__uint_as_float(s[0]), __uint_as_float(s[1]));
    auto t = __builtin_amdgcn_permlane32_swap(__float_as_uint(x), __float_as_uint(x), false, false);
    return fmaxf(__uint_as_float(t[0]), __uint_as_float(t[1]));
}
__device__ __forceinline__ unsigned pk2(float lo, float hi) {
    bf16x2_t v; v[0] = (__bf16)lo; v[1] = (__bf16)hi;
    return __builtin_bit_cast(unsigned, v);
}
__device__ __forceinline__ unsigned pk2(f32x2 v) { return pk2(v[0], v[1]); }
__device__ __forceinline__ f32x2 lo2(f32x4 v) { return __builtin_shufflevector(v, v, 0, 1); }
__device__ __forceinline__ f32x2 hi2(f32x4 v) { return __builtin_shufflevector(v, v, 2, 3); }
__device__ __forceinline__ f32x2 sp2(float v) { return f32x2{v, v}; }
__device__ __forceinline__ f32x2 fma2(f32x2 a, f32x2 b, f32x2 c) { return __builtin_elementwise_fma(a, b, c); }
__device__ __forceinline__ float rbf(float x) { return (float)(__bf16)x; }
__device__ __forceinline__ void ln_stats(float sum, float sq, float& mean, float& den) {
    float m = sum * (1.0f / D_);
    float v = fmaxf(sq * (1.0f / D_) - m * m, 0.0f);
    mean = rbf(m);
    den = rbf(sqrtf(rbf(rbf(v) + 1e-5f)));
}

typedef __attribute__((address_space(1))) unsigned long long gu64;
#define RLX_AGENT __ATOMIC_RELAXED, __HIP_MEMORY_SCOPE_AGENT
#define SB() __builtin_amdgcn_sched_barrier(0)
#define LDSF(p) (*(__attribute__((address_space(3))) float*)(p))
#define LDSV(T, p) (*(__attribute__((address_space(3))) T*)(p))
constexpr size_t SYNC_OFF = 16u << 20, FLAG_OFF = SYNC_OFF + 4096, STAT_OFF = SYNC_OFF + 131072, KV_OFF = 20u << 20;
constexpr int ACT_BYTES = 128 * ACT_TILE_B;
constexpr unsigned long long SPIN_TICKS = 20000000ull;

template <int N> __device__ __forceinline__ float ror_add(float x) {
    return x + __uint_as_float(__builtin_amdgcn_update_dpp(0u, __float_as_uint(x), 0x120 + N, 0xF, 0xF, false));
}
__device__ __forceinline__ float wave_sum(float x) { return xsum(ror_add<1>(ror_add<2>(ror_add<4>(ror_add<8>(x))))); }

__device__ __forceinline__ void start_loads(const char* x, char* ws, int row, int lane, unsigned long long& ep, u32x4& rv) {
    const unsigned zero = 0, ro = (lane < 56 ? lane : 55) * 16;
    asm volatile("global_load_dwordx2 %0, %1, %2 sc1" : "=v"(ep) : "v"(zero), "s"(ws + SYNC_OFF) : "memory");
    asm volatile("global_load_dwordx4 %0, %1, %2" : "=v"(rv) : "v"(ro), "s"(x + (size_t)row * 896) : "memory");
}
__device__ __forceinline__ unsigned long long publish_stats(char* ws, int row, int lane, unsigned long long ep, u32x4 rv) {
    const unsigned long long tag = (((unsigned long long)__builtin_amdgcn_readfirstlane((unsigned)(ep >> 32)) << 32) |
                                    (unsigned)__builtin_amdgcn_readfirstlane((unsigned)ep)) + 1ull;
    if (lane >= 56) rv = u32x4{0u, 0u, 0u, 0u};
    const bf16x8 f = __builtin_bit_cast(bf16x8, rv);
    float mean, den;
    ln_stats(wave_sum(frag_sum(f, 0.f)), wave_sum(frag_sq(f, 0.f)), mean, den);
    const unsigned val = (__float_as_uint(mean) & 0xFFFF0000u) | (__float_as_uint(den) >> 16);
    if (lane == 0) __hip_atomic_store((gu64*)(ws + STAT_OFF) + row, ((unsigned long long)(unsigned)tag << 32) | val, RLX_AGENT);
    return tag;
}
__device__ __forceinline__ void stats_request(char* ws, int row, unsigned long long& v) {
    asm volatile("global_load_dwordx2 %0, %1, %2 sc1" : "=v"(v) : "v"(row * 8), "s"(ws + STAT_OFF) : "memory");
}
__device__ __forceinline__ unsigned stats_check(char* ws, int row, unsigned long long tag, unsigned long long v) {
    const unsigned long long t0 = __builtin_amdgcn_s_memrealtime();
    while (!__all((unsigned)(v >> 32) == (unsigned)tag)) {
        if (__builtin_amdgcn_s_memrealtime() - t0 > SPIN_TICKS) break;
        __builtin_amdgcn_s_sleep(2);
        v = __hip_atomic_load((gu64*)(ws + STAT_OFF) + row, RLX_AGENT);
    }
    return (unsigned)v;
}
__device__ __forceinline__ void wait_flags(char* ws, int first, int n, int lane, unsigned long long tag) {
    const unsigned long long t0 = __builtin_amdgcn_s_memrealtime();
    const int i = first + (lane < n ? lane : n - 1);
    for (;;) {
        const unsigned long long v = __hip_atomic_load((gu64*)(ws + FLAG_OFF + (size_t)i * 64), RLX_AGENT);
        if (__all(v == tag)) break;
        if (__builtin_amdgcn_s_memrealtime() - t0 > SPIN_TICKS) break;
        __builtin_amdgcn_s_sleep(1);
    }
}

struct P2 {
    const char* wd; const char* wo; const char* wg; const char* wu;
    const char* w1; const char* w2; unsigned ring; int n2, th, kg;
    unsigned wv0; int wa[4], own;
};
__device__ __forceinline__ void p2_lane(P2& P) {
    int lane = threadIdx.x & 63;
    asm volatile("" : "+v"(lane));
    const int g = lane >> 4, q = (lane >> 2) & 3, p = lane & 3, f = ((q >> 1) & 1) | ((g & 1) << 1);
    P.wv0 = (lane >> 3) * 896 + (((lane & 7) ^ (((lane >> 4) & 1) << 1)) << 4);
#pragma unroll
    for (int t = 0; t < 4; ++t) P.wa[t] = P.kg * 4096 + (8 * g + q) * 128 + ((t ^ f) << 5) + p * 8;
    P.own = P.th * 2048 + (g >> 1) * (16384 - 2048);
}
__device__ __forceinline__ void p2_dma(const P2& P, int u, int slot, int half) {
    const char* src = u < 14 ? P.w1 + u * (32 * 896) : P.w2 + (u - 14 < P.n2 ? u - 14 : P.n2 - 1) * (32 * 896);
    glds16s(src + half * (8 * 896), half ? (P.wv0 ^ 64) : P.wv0, P.ring + slot * 16384 + half * 1024);
}
__device__ __forceinline__ void p2_dma2(const P2& P, int u, int slot) { p2_dma(P, u, slot, 0); p2_dma(P, u, slot, 1); }
#define P2_WRD(t, SLOT) lds_tr2(L + P.wa[t] + ((SLOT) >> 2) * 65536 + ((SLOT) & 3) * 16384, 512)
__device__ __forceinline__ void p2_dma_own(const P2& P, int u, int sl, int j) {
    glds16s(P.w1 + (u * 32 + j * 8 - P.th * 16) * 896, (j & 1) ? (P.wv0 ^ 64) : P.wv0, P.ring + (sl + (j >> 1)) * 16384 + (j & 1) * 1024);
}
__device__ __forceinline__ void p2_dma_own4(const P2& P, int u, int sl) {
    p2_dma_own(P, u, sl, 0); p2_dma_own(P, u, sl, 1); p2_dma_own(P, u, sl, 2); p2_dma_own(P, u, sl, 3);
}
__device__ __forceinline__ void p2_to_regs(const P2& P, lds_ptr L, bf16x8 (&wr)[4], int sl) {
#pragma unroll
    for (int t = 0; t < 4; ++t) wr[t] = lds_tr2(L + P.wa[t] + P.own + (sl >> 2) * 65536 + (sl & 3) * 16384, 512);
}
__device__ __forceinline__ void p2_touch(const P2& P, int w, int lane, unsigned lb) {
    const unsigned o1 = blockIdx.x * 6272 + w * 1024 + lane * 16, o2 = blockIdx.x * 1568 + (w & 1) * 1024 + lane * 16;
    if (w < 7) glds16s(P.wd, o1 < F_ * 896 - 16 ? o1 : F_ * 896 - 16, lb + 162816);
    if (w >= 6) glds16s(P.wo, o2 < D_ * 896 - 16 ? o2 : D_ * 896 - 16, lb + 162816);
}
#define WAITL0() asm volatile("s_waitcnt lgkmcnt(0)" ::: "memory")

constexpr int FF_XS = 32768, FF_UOFF = 15376, FF_WS = 2 * FF_UOFF, FF_W0 = 2 * FF_XS, FF_CS = 163840 - 1024;

__device__ __forceinline__ unsigned long long ffn_block(const char* x, const char* wg, const char* wu, char* ws, int rb, int cb,
                                                        lds_ptr L, P2& P, bf16x8 (&wr)[5][4] STAMP_ARG) {
    const int tid = threadIdx.x, lane = tid & 63;
    const int w = __builtin_amdgcn_readfirstlane(tid >> 6);
    const int wm = w & 3, wn = w >> 2;
    const unsigned lb = (unsigned)(size_t)L;
    unsigned long long ep; u32x4 rv;
    const int srow = rb * 256 + (16 + cb) * 8 + w;
    start_loads(x, ws, srow, lane, ep, rv);
    const unsigned xo0 = (lane >> 3) * 896 + (((lane & 7) ^ (lane >> 4)) << 4), xo1 = xo0 ^ 64;
    const char* xb = x + (size_t)(rb * 256 + 64 * wm) * 896;
    auto dma_x = [&](int c, int j) { glds16s(xb + j * (8 * 896) + c * 128, (j & 1) ? xo1 : xo0, lb + (c & 1) * FF_XS + (64 * wm + 8 * j) * 128); };
    unsigned woff[8];
    auto dma_w1 = [&](int c, int buf, int j) {
        const int n = (j == 7 && wm > 1) ? 29 : wm + 4 * j, mat = n >= 15, nn = n - 15 * mat;
        glds16s((mat ? wu : wg) + cb * 224 + (size_t)c * (64 * F_ * 2), woff[j], lb + FF_W0 + mat * FF_UOFF + nn * 1024 + buf);
    };
    if (wn == 0) {
#pragma unroll
        for (int j = 0; j < 8; ++j) dma_x(0, j);
#pragma unroll
        for (int j = 0; j < 3; ++j) dma_x(1, j);
    }
#pragma unroll
    for (int j = 0; j < 8; ++j) {
        int n = wm + 4 * j; if (n > 29) n = 29;
        const int nn = n >= 15 ? n - 15 : n;
        const int g0 = (nn * 64) / 120, r0 = nn * 64 - g0 * 120;
        int rem = r0 + lane;
        const bool wrap = rem >= 120;
        rem = wrap ? rem - 120 : (rem >= 112 ? rem - 8 : rem);
        const int r = (rem * 147) >> 11;
        woff[j] = (8 * g0 + (wrap ? 8 : 0) + r) * (F_ * 2) + (rem - r * 14) * 16;
        if (wn == 1) dma_w1(0, 0, j);
    }
    if (wn == 1) {
#pragma unroll
        for (int j = 0; j < 8; ++j) dma_w1(1, FF_WS, j);
    }
    p2_touch(P, w, lane, lb);
    asm volatile("s_waitcnt vmcnt(12)" : "+v"(ep), "+v"(rv) :: "memory");
    const unsigned long long tag = publish_stats(ws, srow, lane, ep, rv);
    const int g = lane >> 4, q = (lane >> 2) & 3, p = lane & 3, tk = lane & 15;
    const int xa = (64 * wm + tk) * 128 + ((g ^ ((lane >> 1) & 7)) << 4);
    const int wa = FF_W0 + g * 1920 + q * 224 + (p & 1) * 8 + (p >> 1) * FF_UOFF + wn * 112;
    const int tc1 = (wm + 4 > 6) ? 6 : wm + 4;
    f32x4 acc[4][7];
#pragma unroll
    for (int i = 0; i < 4; ++i)
#pragma unroll
        for (int t = 0; t < 7; ++t) acc[i][t] = f32x4{0.f, 0.f, 0.f, 0.f};
    f32x4 cs0 = f32x4{0.f, 0.f, 0.f, 0.f}, cs1 = cs0;
    const bf16x8 ones = __builtin_bit_cast(bf16x8, u32x4{0x3F803F80u, 0x3F803F80u, 0x3F803F80u, 0x3F803F80u});
    bf16x8 xf[2][4], wf[4], cf;
    if (wn == 0) asm volatile("s_waitcnt vmcnt(5)\n\ts_barrier" ::: "memory");
    else asm volatile("s_waitcnt vmcnt(10)\n\ts_barrier" ::: "memory");
    STAMP(1)
#define XRD(B, ks, i) xf[ks][i] = lds_b128((B) + (xa ^ ((ks) * 64)) + (i) * 2048)
#define WRD(B, s) lds_tr2((B) + ((s) / 7) * 7680 + ((s) % 7) * 16, 896)
    {
        lds_ptr W = L + wa;
        XRD(L, 0, 0); XRD(L, 0, 1); XRD(L, 0, 2); XRD(L, 0, 3);
        wf[0] = WRD(W, 0); wf[1] = WRD(W, 1); wf[2] = WRD(W, 2);
    }
    unsigned long long sreq;
    int wb = 0;
#define FCHUNK(c, ROLE, MODE)                                                                                          \
    {                                                                                                                  \
        const int wb1 = (wb == 2 * FF_WS) ? 0 : wb + FF_WS, wb2 = (wb == 0) ? 2 * FF_WS : wb - FF_WS;                  \
        lds_ptr X = L + ((c) & 1) * FF_XS, Xn = L + (((c) + 1) & 1) * FF_XS, W = L + wa + wb, Wn = L + wa + wb1;       \
        _Pragma("unroll") for (int s = 0; s < 14; ++s) {                                                               \
            const int ks = s / 7, t = s % 7;                                                                           \
            if (s == 11 && MODE < 2) {                                                                                 \
                if (ROLE == 1 && MODE == 0) asm volatile("s_waitcnt vmcnt(8) lgkmcnt(0)\n\ts_barrier" ::: "memory");  \
                else WAIT_BAR0();                                                                                      \
            }                                                                                                          \
            if (s < 11) wf[(s + 3) % 4] = WRD(W, s + 3); else if (MODE < 2) wf[(s + 3) % 4] = WRD(Wn, s - 11);        \
            if (s < 4) XRD(X, 1, s);                                                                                   \
            if (s == 11 && MODE < 2) { XRD(Xn, 0, 0); XRD(Xn, 0, 1); }                                                 \
            if (s == 12 && MODE < 2) { XRD(Xn, 0, 2); XRD(Xn, 0, 3); }                                                 \
            if (s == 4 || s == 6 || s == 8 || s == 10) cf = lds_tr2(W + (s >= 8) * 7680 + ((s == 4 || s == 8) ? wm : tc1) * 16, 896); \
            if (ROLE == 0 && MODE < 2 && s < 5) dma_x((c) + 1, 3 + s);                                                 \
            if (ROLE > 0 && MODE == 0 && s >= 3 && s < 11) dma_w1((c) + 2, wb2, s - 3);                         \
            if (ROLE == 0 && MODE == 0 && s >= 11) dma_x((c) + 2, s - 11);                                             \
            if (MODE == 3 && s >= 2 && s < 10) p2_dma_own(P, (s - 2) >> 2, s < 6 ? 2 : 6, (s - 2) & 3);                \
            _Pragma("unroll") for (int i = 0; i < 4; ++i) acc[i][t] = MFMA16(wf[s % 4], xf[ks][i], acc[i][t]);        \
            if (s == 5 || s == 9) cs0 = MFMA16(cf, ones, cs0);                                                         \
            if (s == 7 || s == 12) cs1 = MFMA16(cf, ones, cs1);                                                        \
            SB();                                                                                                      \
        }                                                                                                              \
        { bf16x8 a_ = wf[2], b_ = wf[3], d_ = wf[0]; wf[0] = a_; wf[1] = b_; wf[2] = d_; }                             \
        wb = wb1;                                                                                                      \
    }
    if (wn == 0) {
#pragma unroll 1
        for (int c = 0; c < 5; ++c) FCHUNK(c, 0, 0)
        stats_request(ws, rb * 256 + 64 * wm + lane, sreq);
        FCHUNK(5, 0, 1)
    } else {
#pragma unroll 1
        for (int c = 0; c < 5; ++c) FCHUNK(c, 1, 0)
        stats_request(ws, rb * 256 + 64 * wm + lane, sreq);
        FCHUNK(5, 1, 1)
    }
    STAMP(3)
    p2_lane(P);
    FCHUNK(6, 0, 3)
    STAMP(4)
    if (tk == 0) {
        LDSV(f32x4, L + FF_CS + (7 * wn + wm) * 64 + g * 16) = cs0;
        if (wm < 3) LDSV(f32x4, L + FF_CS + (7 * wn + wm + 4) * 64 + g * 16) = cs1;
    }
    asm volatile("" : "+v"(sreq));
    const unsigned sv = stats_check(ws, rb * 256 + 64 * wm + lane, tag, sreq);
    LBAR();
    STAMP(5)
    const __amdgpu_buffer_rsrc_t ra = __builtin_amdgcn_make_buffer_rsrc(ws, (short)0, ACT_BYTES, 0x00020000);
    f32x4 csr[7];
#pragma unroll
    for (int t = 0; t < 7; ++t) csr[t] = lds_f4(L + FF_CS + (7 * wn + t) * 64 + g * 16);
    LBAR();
    const float K1 = -2.3022081986f, K2 = -0.1029432398f;
#pragma unroll
    for (int ip = 0; ip < 2; ++ip) {
        const unsigned sa = __shfl(sv, 32 * ip + tk), sb = __shfl(sv, 32 * ip + 16 + tk);
        const float ma = -__uint_as_float(sa & 0xFFFF0000u), mb = -__uint_as_float(sb & 0xFFFF0000u);
        const float dn = __uint_as_float((lane < 32 ? sa : sb) << 16);
        const float a = __builtin_amdgcn_rcpf(dn), ia2 = dn * dn, c1 = K1 * a, c2 = K2 * a * a * a;
        const unsigned dst = (rb * 16 + wm * 4 + 2 * ip + (lane >> 5)) * ACT_TILE_B + (cb * 14 + wn * 7) * 256 + tk * 16;
        u32x2 hp[7];
#pragma unroll
        for (int t = 0; t < 7; ++t) {
            const f32x4 cs = csr[t];
#pragma unroll
            for (int u = 0; u < 2; ++u) {
                const f32x2 c2v = u ? hi2(cs) : lo2(cs);
                const f32x2 va = fma2(sp2(ma), c2v, u ? hi2(acc[2 * ip][t]) : lo2(acc[2 * ip][t]));
                const f32x2 vb = fma2(sp2(mb), c2v, u ? hi2(acc[2 * ip + 1][t]) : lo2(acc[2 * ip + 1][t]));
                auto s0 = __builtin_amdgcn_permlane32_swap(__float_as_uint(va[0]), __float_as_uint(vb[0]), false, false);
                auto s1 = __builtin_amdgcn_permlane32_swap(__float_as_uint(va[1]), __float_as_uint(vb[1]), false, false);
                const f32x2 G = f32x2{__uint_as_float(s0[0]), __uint_as_float(s1[0])}, U = f32x2{__uint_as_float(s0[1]), __uint_as_float(s1[1])};
                const f32x2 z = G * fma2(G * G, sp2(c2), sp2(c1));
                const f32x2 e = f32x2{__builtin_amdgcn_exp2f(z[0]), __builtin_amdgcn_exp2f(z[1])};
                const f32x2 d = fma2(e, sp2(ia2), sp2(ia2));
                hp[t][u] = pk2((G * U) * f32x2{__builtin_amdgcn_rcpf(d[0]), __builtin_amdgcn_rcpf(d[1])});
            }
            if (t & 1) {
                auto s0 = __builtin_amdgcn_permlane16_swap(hp[t - 1][0], hp[t][0], false, false);
                auto s1 = __builtin_amdgcn_permlane16_swap(hp[t - 1][1], hp[t][1], false, false);
                __builtin_amdgcn_raw_buffer_store_b128(u32x4{s0[0], s1[0], s0[1], s1[1]}, ra, dst + (g & 1) * 256, (t - 1) * 256, 16);
            }
            if (t == 6) __builtin_amdgcn_raw_buffer_store_b64(hp[6], ra, dst + (g & 1) * 8, 6 * 256, 16);
            {
                if (ip == 0) {
                    if (t < 6) { p2_dma_own(P, 2 + (t >> 1), 4 * (t >> 1), 2 * (t & 1)); p2_dma_own(P, 2 + (t >> 1), 4 * (t >> 1), 2 * (t & 1) + 1); }
                } else if (t < 5) p2_dma2(P, 5 + t, t);
            }
            SB();
        }
        if (ip == 0) {
            asm volatile("s_waitcnt vmcnt(1)" ::: "memory");
            p2_to_regs(P, L, wr[0], 2); p2_to_regs(P, L, wr[1], 6); p2_to_regs(P, L, wr[2], 0); p2_to_regs(P, L, wr[3], 4); p2_to_regs(P, L, wr[4], 8);
            WAITL0();
            SB();
        }
    }
    STAMP(6)
    WAIT_BAR0();
    if (tid == 0) __hip_atomic_store((gu64*)(ws + FLAG_OFF + (size_t)(rb * 32 + 16 + cb) * 64), tag, RLX_AGENT);
#pragma unroll
    for (int k = 5; k < 10; ++k) p2_dma2(P, 5 + k, k);
    STAMP(7)
    return tag;
}

constexpr int TN = 0;
constexpr int AT_WM = 4096, AT_WS = 3 * AT_WM, AT_W = 131072;
constexpr int AT_K = 98304, AT_V = AT_K + 32768;

__device__ __forceinline__ unsigned long long att_block(const char* x, const char* wqkv, char* ws, int ch, int head, lds_ptr L,
                                                        P2& P, bf16x8 (&wr)[5][4] STAMP_ARG) {
    const int tid = threadIdx.x, lane = tid & 63;
    const int w = __builtin_amdgcn_readfirstlane(tid >> 6);
    const unsigned lb = (unsigned)(size_t)L;
    const int g = lane >> 4, q = (lane >> 2) & 3, p = lane & 3, tk = lane & 15;
    unsigned long long ep; u32x4 rv;
    const int srow = ch * 256 + head * 8 + w;
    start_loads(x, ws, srow, lane, ep, rv);
    const unsigned wo = (lane >> 2) * (QKV_ * 2) + (((lane & 3) ^ (((lane >> 5) & 1) << 1)) << 4);
    const char* wsrc0 = wqkv + (w & 3) * (16 * QKV_ * 2) + (w >> 2) * 896 + head * 56;
    const char* wsrc1 = wqkv + (w & 3) * (16 * QKV_ * 2) + 2 * 896 + head * 56 - 8;
    auto dma_w = [&](int c) {
        glds16s(wsrc0 + c * (64 * QKV_ * 2), wo, lb + AT_W + (c & 1) * AT_WS + w * 1024);
        if (w < 4) glds16s(wsrc1 + c * (64 * QKV_ * 2), wo, lb + AT_W + (c & 1) * AT_WS + 8192 + w * 1024);
    };
    const int orow = ch * 256 + 32 * w;
    const unsigned xo0 = (lane >> 3) * 896 + (((lane & 7) ^ (lane >> 4)) << 4), xo1 = xo0 ^ 64;
    const char* xs = x + (size_t)orow * 896;
    auto dma_x = [&](int c, int i) {
        const char* src = xs + i * (16 * 896) + c * 128;
        glds16s(src, xo0, lb + (c & 3) * 32768 + w * 4096 + i * 2048);
        glds16s(src + 8 * 896, xo1, lb + (c & 3) * 32768 + w * 4096 + i * 2048 + 1024);
    };
    dma_w(0); dma_x(0, 0); dma_x(0, 1);
    dma_w(1); dma_x(1, 0); dma_x(1, 1); dma_x(2, 0); dma_x(2, 1);
    p2_touch(P, w, lane, lb);
    asm volatile("s_waitcnt vmcnt(15)" : "+v"(ep), "+v"(rv) :: "memory");
    const unsigned long long tag = publish_stats(ws, srow, lane, ep, rv);
    const int xa0 = w * 4096 + tk * 128 + ((g ^ ((lane >> 1) & 7)) << 4), xa1 = xa0 ^ 64;
    const int wA0 = AT_W + (8 * g + q) * 64 + ((g & 1) << 5) + p * 8, wA1 = wA0 ^ 32;
    const int cidx = w > 5 ? 5 : w;
    const int wC = (cidx >> 1) * AT_WM + (wA0 ^ ((cidx & 1) << 5));
    f32x4 aK[2][2], aV[2][2], aQ[2][2], cs = f32x4{0.f, 0.f, 0.f, 0.f};
#pragma unroll
    for (int i = 0; i < 2; ++i) aK[i][0] = aK[i][1] = aV[i][0] = aV[i][1] = aQ[i][0] = aQ[i][1] = f32x4{0.f, 0.f, 0.f, 0.f};
    const bf16x8 ones = __builtin_bit_cast(bf16x8, u32x4{0x3F803F80u, 0x3F803F80u, 0x3F803F80u, 0x3F803F80u});
    bf16x8 wf[2][3][2], cf[2], xf[2][2];
#define AWRD(B, ks) { _Pragma("unroll") for (int m = 0; m < 3; ++m) { wf[ks][m][0] = lds_tr2((B) + wA0 + m * AT_WM + (ks) * 2048, 256); \
                      wf[ks][m][1] = lds_tr2((B) + wA1 + m * AT_WM + (ks) * 2048, 256); } cf[ks] = lds_tr2((B) + wC + (ks) * 2048, 256); }
#define AXRD(c, i) { xf[i][0] = lds_b128(L + xa0 + ((c) & 3) * 32768 + (i) * 2048); xf[i][1] = lds_b128(L + xa1 + ((c) & 3) * 32768 + (i) * 2048); }
#define AMM(i, ks) { _Pragma("unroll") for (int t = 0; t < 2; ++t) { \
                       aK[i][t] = MFMA16(wf[ks][1][t], xf[i][ks], aK[i][t]); aV[i][t] = MFMA16(xf[i][ks], wf[ks][2][t], aV[i][t]); \
                       aQ[i][t] = MFMA16(wf[ks][0][t], xf[i][ks], aQ[i][t]); } }
    asm volatile("s_waitcnt vmcnt(%0)\n\ts_barrier" :: "i"(11 + TN) : "memory");
    STAMP(1)
    AXRD(0, 0) AWRD(L, 0) AWRD(L, 1)
    SB();
    const int strow = orow + (lane & 31);
    unsigned long long sreq;
#pragma unroll
    for (int c = 0; c < 7; ++c) {
        AXRD(c, 1)
        AMM(0, 0)
        cs = MFMA16(cf[0], ones, cs);
        AMM(0, 1)
        cs = MFMA16(cf[1], ones, cs);
        SB();
        if (c + 3 < 7) dma_x(c + 3, 0);
        if (c == 4) stats_request(ws, strow, sreq);
        if (c < 6) {
            asm volatile("s_waitcnt vmcnt(%0)\n\ts_barrier" :: "i"(c == 0 ? 8 + TN : c < 4 ? 4 : c == 4 ? 3 : 0) : "memory");
            AXRD(c + 1, 0)
            if (c + 2 < 7) dma_w(c + 2);
        }
        AMM(1, 0)
        SB();
        if (c < 6) AWRD(L + ((c + 1) & 1) * AT_WS, 0)
        AMM(1, 1)
        SB();
        if (c < 6) AWRD(L + ((c + 1) & 1) * AT_WS, 1)
        if (c + 3 < 7) dma_x(c + 3, 1);
        SB();
    }
    asm volatile("" : "+v"(sreq));
    STAMP(4)
    lds_ptr ST = L + w * 4096;
    if (tk == 0) LDSV(f32x4, ST + 512 + g * 16) = cs;
    {
        const unsigned sv = stats_check(ws, strow, tag, sreq);
        LDSF(ST + lane * 4) = -__uint_as_float(sv & 0xFFFF0000u);
        LDSF(ST + 256 + lane * 4) = __builtin_amdgcn_rcpf(__uint_as_float(sv << 16));
    }
    LBAR();
    bf16x8 qf[2];
    const bool pad = (lane >= 48);
    const __amdgpu_buffer_rsrc_t rk = __builtin_amdgcn_make_buffer_rsrc(ws + KV_OFF, (short)0, 128 * 32768, 0x00020000);
    {
        const unsigned kvo = (ch * 16 + head) * 32768 + w * 2048 + lane * 16;
        f32x4 cq[2], ck[2];
        float cv[2];
#pragma unroll
        for (int t = 0; t < 2; ++t) {
            cq[t] = lds_f4(L + 512 + t * 4096 + g * 16);
            ck[t] = lds_f4(L + 512 + (2 + t) * 4096 + g * 16);
            cv[t] = LDSF(L + 512 + (4 + t) * 4096 + tk * 4);
        }
#define CORR(A, C) u32x4{pk2(fma2(sp2(nm), lo2(C[0]), lo2(A[0])) * sp2(rs)), pk2(fma2(sp2(nm), hi2(C[0]), hi2(A[0])) * sp2(rs)), \
                         pk2(fma2(sp2(nm), lo2(C[1]), lo2(A[1])) * sp2(rs)), pk2(fma2(sp2(nm), hi2(C[1]), hi2(A[1])) * sp2(rs))}
#pragma unroll
        for (int i = 0; i < 2; ++i) {
            const float nm = LDSF(ST + i * 64 + tk * 4), rs = LDSF(ST + 256 + i * 64 + tk * 4);
            u32x4 kv = CORR(aK[i], ck);
            kv[2] = pad ? 0u : kv[2];
            kv[3] = pad ? 0u : kv[3];
            __builtin_amdgcn_raw_buffer_store_b128(kv, rk, kvo, i * 1024, 16);
            LDSV(u32x4, L + AT_K + (16 + 2 * w + i) * 1024 + lane * 16) = kv;
        }
        f32x4 s0[2], s1[2];
#pragma unroll
        for (int u = 0; u < 2; ++u) {
            s0[u] = lds_f4(ST + u * 64 + g * 16);
            s1[u] = lds_f4(ST + 256 + u * 64 + g * 16);
        }
#pragma unroll
        for (int t = 0; t < 2; ++t) {
            u32x4 o;
#pragma unroll
            for (int u = 0; u < 2; ++u) {
                o[2 * u] = pk2(fma2(lo2(s0[u]), sp2(cv[t]), lo2(aV[u][t])) * lo2(s1[u]));
                o[2 * u + 1] = pk2(fma2(hi2(s0[u]), sp2(cv[t]), hi2(aV[u][t])) * hi2(s1[u]));
            }
            if (t == 0 && tk == 0) o = u32x4{0x3F803F80u, 0x3F803F80u, 0x3F803F80u, 0x3F803F80u};
            __builtin_amdgcn_raw_buffer_store_b128(o, rk, kvo, 16384 + t * 1024, 16);
            LDSV(u32x4, L + AT_V + ((8 + w) * 2 + t) * 1024 + lane * 16) = o;
        }
#pragma unroll
        for (int i = 0; i < 2; ++i) {
            const float nm = LDSF(ST + i * 64 + tk * 4), rs = LDSF(ST + 256 + i * 64 + tk * 4);
            u32x4 qv = CORR(aQ[i], cq);
            qv[2] = pad ? 0x3F80u : qv[2];
            qv[3] = pad ? 0u : qv[3];
            qf[i] = __builtin_bit_cast(bf16x8, qv);
        }
    }
    asm volatile("s_waitcnt vmcnt(0)" ::: "memory");
    if (lane == 0) __hip_atomic_store((gu64*)(ws + FLAG_OFF + (size_t)(256 + (ch * 16 + head) * 8 + w) * 64), tag, RLX_AGENT);
    LBAR();
    STAMP(3)
    p2_lane(P);
    p2_dma_own4(P, 0, 0); p2_dma_own4(P, 1, 2); p2_dma_own4(P, 2, 4);
    {
        u32x4 hk[2], hv[2];
        if (ch) {
            wait_flags(ws, 256 + ((ch - 1) * 16 + head) * 8 + w, 1, lane, tag);
            const unsigned kvo = ((ch - 1) * 16 + head) * 32768 + w * 2048 + lane * 16;
#pragma unroll
            for (int i = 0; i < 2; ++i) {
                hk[i] = __builtin_amdgcn_raw_buffer_load_b128(rk, kvo, i * 1024, 16);
                hv[i] = __builtin_amdgcn_raw_buffer_load_b128(rk, kvo, 16384 + i * 1024, 16);
            }
        } else {
            hk[0] = hk[1] = u32x4{0u, 0u, pad ? 0xF149u : 0u, 0u};
            hv[0] = hv[1] = u32x4{0u, 0u, 0u, 0u};
        }
#pragma unroll
        for (int i = 0; i < 2; ++i) {
            LDSV(u32x4, L + AT_K + (2 * w + i) * 1024 + lane * 16) = hk[i];
            LDSV(u32x4, L + AT_V + (w * 2 + i) * 1024 + lane * 16) = hv[i];
        }
    }
    asm volatile("s_waitcnt vmcnt(0)" ::: "memory");
    p2_to_regs(P, L, wr[0], 0); p2_to_regs(P, L, wr[1], 2); p2_to_regs(P, L, wr[2], 4);
    LBAR();
    STAMP(5)
    const float NEG = -1e30f;
    f32x4 mle, mgt;
#pragma unroll
    for (int r = 0; r < 4; ++r) { mle[r] = (4 * g + r <= tk) ? 0.f : NEG; mgt[r] = (4 * g + r > tk) ? 0.f : NEG; }
    f32x4 Sc[2][18];
#pragma unroll
    for (int j = 0; j < 18; ++j) {
        const bf16x8 kf = lds_b128(L + AT_K + (2 * w + j) * 1024 + lane * 16);
#pragma unroll
        for (int qt = 0; qt < 2; ++qt) {
            if (j >= qt && j <= qt + 16) {
                f32x4 c0;
                if (j == qt + 16) c0 = mle;
                else if (j == qt) c0 = mgt;
                else c0 = f32x4{0.f, 0.f, 0.f, 0.f};
                Sc[qt][j] = MFMA16(kf, qf[qt], c0);
            } else {
                Sc[qt][j] = f32x4{0.f, 0.f, 0.f, 0.f};
            }
        }
        if (j < 8) p2_dma_own(P, 3 + (j >> 2), 2 * (j >> 2), j & 3);
        else if (j < 12) p2_dma(P, 9 + ((j - 8) >> 1), 4 + ((j - 8) >> 1), j & 1);
    }
    const float CS = 0.18898223650461362f * 1.4426950408889634f;
    u32x4 pf[2][9];
#pragma unroll
    for (int qt = 0; qt < 2; ++qt) {
        float m = NEG, m2 = NEG;
#pragma unroll
        for (int j = qt; j <= qt + 16; ++j) { m = max3f(m, Sc[qt][j][0], Sc[qt][j][1]); m2 = max3f(m2, Sc[qt][j][2], Sc[qt][j][3]); }
        m = max3f(m, m2, m2);
        m = xmax(m);
        const float mc = -m * CS;
#pragma unroll
        for (int j = qt; j <= qt + 16; ++j) {
            const f32x2 a = fma2(lo2(Sc[qt][j]), sp2(CS), sp2(mc)), b = fma2(hi2(Sc[qt][j]), sp2(CS), sp2(mc));
            Sc[qt][j] = f32x4{__builtin_amdgcn_exp2f(a[0]), __builtin_amdgcn_exp2f(a[1]), __builtin_amdgcn_exp2f(b[0]), __builtin_amdgcn_exp2f(b[1])};
            if (qt == 1 && (j & 1) == 0) { p2_dma(P, 5 + ((j - 2) >> 2), (j - 2) >> 2, ((j - 2) >> 1) & 1); SB(); }
        }
#pragma unroll
        for (int b = 0; b < 9; ++b)
            pf[qt][b] = u32x4{pk2(Sc[qt][2 * b][0], Sc[qt][2 * b][1]), pk2(Sc[qt][2 * b][2], Sc[qt][2 * b][3]),
                              pk2(Sc[qt][2 * b + 1][0], Sc[qt][2 * b + 1][1]), pk2(Sc[qt][2 * b + 1][2], Sc[qt][2 * b + 1][3])};
        if (qt == 0) {
            SB();
            asm volatile("s_waitcnt vmcnt(0)" ::: "memory");
            p2_to_regs(P, L, wr[3], 0); p2_to_regs(P, L, wr[4], 2);
            WAITL0();
            SB();
        }
    }
    f32x4 O[2][2];
    O[0][0] = O[0][1] = O[1][0] = O[1][1] = f32x4{0.f, 0.f, 0.f, 0.f};
#pragma unroll
    for (int b = 0; b < 9; ++b)
#pragma unroll
        for (int t = 0; t < 2; ++t) {
            const bf16x8 vf = lds_b128(L + AT_V + ((w + b) * 2 + t) * 1024 + lane * 16);
#pragma unroll
            for (int qt = 0; qt < 2; ++qt) O[qt][t] = MFMA16(vf, __builtin_bit_cast(bf16x8, pf[qt][b]), O[qt][t]);
        }
    const __amdgpu_buffer_rsrc_t ra = __builtin_amdgcn_make_buffer_rsrc(ws, (short)0, ACT_BYTES, 0x00020000);
#pragma unroll
    for (int qt = 0; qt < 2; ++qt) {
        const float l = __shfl(O[qt][0][0], tk);
        const float inv = __builtin_amdgcn_rcpf(l);
        const unsigned dst = (ch * 16 + 2 * w + qt) * ACT_TILE_B + 224 * 256 + tk * 16;
#pragma unroll
        for (int t = 0; t < 2; ++t) {
            const int u = 7 * head + 4 * t + g - 1;
            if (t == 1 || g > 0)
                __builtin_amdgcn_raw_buffer_store_b64(
                    u32x2{pk2(O[qt][t][0] * inv, O[qt][t][1] * inv), pk2(O[qt][t][2] * inv, O[qt][t][3] * inv)}, ra,
                    dst + (u >> 1) * 256 + (u & 1) * 8, 0, 16);
        }
    }
    STAMP(6)
    WAIT_BAR0();
    if (tid == 0) __hip_atomic_store((gu64*)(ws + FLAG_OFF + (size_t)(ch * 32 + head) * 64), tag, RLX_AGENT);
#pragma unroll
    for (int k = 6; k < 10; ++k) p2_dma2(P, 5 + k, k);
    STAMP(7)
    return tag;
}

constexpr int LDS_BYTES = 163840;

__global__ __launch_bounds__(512, 2) void k_fused(const char* x, const char* wqkv, const char* wo, const char* wg, const char* wu,
                                                  const char* wd, char* out, char* ws) {
    extern __shared__ __attribute__((aligned(16))) char smem[];
    lds_ptr L = (lds_ptr)smem;
    const unsigned lb = (unsigned)(size_t)L;
    STAMP_DECL STAMP(0)
    const int tid = threadIdx.x, lane = tid & 63;
    const int w = __builtin_amdgcn_readfirstlane(tid >> 6);
    const int xcd = blockIdx.x & 7, idx = blockIdx.x >> 3;
    const int rblk = 4 * (xcd & 1) + (idx & 3), cblk = 4 * (xcd >> 1) + ((idx >> 2) & 3);
    if (w == 7) {
        unsigned long long pc;
        asm volatile("s_getpc_b64 %0" : "=s"(pc));
        glds16s((const char*)(pc & ~255ull), idx * 1024 + lane * 16, lb + 162816);
        if (idx < 15) glds16s((const char*)(pc & ~255ull), 32768 + idx * 1024 + lane * 16, lb + 162816);
    }
    const int th = w & 1, kg = w >> 1;
    const bool has_p2 = idx >= 4;
    const int rb = 4 * xcd + (idx & 3), cb = has_p2 ? (idx >> 2) - 1 : 0;
    const int a1 = kg ? 14 * (kg - 1) : 56;
    const int a2 = 42 + (kg < 2 ? 3 * kg : 4 * kg - 2);
    P2 P;
    P.n2 = kg < 2 ? 3 : 4;
    P.w1 = (kg ? wd + (size_t)(14 * (kg - 1)) * 32 * 896 : wo) + cb * 128 + th * 16 * 896;
    P.w2 = wd + (size_t)a2 * 32 * 896 + cb * 128 + th * 16 * 896;
    P.ring = lb + kg * 4096 + th * 2048;
    P.th = th; P.kg = kg; P.wd = wd; P.wo = wo; P.wg = wg; P.wu = wu;
    bf16x8 wr[5][4];
    unsigned long long tag;
    if (idx < 16) tag = att_block(x, wqkv, ws, rblk, cblk, L, P, wr STAMP_PASS);
    else tag = ffn_block(x, wg, wu, ws, rblk, cblk, L, P, wr STAMP_PASS);
    if (!has_p2) {
        asm volatile("s_waitcnt vmcnt(0)" ::: "memory");
        if (blockIdx.x == 0 && w == 0) {
#pragma unroll 1
            for (int i = 0; i < 4; ++i) wait_flags(ws, 64 * i, 64, lane, tag);
            if (lane == 0) __hip_atomic_store((gu64*)(ws + SYNC_OFF), tag, RLX_AGENT);
        }
        STAMP_FLUSH(ws, 0, 8)
        return;
    }
    int ln = lane;
    asm volatile("" : "+v"(ln));
    const int g = ln >> 4, tk = ln & 15;
    const size_t oo = (size_t)(rb * 64 + 32 * th + tk) * 896 + cb * 128 + kg * 32 + g * 8;
    const u32x2 xr0 = *(const u32x2*)(x + oo), xr1 = *(const u32x2*)(x + oo + 16 * 896);
    STAMP(8)
    if (w == 0) wait_flags(ws, (rb >> 2) * 32, 32, lane, tag);
    asm volatile("s_barrier" ::: "memory");
    STAMP(9)
    const __amdgpu_buffer_rsrc_t ra = __builtin_amdgcn_make_buffer_rsrc(ws, (short)0, ACT_BYTES, 0x00020000);
    const unsigned ab = (rb * 4 + 2 * th) * ACT_TILE_B + lane * 16;
    bf16x8 af[18][2];
#define ALD(u) { const int ks_ = (u) < 14 ? a1 + (u) : a2 + ((u) - 14 < P.n2 ? (u) - 14 : P.n2 - 1);                                      \
                 af[u][0] = __builtin_bit_cast(bf16x8, __builtin_amdgcn_raw_buffer_load_b128(ra, ab, ks_ * 1024, 16));                    \
                 af[u][1] = __builtin_bit_cast(bf16x8, __builtin_amdgcn_raw_buffer_load_b128(ra, ab + ACT_TILE_B, ks_ * 1024, 16)); SB(); }
#pragma unroll
    for (int u = 0; u < 5; ++u) ALD(u)
    f32x4 c1[2][4], c2[2][4];
#pragma unroll
    for (int i = 0; i < 2; ++i)
#pragma unroll
        for (int t = 0; t < 4; ++t) c1[i][t] = c2[i][t] = f32x4{0.f, 0.f, 0.f, 0.f};
#define MM8(ACC, WF, u) { _Pragma("unroll") for (int t = 0; t < 4; ++t) {            \
            ACC[0][t] = MFMA16(WF[t], af[u][0], ACC[0][t]);                          \
            ACC[1][t] = MFMA16(WF[t], af[u][1], ACC[1][t]); } }
#define STEP(ACC, u, SLOT) { bf16x8 wf[4]; _Pragma("unroll") for (int t = 0; t < 4; ++t) wf[t] = P2_WRD(t, SLOT); MM8(ACC, wf, u) }
    STAMP(10)
#pragma unroll
    for (int u = 0; u < 5; ++u) {
        MM8(c1, wr[u], u)
        SB();
        ALD(u + 5)
    }
    asm volatile("s_barrier" ::: "memory");
    STAMP(11)
#pragma unroll
    for (int u = 5; u < 8; ++u) { STEP(c1, u, u - 5) SB(); ALD(u + 5) }
    LBAR();
#pragma unroll
    for (int u = 8; u < 14; ++u) {
        STEP(c1, u, u - 5)
        SB();
        if (u < 13) ALD(u + 5)
        if (u < 11) p2_dma2(P, u + 7, u - 8);
    }
    STAMP(12)
    STEP(c2, 14, 9)
    SB();
    WAIT_BAR0();
    STAMP(13)
    if (kg == 0) {
#pragma unroll
        for (int i = 0; i < 2; ++i)
#pragma unroll
            for (int t = 0; t < 4; ++t) LDSV(f32x4, L + 131072 + th * 8192 + (i * 4 + t) * 1024 + lane * 16) = c1[i][t];
    } else {
#pragma unroll
        for (int i = 0; i < 2; ++i)
#pragma unroll
            for (int t = 0; t < 4; ++t) c2[i][t] += c1[i][t];
    }
    STEP(c2, 15, 0) STEP(c2, 16, 1)
    if (P.n2 == 4) STEP(c2, 17, 2)
    STAMP(14)
    f32x4 fs[2];
#define PUT(K)                                                                                             \
    {                                                                                                      \
        _Pragma("unroll") for (int i = 0; i < 2; ++i) {                                                    \
            _Pragma("unroll") for (int t = 0; t < 4; ++t)                                                  \
                if (t != K) LDSV(f32x4, L + 49152 + w * 8192 + (i * 4 + t) * 1024 + lane * 16) = c2[i][t]; \
            fs[i] = c2[i][K];                                                                              \
        }                                                                                                  \
    }
    if (kg == 0) PUT(0) else if (kg == 1) PUT(1) else if (kg == 2) PUT(2) else PUT(3)
    LBAR();
#pragma unroll
    for (int i = 0; i < 2; ++i) {
#pragma unroll
        for (int k = 1; k < 4; ++k) fs[i] += lds_f4(L + 49152 + (2 * ((kg + k) & 3) + th) * 8192 + (i * 4 + kg) * 1024 + lane * 16);
        const f32x4 sv = lds_f4(L + 131072 + th * 8192 + (i * 4 + kg) * 1024 + lane * 16);
        const u32x2 xr = i ? xr1 : xr0;
        u32x2 o;
#pragma unroll
        for (int u = 0; u < 2; ++u) {
            const f32x2 fv = u ? hi2(fs[i]) : lo2(fs[i]), z = fv * sp2(-1.4426950408889634f);
            const f32x2 d = f32x2{__builtin_amdgcn_exp2f(z[0]), __builtin_amdgcn_exp2f(z[1])} + sp2(1.0f);
            const f32x2 gt = fv * f32x2{__builtin_amdgcn_rcpf(d[0]), __builtin_amdgcn_rcpf(d[1])};
            const f32x2 xv = f32x2{__uint_as_float(xr[u] << 16), __uint_as_float(xr[u] & 0xFFFF0000u)};
            o[u] = pk2(fma2(u ? hi2(sv) : lo2(sv), gt, xv));
        }
        *(u32x2*)(out + oo + (size_t)i * 16 * 896) = o;
    }
    STAMP(15)
    STAMP_FLUSH(ws, 0, 16)
}

extern "C" void kernel_launch(void* const* d_in, const int* in_sizes, int n_in, void* d_out, int out_size, void* d_ws,
                              size_t ws_size, hipStream_t stream) {
    (void)hipFuncSetAttribute((const void*)k_fused, hipFuncAttributeMaxDynamicSharedMemorySize, LDS_BYTES);
    k_fused<<<256, 512, LDS_BYTES, stream>>>((const char*)d_in[0], (const char*)d_in[1], (const char*)d_in[2], (const char*)d_in[4],
                                             (const char*)d_in[5], (const char*)d_in[6], (char*)d_out, (char*)d_ws);
}
```

```cpp
#include <hip/hip_runtime.h>
#include <hip/hip_bf16.h>

typedef short bf16x8 __attribute__((ext_vector_type(8)));
typedef short s16x4 __attribute__((ext_vector_type(4)));
typedef float f32x4 __attribute__((ext_vector_type(4)));
typedef float f32x2 __attribute__((ext_vector_type(2)));
typedef unsigned u32x4 __attribute__((ext_vector_type(4)));
typedef unsigned u32x2 __attribute__((ext_vector_type(2)));
typedef __bf16 bf16x2_t __attribute__((ext_vector_type(2)));
typedef __attribute__((address_space(3))) char* lds_ptr;

#define MFMA16(a, b, c) __builtin_amdgcn_mfma_f32_16x16x32_bf16(a, b, c, 0, 0, 0)
#define WAIT_BAR0() asm volatile("s_waitcnt vmcnt(0) lgkmcnt(0)\n\ts_barrier" ::: "memory")
#define LBAR() asm volatile("s_waitcnt lgkmcnt(0)\n\ts_barrier" ::: "memory")

#define STAMP_ARG
#define STAMP_PASS
#define STAMP_DECL
#define STAMP(i)
#define STAMP_FLUSH(actp, lo, hi)
constexpr int S_ = 2048, D_ = 448, F_ = 1792, QKV_ = 1344;
constexpr int ACT_OCT = 280;
constexpr int ACT_TILE_B = ACT_OCT * 256 + 256;

__device__ __forceinline__ void glds16(const void* g, unsigned lds_dst) {
    unsigned sv;
    asm volatile("s_mov_b32 %0, m0\n\ts_mov_b32 m0, %2\n\ts_nop 0\n\tglobal_load_lds_dwordx4 %1, off\n\ts_mov_b32 m0, %0"
                 : "=&s"(sv) : "v"(g), "s"(lds_dst) : "memory");
}
__device__ __forceinline__ void glds16s(const void* sbase, unsigned voff, unsigned lds_dst) {
    unsigned sv;
    asm volatile("s_mov_b32 %0, m0\n\ts_mov_b32 m0, %3\n\ts_nop 0\n\tglobal_load_lds_dwordx4 %1, %2\n\ts_mov_b32 m0, %0"
                 : "=&s"(sv) : "v"(voff), "s"(sbase), "s"(lds_dst) : "memory");
}
__device__ __forceinline__ bf16x8 lds_b128(lds_ptr p) { return *(const __attribute__((address_space(3))) bf16x8*)p; }
__device__ __forceinline__ f32x4 lds_f4(lds_ptr p) { return *(const __attribute__((address_space(3))) f32x4*)p; }
__device__ __forceinline__ s16x4 lds_tr(lds_ptr p) {
    return __builtin_amdgcn_ds_read_tr16_b64_v4i16((__attribute__((address_space(3))) s16x4*)p);
}
__device__ __forceinline__ bf16x8 lds_tr2(lds_ptr p, int second) {
    s16x4 a = lds_tr(p), b = lds_tr(p + second);
    return __builtin_shufflevector(a, b, 0, 1, 2, 3, 4, 5, 6, 7);
}
__device__ __forceinline__ float dot2(unsigned a, unsigned b, float c) {
    return __builtin_amdgcn_fdot2_f32_bf16(__builtin_bit_cast(bf16x2_t, a), __builtin_bit_cast(bf16x2_t, b), c, false);
}
__device__ __forceinline__ float frag_sum(bf16x8 f, float c) {
    u32x4 u = __builtin_bit_cast(u32x4, f);
    c = dot2(u[0], 0x3F803F80u, c); c = dot2(u[1], 0x3F803F80u, c);
    c = dot2(u[2], 0x3F803F80u, c); c = dot2(u[3], 0x3F803F80u, c);
    return c;
}
__device__ __forceinline__ float frag_sq(bf16x8 f, float c) {
    u32x4 u = __builtin_bit_cast(u32x4, f);
    c = dot2(u[0], u[0], c); c = dot2(u[1], u[1], c); c = dot2(u[2], u[2], c); c = dot2(u[3], u[3], c);
    return c;
}
__device__ __forceinline__ float xsum(float x) {
    auto s = __builtin_amdgcn_permlane16_swap(__float_as_uint(x), __float_as_uint(x), false, false);
    x = __uint_as_float(s[0]) + __uint_as_float(s[1]);
    auto t = __builtin_amdgcn_permlane32_swap(__float_as_uint(x), __float_as_uint(x), false, false);
    return __uint_as_float(t[0]) + __uint_as_float(t[1]);
}
__device__ __forceinline__ float max3f(float a, float b, float c) { return __builtin_fmaxf(__builtin_fmaxf(a, b), c); }
__device__ __forceinline__ float xmax(float x) {
    auto s = __builtin_amdgcn_permlane16_swap(__float_as_uint(x), __float_as_uint(x), false, false);
    x = fmaxf(__uint_as_float(s[0]), __uint_as_float(s[1]));
    auto t = __builtin_amdgcn_permlane32_swap(__float_as_uint(x), __float_as_uint(x), false, false);
    return fmaxf(__uint_as_float(t[0]), __uint_as_float(t[1]));
}
__device__ __forceinline__ unsigned pk2(float lo, float hi) {
    bf16x2_t v; v[0] = (__bf16)lo; v[1] = (__bf16)hi;
    return __builtin_bit_cast(unsigned, v);
}
__device__ __forceinline__ unsigned pk2(f32x2 v) { return pk2(v[0], v[1]); }
__device__ __forceinline__ f32x2 lo2(f32x4 v) { return __builtin_shufflevector(v, v, 0, 1); }
__device__ __forceinline__ f32x2 hi2(f32x4 v) { return __builtin_shufflevector(v, v, 2, 3); }
__device__ __forceinline__ f32x2 sp2(float v) { return f32x2{v, v}; }
__device__ __forceinline__ f32x2 fma2(f32x2 a, f32x2 b, f32x2 c) { return __builtin_elementwise_fma(a, b, c); }
__device__ __forceinline__ float rbf(float x) { return (float)(__bf16)x; }
__device__ __forceinline__ void ln_stats(float sum, float sq, float& mean, float& den) {
    float m = sum * (1.0f / D_);
    float v = fmaxf(sq * (1.0f / D_) - m * m, 0.0f);
    mean = rbf(m);
    den = rbf(sqrtf(rbf(rbf(v) + 1e-5f)));
}

typedef __attribute__((address_space(1))) unsigned long long gu64;
#define RLX_AGENT __ATOMIC_RELAXED, __HIP_MEMORY_SCOPE_AGENT
#define SB() __builtin_amdgcn_sched_barrier(0)
#define LDSF(p) (*(__attribute__((address_space(3))) float*)(p))
#define LDSV(T, p) (*(__attribute__((address_space(3))) T*)(p))
constexpr size_t SYNC_OFF = 16u << 20, FLAG_OFF = SYNC_OFF + 4096, STAT_OFF = SYNC_OFF + 131072, KV_OFF = 20u << 20;
constexpr int ACT_BYTES = 128 * ACT_TILE_B;
constexpr unsigned long long SPIN_TICKS = 20000000ull;

template <int N> __device__ __forceinline__ float ror_add(float x) {
    return x + __uint_as_float(__builtin_amdgcn_update_dpp(0u, __float_as_uint(x), 0x120 + N, 0xF, 0xF, false));
}
__device__ __forceinline__ float wave_sum(float x) { return xsum(ror_add<1>(ror_add<2>(ror_add<4>(ror_add<8>(x))))); }

__device__ __forceinline__ void start_loads(const char* x, char* ws, int row, int lane, unsigned long long& ep, u32x4& rv) {
    const unsigned zero = 0, ro = (lane < 56 ? lane : 55) * 16;
    asm volatile("global_load_dwordx2 %0, %1, %2 sc1" : "=v"(ep) : "v"(zero), "s"(ws + SYNC_OFF) : "memory");
    asm volatile("global_load_dwordx4 %0, %1, %2" : "=v"(rv) : "v"(ro), "s"(x + (size_t)row * 896) : "memory");
}
__device__ __forceinline__ unsigned long long publish_stats(char* ws, int row, int lane, unsigned long long ep, u32x4 rv) {
    const unsigned long long tag = (((unsigned long long)__builtin_amdgcn_readfirstlane((unsigned)(ep >> 32)) << 32) |
                                    (unsigned)__builtin_amdgcn_readfirstlane((unsigned)ep)) + 1ull;
    if (lane >= 56) rv = u32x4{0u, 0u, 0u, 0u};
    const bf16x8 f = __builtin_bit_cast(bf16x8, rv);
    float mean, den;
    ln_stats(wave_sum(frag_sum(f, 0.f)), wave_sum(frag_sq(f, 0.f)), mean, den);
    const unsigned val = (__float_as_uint(mean) & 0xFFFF0000u) | (__float_as_uint(den) >> 16);
    if (lane == 0) __hip_atomic_store((gu64*)(ws + STAT_OFF) + row, ((unsigned long long)(unsigned)tag << 32) | val, RLX_AGENT);
    return tag;
}
__device__ __forceinline__ void stats_request(char* ws, int row, unsigned long long& v) {
    asm volatile("global_load_dwordx2 %0, %1, %2 sc1" : "=v"(v) : "v"(row * 8), "s"(ws + STAT_OFF) : "memory");
}
__device__ __forceinline__ unsigned stats_check(char* ws, int row, unsigned long long tag, unsigned long long v) {
    const unsigned long long t0 = __builtin_amdgcn_s_memrealtime();
    while (!__all((unsigned)(v >> 32) == (unsigned)tag)) {
        if (__builtin_amdgcn_s_memrealtime() - t0 > SPIN_TICKS) break;
        __builtin_amdgcn_s_sleep(2);
        v = __hip_atomic_load((gu64*)(ws + STAT_OFF) + row, RLX_AGENT);
    }
    return (unsigned)v;
}
__device__ __forceinline__ void wait_flags(char* ws, int first, int n, int lane, unsigned long long tag) {
    const unsigned long long t0 = __builtin_amdgcn_s_memrealtime();
    const int i = first + (lane < n ? lane : n - 1);
    for (;;) {
        const unsigned long long v = __hip_atomic_load((gu64*)(ws + FLAG_OFF + (size_t)i * 64), RLX_AGENT);
        if (__all(v == tag)) break;
        if (__builtin_amdgcn_s_memrealtime() - t0 > SPIN_TICKS) break;
        __builtin_amdgcn_s_sleep(1);
    }
}

struct P2 {
    const char* wd; const char* wo; const char* wg; const char* wu;
    const char* w1; const char* w2; unsigned ring; int n2, th, kg;
    unsigned wv0; int wa[4], own;
};
__device__ __forceinline__ void p2_lane(P2& P) {
    int lane = threadIdx.x & 63;
    asm volatile("" : "+v"(lane));
    const int g = lane >> 4, q = (lane >> 2) & 3, p = lane & 3, f = ((q >> 1) & 1) | ((g & 1) << 1);
    P.wv0 = (lane >> 3) * 896 + (((lane & 7) ^ (((lane >> 4) & 1) << 1)) << 4);
#pragma unroll
    for (int t = 0; t < 4; ++t) P.wa[t] = P.kg * 4096 + (8 * g + q) * 128 + ((t ^ f) << 5) + p * 8;
    P.own = P.th * 2048 + (g >> 1) * (16384 - 2048);
}
__device__ __forceinline__ void p2_dma(const P2& P, int u, int slot, int half) {
    const char* src = u < 14 ? P.w1 + u * (32 * 896) : P.w2 + (u - 14 < P.n2 ? u - 14 : P.n2 - 1) * (32 * 896);
    glds16s(src + half * (8 * 896), half ? (P.wv0 ^ 64) : P.wv0, P.ring + slot * 16384 + half * 1024);
}
__device__ __forceinline__ void p2_dma2(const P2& P, int u, int slot) { p2_dma(P, u, slot, 0); p2_dma(P, u, slot, 1); }
#define P2_WRD(t, SLOT) lds_tr2(L + P.wa[t] + ((SLOT) >> 2) * 65536 + ((SLOT) & 3) * 16384, 512)
__device__ __forceinline__ void p2_dma_own(const P2& P, int u, int sl, int j) {
    glds16s(P.w1 + (u * 32 + j * 8 - P.th * 16) * 896, (j & 1) ? (P.wv0 ^ 64) : P.wv0, P.ring + (sl + (j >> 1)) * 16384 + (j & 1) * 1024);
}
__device__ __forceinline__ void p2_dma_own4(const P2& P, int u, int sl) {
    p2_dma_own(P, u, sl, 0); p2_dma_own(P, u, sl, 1); p2_dma_own(P, u, sl, 2); p2_dma_own(P, u, sl, 3);
}
__device__ __forceinline__ void p2_to_regs(const P2& P, lds_ptr L, bf16x8 (&wr)[4], int sl) {
#pragma unroll
    for (int t = 0; t < 4; ++t) wr[t] = lds_tr2(L + P.wa[t] + P.own + (sl >> 2) * 65536 + (sl & 3) * 16384, 512);
}
__device__ __forceinline__ void p2_touch(const P2& P, int w, int lane, unsigned lb) {
    const unsigned o1 = blockIdx.x * 6272 + w * 1024 + lane * 16, o2 = blockIdx.x * 1568 + (w & 1) * 1024 + lane * 16;
    if (w < 7) glds16s(P.wd, o1 < F_ * 896 - 16 ? o1 : F_ * 896 - 16, lb + 162816);
    if (w >= 6) glds16s(P.wo, o2 < D_ * 896 - 16 ? o2 : D_ * 896 - 16, lb + 162816);
}
#define WAITL0() asm volatile("s_waitcnt lgkmcnt(0)" ::: "memory")

constexpr int FF_XS = 32768, FF_UOFF = 15376, FF_WS = 2 * FF_UOFF, FF_W0 = 2 * FF_XS, FF_CS = 163840 - 1024;

__device__ __forceinline__ unsigned long long ffn_block(const char* x, const char* wg, const char* wu, char* ws, int rb, int cb,
                                                        lds_ptr L, P2& P, bf16x8 (&wr)[5][4] STAMP_ARG) {
    const int tid = threadIdx.x, lane = tid & 63;
    const int w = __builtin_amdgcn_readfirstlane(tid >> 6);
    const int wm = w & 3, wn = w >> 2;
    const unsigned lb = (unsigned)(size_t)L;
    unsigned long long ep; u32x4 rv;
    const int srow = rb * 256 + (16 + cb) * 8 + w;
    start_loads(x, ws, srow, lane, ep, rv);
    const unsigned xo0 = (lane >> 3) * 896 + (((lane & 7) ^ (lane >> 4)) << 4), xo1 = xo0 ^ 64;
    const char* xb = x + (size_t)(rb * 256 + 64 * wm) * 896;
    auto dma_x = [&](int c, int j) { glds16s(xb + j * (8 * 896) + c * 128, (j & 1) ? xo1 : xo0, lb + (c & 1) * FF_XS + (64 * wm + 8 * j) * 128); };
    unsigned woff[8];
    auto dma_w1 = [&](int c, int buf, int j) {
        const int n = (j == 7 && wm > 1) ? 29 : wm + 4 * j, mat = n >= 15, nn = n - 15 * mat;
        glds16s((mat ? wu : wg) + cb * 224 + (size_t)c * (64 * F_ * 2), woff[j], lb + FF_W0 + mat * FF_UOFF + nn * 1024 + buf);
    };
    if (wn == 0) {
#pragma unroll
        for (int j = 0; j < 8; ++j) dma_x(0, j);
#pragma unroll
        for (int j = 0; j < 3; ++j) dma_x(1, j);
    }
#pragma unroll
    for (int j = 0; j < 8; ++j) {
        int n = wm + 4 * j; if (n > 29) n = 29;
        const int nn = n >= 15 ? n - 15 : n;
        const int g0 = (nn * 64) / 120, r0 = nn * 64 - g0 * 120;
        int rem = r0 + lane;
        const bool wrap = rem >= 120;
        rem = wrap ? rem - 120 : (rem >= 112 ? rem - 8 : rem);
        const int r = (rem * 147) >> 11;
        woff[j] = (8 * g0 + (wrap ? 8 : 0) + r) * (F_ * 2) + (rem - r * 14) * 16;
        if (wn == 1) dma_w1(0, 0, j);
    }
    if (wn == 1) {
#pragma unroll
        for (int j = 0; j < 8; ++j) dma_w1(1, FF_WS, j);
    }
    p2_touch(P, w, lane, lb);
    asm volatile("s_waitcnt vmcnt(12)" : "+v"(ep), "+v"(rv) :: "memory");
    const unsigned long long tag = publish_stats(ws, srow, lane, ep, rv);
    const int g = lane >> 4, q = (lane >> 2) & 3, p = lane & 3, tk = lane & 15;
    const int xa = (64 * wm + tk) * 128 + ((g ^ ((lane >> 1) & 7)) << 4);
    const int wa = FF_W0 + g * 1920 + q * 224 + (p & 1) * 8 + (p >> 1) * FF_UOFF + wn * 112;
    const int ce = tid < 224 ? tid : 223, crow = 2048 + ((ce >> 3) & 1) * F_ + cb * 112 + (ce >> 4) * 8 + (ce & 7);
    f32x4 acc[4][7];
#pragma unroll
    for (int i = 0; i < 4; ++i)
#pragma unroll
        for (int t = 0; t < 7; ++t) acc[i][t] = f32x4{0.f, 0.f, 0.f, 0.f};
    bf16x8 xf[2][4], wf[4];
    if (wn == 0) asm volatile("s_waitcnt vmcnt(5)\n\ts_barrier" ::: "memory");
    else asm volatile("s_waitcnt vmcnt(10)\n\ts_barrier" ::: "memory");
    STAMP(1)
#define XRD(B, ks, i) xf[ks][i] = lds_b128((B) + (xa ^ ((ks) * 64)) + (i) * 2048)
#define WRD(B, s) lds_tr2((B) + ((s) / 7) * 7680 + ((s) % 7) * 16, 896)
    {
        lds_ptr W = L + wa;
        XRD(L, 0, 0); XRD(L, 0, 1); XRD(L, 0, 2); XRD(L, 0, 3);
        wf[0] = WRD(W, 0); wf[1] = WRD(W, 1); wf[2] = WRD(W, 2);
    }
    unsigned long long sreq, creq;
    int wb = 0;
#define FCHUNK(c, ROLE, MODE)                                                                                          \
    {                                                                                                                  \
        const int wb1 = (wb == 2 * FF_WS) ? 0 : wb + FF_WS, wb2 = (wb == 0) ? 2 * FF_WS : wb - FF_WS;                  \
        lds_ptr X = L + ((c) & 1) * FF_XS, Xn = L + (((c) + 1) & 1) * FF_XS, W = L + wa + wb, Wn = L + wa + wb1;       \
        _Pragma("unroll") for (int s = 0; s < 14; ++s) {                                                               \
            const int ks = s / 7, t = s % 7;                                                                           \
            if (s == 11 && MODE < 2) {                                                                                 \
                if (ROLE == 1 && MODE == 0) asm volatile("s_waitcnt vmcnt(8) lgkmcnt(0)\n\ts_barrier" ::: "memory");  \
                else WAIT_BAR0();                                                                                      \
            }                                                                                                          \
            if (s < 11) wf[(s + 3) % 4] = WRD(W, s + 3); else if (MODE < 2) wf[(s + 3) % 4] = WRD(Wn, s - 11);        \
            if (s < 4) XRD(X, 1, s);                                                                                   \
            if (s == 11 && MODE < 2) { XRD(Xn, 0, 0); XRD(Xn, 0, 1); }                                                 \
            if (s == 12 && MODE < 2) { XRD(Xn, 0, 2); XRD(Xn, 0, 3); }                                                 \
            if (ROLE == 0 && MODE < 2 && s < 5) dma_x((c) + 1, 3 + s);                                                 \
            if (ROLE > 0 && MODE == 0 && s >= 3 && s < 11) dma_w1((c) + 2, wb2, s - 3);                         \
            if (ROLE == 0 && MODE == 0 && s >= 11) dma_x((c) + 2, s - 11);                                             \
            if (MODE == 3 && s >= 2 && s < 10) p2_dma_own(P, (s - 2) >> 2, s < 6 ? 2 : 6, (s - 2) & 3);                \
            _Pragma("unroll") for (int i = 0; i < 4; ++i) acc[i][t] = MFMA16(wf[s % 4], xf[ks][i], acc[i][t]);        \
            SB();                                                                                                      \
        }                                                                                                              \
        { bf16x8 a_ = wf[2], b_ = wf[3], d_ = wf[0]; wf[0] = a_; wf[1] = b_; wf[2] = d_; }                             \
        wb = wb1;                                                                                                      \
    }
    if (wn == 0) {
#pragma unroll 1
        for (int c = 0; c < 5; ++c) FCHUNK(c, 0, 0)
        stats_request(ws, rb * 256 + 64 * wm + lane, sreq);
        FCHUNK(5, 0, 1)
    } else {
#pragma unroll 1
        for (int c = 0; c < 5; ++c) FCHUNK(c, 1, 0)
        stats_request(ws, rb * 256 + 64 * wm + lane, sreq);
        FCHUNK(5, 1, 1)
    }
    STAMP(3)
    p2_lane(P);
    stats_request(ws, crow, creq);
    FCHUNK(6, 0, 3)
    STAMP(4)
    asm volatile("s_waitcnt vmcnt(8)" : "+v"(creq) :: "memory");
    LDSF(L + FF_CS + ce * 4) = __uint_as_float(stats_check(ws, crow, tag, creq));
    asm volatile("" : "+v"(sreq));
    const unsigned sv = stats_check(ws, rb * 256 + 64 * wm + lane, tag, sreq);
    LBAR();
    STAMP(5)
    const __amdgpu_buffer_rsrc_t ra = __builtin_amdgcn_make_buffer_rsrc(ws, (short)0, ACT_BYTES, 0x00020000);
    f32x4 csr[7];
#pragma unroll
    for (int t = 0; t < 7; ++t) csr[t] = lds_f4(L + FF_CS + (7 * wn + t) * 64 + g * 16);
    LBAR();
    const float K1 = -2.3022081986f, K2 = -0.1029432398f;
#pragma unroll
    for (int ip = 0; ip < 2; ++ip) {
        const unsigned sa = __shfl(sv, 32 * ip + tk), sb = __shfl(sv, 32 * ip + 16 + tk);
        const float ma = -__uint_as_float(sa & 0xFFFF0000u), mb = -__uint_as_float(sb & 0xFFFF0000u);
        const float dn = __uint_as_float((lane < 32 ? sa : sb) << 16);
        const float a = __builtin_amdgcn_rcpf(dn), ia2 = dn * dn, c1 = K1 * a, c2 = K2 * a * a * a;
        const unsigned dst = (rb * 16 + wm * 4 + 2 * ip + (lane >> 5)) * ACT_TILE_B + (cb * 14 + wn * 7) * 256 + tk * 16;
        u32x2 hp[7];
#pragma unroll
        for (int t = 0; t < 7; ++t) {
            const f32x4 cs = csr[t];
#pragma unroll
            for (int u = 0; u < 2; ++u) {
                const f32x2 c2v = u ? hi2(cs) : lo2(cs);
                const f32x2 va = fma2(sp2(ma), c2v, u ? hi2(acc[2 * ip][t]) : lo2(acc[2 * ip][t]));
                const f32x2 vb = fma2(sp2(mb), c2v, u ? hi2(acc[2 * ip + 1][t]) : lo2(acc[2 * ip + 1][t]));
                auto s0 = __builtin_amdgcn_permlane32_swap(__float_as_uint(va[0]), __float_as_uint(vb[0]), false, false);
                auto s1 = __builtin_amdgcn_permlane32_swap(__float_as_uint(va[1]), __float_as_uint(vb[1]), false, false);
                const f32x2 G = f32x2{__uint_as_float(s0[0]), __uint_as_float(s1[0])}, U = f32x2{__uint_as_float(s0[1]), __uint_as_float(s1[1])};
                const f32x2 z = G * fma2(G * G, sp2(c2), sp2(c1));
                const f32x2 e = f32x2{__builtin_amdgcn_exp2f(z[0]), __builtin_amdgcn_exp2f(z[1])};
                const f32x2 d = fma2(e, sp2(ia2), sp2(ia2));
                hp[t][u] = pk2((G * U) * f32x2{__builtin_amdgcn_rcpf(d[0]), __builtin_amdgcn_rcpf(d[1])});
            }
            if (t & 1) {
                auto s0 = __builtin_amdgcn_permlane16_swap(hp[t - 1][0], hp[t][0], false, false);
                auto s1 = __builtin_amdgcn_permlane16_swap(hp[t - 1][1], hp[t][1], false, false);
                __builtin_amdgcn_raw_buffer_store_b128(u32x4{s0[0], s1[0], s0[1], s1[1]}, ra, dst + (g & 1) * 256, (t - 1) * 256, 16);
            }
            if (t == 6) __builtin_amdgcn_raw_buffer_store_b64(hp[6], ra, dst + (g & 1) * 8, 6 * 256, 16);
            {
                if (ip == 0) {
                    if (t < 6) { p2_dma_own(P, 2 + (t >> 1), 4 * (t >> 1), 2 * (t & 1)); p2_dma_own(P, 2 + (t >> 1), 4 * (t >> 1), 2 * (t & 1) + 1); }
                } else if (t < 5) p2_dma2(P, 5 + t, t);
            }
            SB();
        }
        if (ip == 0) {
            asm volatile("s_waitcnt vmcnt(1)" ::: "memory");
            p2_to_regs(P, L, wr[0], 2); p2_to_regs(P, L, wr[1], 6); p2_to_regs(P, L, wr[2], 0); p2_to_regs(P, L, wr[3], 4); p2_to_regs(P, L, wr[4], 8);
            WAITL0();
            SB();
        }
    }
    STAMP(6)
    WAIT_BAR0();
    if (tid == 0) __hip_atomic_store((gu64*)(ws + FLAG_OFF + (size_t)(rb * 32 + 16 + cb) * 64), tag, RLX_AGENT);
#pragma unroll
    for (int k = 5; k < 10; ++k) p2_dma2(P, 5 + k, k);
    STAMP(7)
    return tag;
}

constexpr int AT_WM = 4096, AT_WS = 3 * AT_WM, AT_W = 131072;
constexpr int AT_K = 98304, AT_V = AT_K + 32768;

__device__ __forceinline__ unsigned long long att_block(const char* x, const char* wqkv, char* ws, int ch, int head, lds_ptr L,
                                                        P2& P, bf16x8 (&wr)[5][4] STAMP_ARG) {
    const int tid = threadIdx.x, lane = tid & 63;
    const int w = __builtin_amdgcn_readfirstlane(tid >> 6);
    const unsigned lb = (unsigned)(size_t)L;
    const int g = lane >> 4, q = (lane >> 2) & 3, p = lane & 3, tk = lane & 15;
    unsigned long long ep; u32x4 rv;
    const int srow = ch * 256 + head * 8 + w;
    start_loads(x, ws, srow, lane, ep, rv);
    const unsigned wo = (lane >> 2) * (QKV_ * 2) + (((lane & 3) ^ (((lane >> 5) & 1) << 1)) << 4);
    const char* wsrc0 = wqkv + (w & 3) * (16 * QKV_ * 2) + (w >> 2) * 896 + head * 56;
    const char* wsrc1 = wqkv + (w & 3) * (16 * QKV_ * 2) + 2 * 896 + head * 56 - 8;
    auto dma_w = [&](int c) {
        glds16s(wsrc0 + c * (64 * QKV_ * 2), wo, lb + AT_W + (c & 1) * AT_WS + w * 1024);
        if (w < 4) glds16s(wsrc1 + c * (64 * QKV_ * 2), wo, lb + AT_W + (c & 1) * AT_WS + 8192 + w * 1024);
    };
    const int orow = ch * 256 + 32 * w;
    const unsigned xo0 = (lane >> 3) * 896 + (((lane & 7) ^ (lane >> 4)) << 4), xo1 = xo0 ^ 64;
    const char* xs = x + (size_t)orow * 896;
    auto dma_x = [&](int c, int i) {
        const char* src = xs + i * (16 * 896) + c * 128;
        glds16s(src, xo0, lb + (c & 3) * 32768 + w * 4096 + i * 2048);
        glds16s(src + 8 * 896, xo1, lb + (c & 3) * 32768 + w * 4096 + i * 2048 + 1024);
    };
    dma_w(0); dma_x(0, 0); dma_x(0, 1);
    const int cblk = (ch * 16 + head) < 112 ? ch * 16 + head : 111;
    auto dma_cs = [&]() {
        const char* cp = (cblk < 56 ? P.wg + cblk * 64 : P.wu + (cblk - 56) * 64);
        const unsigned co = (lane >> 2) * (F_ * 2) + (((lane & 3) ^ (((lane >> 5) & 1) << 1)) << 4);
#pragma unroll
        for (int j = 0; j < 4; ++j) {
            const int ks = j < 2 ? w : (w < 6 ? w + 8 : 13);
            glds16s(cp + (size_t)(ks * 32 + (j & 1) * 16) * (F_ * 2), co, lb + 98304 + w * 4096 + j * 1024);
        }
    };
    dma_w(1); dma_x(1, 0); dma_x(1, 1); dma_x(2, 0); dma_x(2, 1);
    p2_touch(P, w, lane, lb);
    asm volatile("s_waitcnt vmcnt(15)" : "+v"(ep), "+v"(rv) :: "memory");
    const unsigned long long tag = publish_stats(ws, srow, lane, ep, rv);

    const int xa0 = w * 4096 + tk * 128 + ((g ^ ((lane >> 1) & 7)) << 4), xa1 = xa0 ^ 64;
    const int wA0 = AT_W + (8 * g + q) * 64 + ((g & 1) << 5) + p * 8, wA1 = wA0 ^ 32;
    const int cidx = w > 5 ? 5 : w;
    const int wC = (cidx >> 1) * AT_WM + (wA0 ^ ((cidx & 1) << 5));
    f32x4 aK[2][2], aV[2][2], aQ[2][2], cs = f32x4{0.f, 0.f, 0.f, 0.f};
#pragma unroll
    for (int i = 0; i < 2; ++i) aK[i][0] = aK[i][1] = aV[i][0] = aV[i][1] = aQ[i][0] = aQ[i][1] = f32x4{0.f, 0.f, 0.f, 0.f};
    const bf16x8 ones = __builtin_bit_cast(bf16x8, u32x4{0x3F803F80u, 0x3F803F80u, 0x3F803F80u, 0x3F803F80u});
    bf16x8 wf[2][3][2], cf[2], xf[2][2];
#define AWRD(B, ks) { _Pragma("unroll") for (int m = 0; m < 3; ++m) { wf[ks][m][0] = lds_tr2((B) + wA0 + m * AT_WM + (ks) * 2048, 256); \
                      wf[ks][m][1] = lds_tr2((B) + wA1 + m * AT_WM + (ks) * 2048, 256); } cf[ks] = lds_tr2((B) + wC + (ks) * 2048, 256); }
#define AXRD(c, i) { xf[i][0] = lds_b128(L + xa0 + ((c) & 3) * 32768 + (i) * 2048); xf[i][1] = lds_b128(L + xa1 + ((c) & 3) * 32768 + (i) * 2048); }
#define AMM(i, ks) { _Pragma("unroll") for (int t = 0; t < 2; ++t) { \
                       aK[i][t] = MFMA16(wf[ks][1][t], xf[i][ks], aK[i][t]); aV[i][t] = MFMA16(xf[i][ks], wf[ks][2][t], aV[i][t]); \
                       aQ[i][t] = MFMA16(wf[ks][0][t], xf[i][ks], aQ[i][t]); } }
    asm volatile("s_waitcnt vmcnt(%0)\n\ts_barrier" :: "i"(11) : "memory");
    STAMP(1)
    AXRD(0, 0) AWRD(L, 0) AWRD(L, 1)
    SB();
    const int strow = orow + (lane & 31);
    unsigned long long sreq;
#pragma unroll
    for (int c = 0; c < 7; ++c) {
        AXRD(c, 1)
        AMM(0, 0)
        cs = MFMA16(cf[0], ones, cs);
        AMM(0, 1)
        cs = MFMA16(cf[1], ones, cs);
        SB();
        if (c + 3 < 7) dma_x(c + 3, 0);
        if (c == 4) stats_request(ws, strow, sreq);
        if (c < 6) {
            asm volatile("s_waitcnt vmcnt(%0)\n\ts_barrier" :: "i"(c == 0 ? 8 : c < 4 ? 4 : c == 4 ? 7 : 0) : "memory");
            AXRD(c + 1, 0)
            if (c + 2 < 7) dma_w(c + 2);
            if (c == 5)
    {
        lds_ptr C = L + 98304 + w * 4096 - AT_W;
        const f32x4 z4 = f32x4{0.f, 0.f, 0.f, 0.f};
        f32x4 p0 = MFMA16(lds_tr2(C + wA0, 256), ones, z4), p1 = MFMA16(lds_tr2(C + wA1, 256), ones, z4);
        const bf16x8 f0 = lds_tr2(C + wA0 + 2048, 256), f1 = lds_tr2(C + wA1 + 2048, 256);
        if (w < 6) { p0 = MFMA16(f0, ones, p0); p1 = MFMA16(f1, ones, p1); }
        if (tk == 0) { LDSV(f32x4, L + w * 4096 + 1024 + g * 16) = p0; LDSV(f32x4, L + w * 4096 + 1088 + g * 16) = p1; }
    }
        }
        AMM(1, 0)
        SB();
        if (c < 6) AWRD(L + ((c + 1) & 1) * AT_WS, 0)
        AMM(1, 1)
        SB();
        if (c < 6) AWRD(L + ((c + 1) & 1) * AT_WS, 1)
        if (c + 3 < 7) dma_x(c + 3, 1);
        if (c == 3) dma_cs();
        SB();
    }
    asm volatile("" : "+v"(sreq));
    STAMP(4)
    lds_ptr ST = L + w * 4096;
    if (tk == 0) LDSV(f32x4, ST + 512 + g * 16) = cs;
    {
        const unsigned sv = stats_check(ws, strow, tag, sreq);
        LDSF(ST + lane * 4) = -__uint_as_float(sv & 0xFFFF0000u);
        LDSF(ST + 256 + lane * 4) = __builtin_amdgcn_rcpf(__uint_as_float(sv << 16));
    }
    LBAR();
    if (w == 7) {
        float t = 0.f;
#pragma unroll
        for (int k = 0; k < 8; ++k) t += LDSF(L + k * 4096 + 1024 + (lane & 31) * 4);
        if (lane < 32 && ch * 16 + head < 112)
            __hip_atomic_store((gu64*)(ws + STAT_OFF) + 2048 + cblk * 32 + lane,
                               ((unsigned long long)(unsigned)tag << 32) | __float_as_uint(t), RLX_AGENT);
    }
    bf16x8 qf[2];
    const bool pad = (lane >= 48);
    const __amdgpu_buffer_rsrc_t rk = __builtin_amdgcn_make_buffer_rsrc(ws + KV_OFF, (short)0, 128 * 32768, 0x00020000);
    {
        const unsigned kvo = (ch * 16 + head) * 32768 + w * 2048 + lane * 16;
        f32x4 cq[2], ck[2];
        float cv[2];
#pragma unroll
        for (int t = 0; t < 2; ++t) {
            cq[t] = lds_f4(L + 512 + t * 4096 + g * 16);
            ck[t] = lds_f4(L + 512 + (2 + t) * 4096 + g * 16);
            cv[t] = LDSF(L + 512 + (4 + t) * 4096 + tk * 4);
        }
#define CORR(A, C) u32x4{pk2(fma2(sp2(nm), lo2(C[0]), lo2(A[0])) * sp2(rs)), pk2(fma2(sp2(nm), hi2(C[0]), hi2(A[0])) * sp2(rs)), \
                         pk2(fma2(sp2(nm), lo2(C[1]), lo2(A[1])) * sp2(rs)), pk2(fma2(sp2(nm), hi2(C[1]), hi2(A[1])) * sp2(rs))}
#pragma unroll
        for (int i = 0; i < 2; ++i) {
            const float nm = LDSF(ST + i * 64 + tk * 4), rs = LDSF(ST + 256 + i * 64 + tk * 4);
            u32x4 kv = CORR(aK[i], ck);
            kv[2] = pad ? 0u : kv[2];
            kv[3] = pad ? 0u : kv[3];
            __builtin_amdgcn_raw_buffer_store_b128(kv, rk, kvo, i * 1024, 16);
            LDSV(u32x4, L + AT_K + (16 + 2 * w + i) * 1024 + lane * 16) = kv;
        }
        f32x4 s0[2], s1[2];
#pragma unroll
        for (int u = 0; u < 2; ++u) {
            s0[u] = lds_f4(ST + u * 64 + g * 16);
            s1[u] = lds_f4(ST + 256 + u * 64 + g * 16);
        }
#pragma unroll
        for (int t = 0; t < 2; ++t) {
            u32x4 o;
#pragma unroll
            for (int u = 0; u < 2; ++u) {
                o[2 * u] = pk2(fma2(lo2(s0[u]), sp2(cv[t]), lo2(aV[u][t])) * lo2(s1[u]));
                o[2 * u + 1] = pk2(fma2(hi2(s0[u]), sp2(cv[t]), hi2(aV[u][t])) * hi2(s1[u]));
            }
            if (t == 0 && tk == 0) o = u32x4{0x3F803F80u, 0x3F803F80u, 0x3F803F80u, 0x3F803F80u};
            __builtin_amdgcn_raw_buffer_store_b128(o, rk, kvo, 16384 + t * 1024, 16);
            LDSV(u32x4, L + AT_V + ((8 + w) * 2 + t) * 1024 + lane * 16) = o;
        }
#pragma unroll
        for (int i = 0; i < 2; ++i) {
            const float nm = LDSF(ST + i * 64 + tk * 4), rs = LDSF(ST + 256 + i * 64 + tk * 4);
            u32x4 qv = CORR(aQ[i], cq);
            qv[2] = pad ? 0x3F80u : qv[2];
            qv[3] = pad ? 0u : qv[3];
            qf[i] = __builtin_bit_cast(bf16x8, qv);
        }
    }
    asm volatile("s_waitcnt vmcnt(0)" ::: "memory");
    if (lane == 0) __hip_atomic_store((gu64*)(ws + FLAG_OFF + (size_t)(256 + (ch * 16 + head) * 8 + w) * 64), tag, RLX_AGENT);
    LBAR();
    STAMP(3)
    p2_lane(P);
    p2_dma_own4(P, 0, 0); p2_dma_own4(P, 1, 2); p2_dma_own4(P, 2, 4);
    {
        u32x4 hk[2], hv[2];
        if (ch) {
            wait_flags(ws, 256 + ((ch - 1) * 16 + head) * 8 + w, 1, lane, tag);
            const unsigned kvo = ((ch - 1) * 16 + head) * 32768 + w * 2048 + lane * 16;
#pragma unroll
            for (int i = 0; i < 2; ++i) {
                hk[i] = __builtin_amdgcn_raw_buffer_load_b128(rk, kvo, i * 1024, 16);
                hv[i] = __builtin_amdgcn_raw_buffer_load_b128(rk, kvo, 16384 + i * 1024, 16);
            }
        } else {
            hk[0] = hk[1] = u32x4{0u, 0u, pad ? 0xF149u : 0u, 0u};
            hv[0] = hv[1] = u32x4{0u, 0u, 0u, 0u};
        }
#pragma unroll
        for (int i = 0; i < 2; ++i) {
            LDSV(u32x4, L + AT_K + (2 * w + i) * 1024 + lane * 16) = hk[i];
            LDSV(u32x4, L + AT_V + (w * 2 + i) * 1024 + lane * 16) = hv[i];
        }
    }
    asm volatile("s_waitcnt vmcnt(0)" ::: "memory");
    p2_to_regs(P, L, wr[0], 0); p2_to_regs(P, L, wr[1], 2); p2_to_regs(P, L, wr[2], 4);
    LBAR();
    STAMP(5)
    const float NEG = -1e30f;
    f32x4 mle, mgt;
#pragma unroll
    for (int r = 0; r < 4; ++r) { mle[r] = (4 * g + r <= tk) ? 0.f : NEG; mgt[r] = (4 * g + r > tk) ? 0.f : NEG; }
    f32x4 Sc[2][18];
#pragma unroll
    for (int j = 0; j < 18; ++j) {
        const bf16x8 kf = lds_b128(L + AT_K + (2 * w + j) * 1024 + lane * 16);
#pragma unroll
        for (int qt = 0; qt < 2; ++qt) {
            if (j >= qt && j <= qt + 16) {
                f32x4 c0;
                if (j == qt + 16) c0 = mle;
                else if (j == qt) c0 = mgt;
                else c0 = f32x4{0.f, 0.f, 0.f, 0.f};
                Sc[qt][j] = MFMA16(kf, qf[qt], c0);
            } else {
                Sc[qt][j] = f32x4{0.f, 0.f, 0.f, 0.f};
            }
        }
        if (j < 8) p2_dma_own(P, 3 + (j >> 2), 2 * (j >> 2), j & 3);
        else if (j < 12) p2_dma(P, 9 + ((j - 8) >> 1), 4 + ((j - 8) >> 1), j & 1);
    }
    const float CS = 0.18898223650461362f * 1.4426950408889634f;
    u32x4 pf[2][9];
#pragma unroll
    for (int qt = 0; qt < 2; ++qt) {
        float m = NEG, m2 = NEG;
#pragma unroll
        for (int j = qt; j <= qt + 16; ++j) { m = max3f(m, Sc[qt][j][0], Sc[qt][j][1]); m2 = max3f(m2, Sc[qt][j][2], Sc[qt][j][3]); }
        m = max3f(m, m2, m2);
        m = xmax(m);
        const float mc = -m * CS;
#pragma unroll
        for (int j = qt; j <= qt + 16; ++j) {
            const f32x2 a = fma2(lo2(Sc[qt][j]), sp2(CS), sp2(mc)), b = fma2(hi2(Sc[qt][j]), sp2(CS), sp2(mc));
            Sc[qt][j] = f32x4{__builtin_amdgcn_exp2f(a[0]), __builtin_amdgcn_exp2f(a[1]), __builtin_amdgcn_exp2f(b[0]), __builtin_amdgcn_exp2f(b[1])};
            if (qt == 1 && (j & 1) == 0) { p2_dma(P, 5 + ((j - 2) >> 2), (j - 2) >> 2, ((j - 2) >> 1) & 1); SB(); }
        }
#pragma unroll
        for (int b = 0; b < 9; ++b)
            pf[qt][b] = u32x4{pk2(Sc[qt][2 * b][0], Sc[qt][2 * b][1]), pk2(Sc[qt][2 * b][2], Sc[qt][2 * b][3]),
                              pk2(Sc[qt][2 * b + 1][0], Sc[qt][2 * b + 1][1]), pk2(Sc[qt][2 * b + 1][2], Sc[qt][2 * b + 1][3])};
        if (qt == 0) {
            SB();
            asm volatile("s_waitcnt vmcnt(0)" ::: "memory");
            p2_to_regs(P, L, wr[3], 0); p2_to_regs(P, L, wr[4], 2);
            WAITL0();
            SB();
        }
    }
    f32x4 O[2][2];
    O[0][0] = O[0][1] = O[1][0] = O[1][1] = f32x4{0.f, 0.f, 0.f, 0.f};
#pragma unroll
    for (int b = 0; b < 9; ++b)
#pragma unroll
        for (int t = 0; t < 2; ++t) {
            const bf16x8 vf = lds_b128(L + AT_V + ((w + b) * 2 + t) * 1024 + lane * 16);
#pragma unroll
            for (int qt = 0; qt < 2; ++qt) O[qt][t] = MFMA16(vf, __builtin_bit_cast(bf16x8, pf[qt][b]), O[qt][t]);
        }
    const __amdgpu_buffer_rsrc_t ra = __builtin_amdgcn_make_buffer_rsrc(ws, (short)0, ACT_BYTES, 0x00020000);
#pragma unroll
    for (int qt = 0; qt < 2; ++qt) {
        const float l = __shfl(O[qt][0][0], tk);
        const float inv = __builtin_amdgcn_rcpf(l);
        const unsigned dst = (ch * 16 + 2 * w + qt) * ACT_TILE_B + 224 * 256 + tk * 16;
#pragma unroll
        for (int t = 0; t < 2; ++t) {
            const int u = 7 * head + 4 * t + g - 1;
            if (t == 1 || g > 0)
                __builtin_amdgcn_raw_buffer_store_b64(
                    u32x2{pk2(O[qt][t][0] * inv, O[qt][t][1] * inv), pk2(O[qt][t][2] * inv, O[qt][t][3] * inv)}, ra,
                    dst + (u >> 1) * 256 + (u & 1) * 8, 0, 16);
        }
    }
    STAMP(6)
    WAIT_BAR0();
    if (tid == 0) __hip_atomic_store((gu64*)(ws + FLAG_OFF + (size_t)(ch * 32 + head) * 64), tag, RLX_AGENT);
#pragma unroll
    for (int k = 6; k < 10; ++k) p2_dma2(P, 5 + k, k);
    STAMP(7)
    return tag;
}

constexpr int LDS_BYTES = 163840;

__global__ __launch_bounds__(512, 2) void k_fused(const char* x, const char* wqkv, const char* wo, const char* wg, const char* wu,
                                                  const char* wd, char* out, char* ws) {
    extern __shared__ __attribute__((aligned(16))) char smem[];
    lds_ptr L = (lds_ptr)smem;
    const unsigned lb = (unsigned)(size_t)L;
    STAMP_DECL STAMP(0)
    const int tid = threadIdx.x, lane = tid & 63;
    const int w = __builtin_amdgcn_readfirstlane(tid >> 6);
    const int xcd = blockIdx.x & 7, idx = blockIdx.x >> 3;
    const int rblk = 4 * (xcd & 1) + (idx & 3), cblk = 4 * (xcd >> 1) + ((idx >> 2) & 3);
    if (w == 7) {
        unsigned long long pc;
        asm volatile("s_getpc_b64 %0" : "=s"(pc));
        glds16s((const char*)(pc & ~255ull), idx * 1024 + lane * 16, lb + 162816);
        if (idx < 15) glds16s((const char*)(pc & ~255ull), 32768 + idx * 1024 + lane * 16, lb + 162816);
    }
    const int th = w & 1, kg = w >> 1;
    const bool has_p2 = idx >= 4;
    const int rb = 4 * xcd + (idx & 3), cb = has_p2 ? (idx >> 2) - 1 : 0;
    const int a1 = kg ? 14 * (kg - 1) : 56;
    const int a2 = 42 + (kg < 2 ? 3 * kg : 4 * kg - 2);
    P2 P;
    P.n2 = kg < 2 ? 3 : 4;
    P.w1 = (kg ? wd + (size_t)(14 * (kg - 1)) * 32 * 896 : wo) + cb * 128 + th * 16 * 896;
    P.w2 = wd + (size_t)a2 * 32 * 896 + cb * 128 + th * 16 * 896;
    P.ring = lb + kg * 4096 + th * 2048;
    P.th = th; P.kg = kg; P.wd = wd; P.wo = wo; P.wg = wg; P.wu = wu;
    bf16x8 wr[5][4];
    unsigned long long tag;
    if (idx < 16) tag = att_block(x, wqkv, ws, rblk, cblk, L, P, wr STAMP_PASS);
    else tag = ffn_block(x, wg, wu, ws, rblk, cblk, L, P, wr STAMP_PASS);
    if (!has_p2) {
        asm volatile("s_waitcnt vmcnt(0)" ::: "memory");
        if (blockIdx.x == 0 && w == 0) {
#pragma unroll 1
            for (int i = 0; i < 4; ++i) wait_flags(ws, 64 * i, 64, lane, tag);
            if (lane == 0) __hip_atomic_store((gu64*)(ws + SYNC_OFF), tag, RLX_AGENT);
        }
        STAMP_FLUSH(ws, 0, 8)
        return;
    }
    int ln = lane;
    asm volatile("" : "+v"(ln));
    const int g = ln >> 4, tk = ln & 15;
    const size_t oo = (size_t)(rb * 64 + 32 * th + tk) * 896 + cb * 128 + kg * 32 + g * 8;
    const u32x2 xr0 = *(const u32x2*)(x + oo), xr1 = *(const u32x2*)(x + oo + 16 * 896);
    STAMP(8)
    if (w == 0) wait_flags(ws, (rb >> 2) * 32, 32, lane, tag);
    asm volatile("s_barrier" ::: "memory");
    STAMP(9)
    const __amdgpu_buffer_rsrc_t ra = __builtin_amdgcn_make_buffer_rsrc(ws, (short)0, ACT_BYTES, 0x00020000);
    const unsigned ab = (rb * 4 + 2 * th) * ACT_TILE_B + lane * 16;
    bf16x8 af[18][2];
#define ALD(u) { const int ks_ = (u) < 14 ? a1 + (u) : a2 + ((u) - 14 < P.n2 ? (u) - 14 : P.n2 - 1);                                      \
                 af[u][0] = __builtin_bit_cast(bf16x8, __builtin_amdgcn_raw_buffer_load_b128(ra, ab, ks_ * 1024, 16));                    \
                 af[u][1] = __builtin_bit_cast(bf16x8, __builtin_amdgcn_raw_buffer_load_b128(ra, ab + ACT_TILE_B, ks_ * 1024, 16)); SB(); }
#pragma unroll
    for (int u = 0; u < 5; ++u) ALD(u)
    f32x4 c1[2][4], c2[2][4];
#pragma unroll
    for (int i = 0; i < 2; ++i)
#pragma unroll
        for (int t = 0; t < 4; ++t) c1[i][t] = c2[i][t] = f32x4{0.f, 0.f, 0.f, 0.f};
#define MM8(ACC, WF, u) { _Pragma("unroll") for (int t = 0; t < 4; ++t) {            \
            ACC[0][t] = MFMA16(WF[t], af[u][0], ACC[0][t]);                          \
            ACC[1][t] = MFMA16(WF[t], af[u][1], ACC[1][t]); } }
#define STEP(ACC, u, SLOT) { bf16x8 wf[4]; _Pragma("unroll") for (int t = 0; t < 4; ++t) wf[t] = P2_WRD(t, SLOT); MM8(ACC, wf, u) }
    STAMP(10)
#pragma unroll
    for (int u = 0; u < 5; ++u) {
        MM8(c1, wr[u], u)
        SB();
        ALD(u + 5)
    }
    asm volatile("s_barrier" ::: "memory");
    STAMP(11)
#pragma unroll
    for (int u = 5; u < 8; ++u) { STEP(c1, u, u - 5) SB(); ALD(u + 5) }
    LBAR();
#pragma unroll
    for (int u = 8; u < 14; ++u) {
        STEP(c1, u, u - 5)
        SB();
        if (u < 13) ALD(u + 5)
        if (u < 11) p2_dma2(P, u + 7, u - 8);
    }
    STAMP(12)
    STEP(c2, 14, 9)
    SB();
    WAIT_BAR0();
    STAMP(13)
    if (kg == 0) {
#pragma unroll
        for (int i = 0; i < 2; ++i)
#pragma unroll
            for (int t = 0; t < 4; ++t) LDSV(f32x4, L + 131072 + th * 8192 + (i * 4 + t) * 1024 + lane * 16) = c1[i][t];
    } else {
#pragma unroll
        for (int i = 0; i < 2; ++i)
#pragma unroll
            for (int t = 0; t < 4; ++t) c2[i][t] += c1[i][t];
    }
    STEP(c2, 15, 0) STEP(c2, 16, 1)
    if (P.n2 == 4) STEP(c2, 17, 2)
    STAMP(14)
    f32x4 fs[2];
#define PUT(K)                                                                                             \
    {                                                                                                      \
        _Pragma("unroll") for (int i = 0; i < 2; ++i) {                                                    \
            _Pragma("unroll") for (int t = 0; t < 4; ++t)                                                  \
                if (t != K) LDSV(f32x4, L + 49152 + w * 8192 + (i * 4 + t) * 1024 + lane * 16) = c2[i][t]; \
            fs[i] = c2[i][K];                                                                              \
        }                                                                                                  \
    }
    if (kg == 0) PUT(0) else if (kg == 1) PUT(1) else if (kg == 2) PUT(2) else PUT(3)
    LBAR();
#pragma unroll
    for (int i = 0; i < 2; ++i) {
#pragma unroll
        for (int k = 1; k < 4; ++k) fs[i] += lds_f4(L + 49152 + (2 * ((kg + k) & 3) + th) * 8192 + (i * 4 + kg) * 1024 + lane * 16);
        const f32x4 sv = lds_f4(L + 131072 + th * 8192 + (i * 4 + kg) * 1024 + lane * 16);
        const u32x2 xr = i ? xr1 : xr0;
        u32x2 o;
#pragma unroll
        for (int u = 0; u < 2; ++u) {
            const f32x2 fv = u ? hi2(fs[i]) : lo2(fs[i]), z = fv * sp2(-1.4426950408889634f);
            const f32x2 d = f32x2{__builtin_amdgcn_exp2f(z[0]), __builtin_amdgcn_exp2f(z[1])} + sp2(1.0f);
            const f32x2 gt = fv * f32x2{__builtin_amdgcn_rcpf(d[0]), __builtin_amdgcn_rcpf(d[1])};
            const f32x2 xv = f32x2{__uint_as_float(xr[u] << 16), __uint_as_float(xr[u] & 0xFFFF0000u)};
            o[u] = pk2(fma2(u ? hi2(sv) : lo2(sv), gt, xv));
        }
        *(u32x2*)(out + oo + (size_t)i * 16 * 896) = o;
    }
    STAMP(15)
    STAMP_FLUSH(ws, 0, 16)
}

extern "C" void kernel_launch(void* const* d_in, const int* in_sizes, int n_in, void* d_out, int out_size, void* d_ws,
                              size_t ws_size, hipStream_t stream) {
    (void)hipFuncSetAttribute((const void*)k_fused, hipFuncAttributeMaxDynamicSharedMemorySize, LDS_BYTES);
    k_fused<<<256, 512, LDS_BYTES, stream>>>((const char*)d_in[0], (const char*)d_in[1], (const char*)d_in[2], (const char*)d_in[4],
                                             (const char*)d_in[5], (const char*)d_in[6], (char*)d_out, (char*)d_ws);
}
```

```cpp
#include <hip/hip_runtime.h>
#include <hip/hip_bf16.h>

typedef short bf16x8 __attribute__((ext_vector_type(8)));
typedef short s16x4 __attribute__((ext_vector_type(4)));
typedef float f32x4 __attribute__((ext_vector_type(4)));
typedef float f32x2 __attribute__((ext_vector_type(2)));
typedef unsigned u32x4 __attribute__((ext_vector_type(4)));
typedef unsigned u32x2 __attribute__((ext_vector_type(2)));
typedef __bf16 bf16x2_t __attribute__((ext_vector_type(2)));
typedef __attribute__((address_space(3))) char* lds_ptr;

#define MFMA16(a, b, c) __builtin_amdgcn_mfma_f32_16x16x32_bf16(a, b, c, 0, 0, 0)
#define WAIT_BAR0() asm volatile("s_waitcnt vmcnt(0) lgkmcnt(0)\n\ts_barrier" ::: "memory")
#define LBAR() asm volatile("s_waitcnt lgkmcnt(0)\n\ts_barrier" ::: "memory")

constexpr int S_ = 2048, D_ = 448, F_ = 1792, QKV_ = 1344;
constexpr int ACT_OCT = 280;
constexpr int ACT_TILE_B = ACT_OCT * 256 + 256;

__device__ __forceinline__ void glds16s(const void* sbase, unsigned voff, unsigned lds_dst) {
    unsigned sv;
    asm volatile("s_mov_b32 %0, m0\n\ts_mov_b32 m0, %3\n\ts_nop 0\n\tglobal_load_lds_dwordx4 %1, %2\n\ts_mov_b32 m0, %0"
                 : "=&s"(sv) : "v"(voff), "s"(sbase), "s"(lds_dst) : "memory");
}
__device__ __forceinline__ bf16x8 lds_b128(lds_ptr p) { return *(const __attribute__((address_space(3))) bf16x8*)p; }
__device__ __forceinline__ f32x4 lds_f4(lds_ptr p) { return *(const __attribute__((address_space(3))) f32x4*)p; }
__device__ __forceinline__ s16x4 lds_tr(lds_ptr p) {
    return __builtin_amdgcn_ds_read_tr16_b64_v4i16((__attribute__((address_space(3))) s16x4*)p);
}
__device__ __forceinline__ bf16x8 lds_tr2(lds_ptr p, int second) {
    s16x4 a = lds_tr(p), b = lds_tr(p + second);
    return __builtin_shufflevector(a, b, 0, 1, 2, 3, 4, 5, 6, 7);
}
__device__ __forceinline__ float dot2(unsigned a, unsigned b, float c) {
    return __builtin_amdgcn_fdot2_f32_bf16(__builtin_bit_cast(bf16x2_t, a), __builtin_bit_cast(bf16x2_t, b), c, false);
}
__device__ __forceinline__ float frag_sum(bf16x8 f, float c) {
    u32x4 u = __builtin_bit_cast(u32x4, f);
    c = dot2(u[0], 0x3F803F80u, c); c = dot2(u[1], 0x3F803F80u, c);
    c = dot2(u[2], 0x3F803F80u, c); c = dot2(u[3], 0x3F803F80u, c);
    return c;
}
__device__ __forceinline__ float frag_sq(bf16x8 f, float c) {
    u32x4 u = __builtin_bit_cast(u32x4, f);
    c = dot2(u[0], u[0], c); c = dot2(u[1], u[1], c); c = dot2(u[2], u[2], c); c = dot2(u[3], u[3], c);
    return c;
}
__device__ __forceinline__ float xsum(float x) {
    auto s = __builtin_amdgcn_permlane16_swap(__float_as_uint(x), __float_as_uint(x), false, false);
    x = __uint_as_float(s[0]) + __uint_as_float(s[1]);
    auto t = __builtin_amdgcn_permlane32_swap(__float_as_uint(x), __float_as_uint(x), false, false);
    return __uint_as_float(t[0]) + __uint_as_float(t[1]);
}
__device__ __forceinline__ float max3f(float a, float b, float c) { return __builtin_fmaxf(__builtin_fmaxf(a, b), c); }
__device__ __forceinline__ float xmax(float x) {
    auto s = __builtin_amdgcn_permlane16_swap(__float_as_uint(x), __float_as_uint(x), false, false);
    x = fmaxf(__uint_as_float(s[0]), __uint_as_float(s[1]));
    auto t = __builtin_amdgcn_permlane32_swap(__float_as_uint(x), __float_as_uint(x), false, false);
    return fmaxf(__uint_as_float(t[0]), __uint_as_float(t[1]));
}
__device__ __forceinline__ unsigned pk2(float lo, float hi) {
    bf16x2_t v; v[0] = (__bf16)lo; v[1] = (__bf16)hi;
    return __builtin_bit_cast(unsigned, v);
}
__device__ __forceinline__ unsigned pk2(f32x2 v) { return pk2(v[0], v[1]); }
__device__ __forceinline__ f32x2 lo2(f32x4 v) { return __builtin_shufflevector(v, v, 0, 1); }
__device__ __forceinline__ f32x2 hi2(f32x4 v) { return __builtin_shufflevector(v, v, 2, 3); }
__device__ __forceinline__ f32x2 sp2(float v) { return f32x2{v, v}; }
__device__ __forceinline__ f32x2 fma2(f32x2 a, f32x2 b, f32x2 c) { return __builtin_elementwise_fma(a, b, c); }
__device__ __forceinline__ float rbf(float x) { return (float)(__bf16)x; }
__device__ __forceinline__ void ln_stats(float sum, float sq, float& mean, float& den) {
    float m = sum * (1.0f / D_);
    float v = fmaxf(sq * (1.0f / D_) - m * m, 0.0f);
    mean = rbf(m);
    den = rbf(sqrtf(rbf(rbf(v) + 1e-5f)));
}


typedef __attribute__((address_space(1))) unsigned long long gu64;
#define RLX_AGENT __ATOMIC_RELAXED, __HIP_MEMORY_SCOPE_AGENT
#define SB() __builtin_amdgcn_sched_barrier(0)
#define LDSF(p) (*(__attribute__((address_space(3))) float*)(p))
#define LDSV(T, p) (*(__attribute__((address_space(3))) T*)(p))
constexpr size_t SYNC_OFF = 16u << 20, FLAG_OFF = SYNC_OFF + 4096, STAT_OFF = SYNC_OFF + 131072, KV_OFF = 20u << 20;
constexpr int ACT_BYTES = 128 * ACT_TILE_B;
constexpr unsigned long long SPIN_TICKS = 20000000ull;


template <int N> __device__ __forceinline__ float ror_add(float x) {
    return x + __uint_as_float(__builtin_amdgcn_update_dpp(0u, __float_as_uint(x), 0x120 + N, 0xF, 0xF, false));
}
__device__ __forceinline__ float wave_sum(float x) { return xsum(ror_add<1>(ror_add<2>(ror_add<4>(ror_add<8>(x))))); }

__device__ __forceinline__ void start_loads(const char* x, char* ws, int row, int lane, unsigned long long& ep, u32x4& rv) {
    const unsigned zero = 0, ro = (lane < 56 ? lane : 55) * 16;
    asm volatile("global_load_dwordx2 %0, %1, %2 sc1" : "=v"(ep) : "v"(zero), "s"(ws + SYNC_OFF) : "memory");
    asm volatile("global_load_dwordx4 %0, %1, %2" : "=v"(rv) : "v"(ro), "s"(x + (size_t)row * 896) : "memory");
}
__device__ __forceinline__ unsigned long long publish_stats(char* ws, int row, int lane, unsigned long long ep, u32x4 rv) {
    const unsigned long long tag = (((unsigned long long)__builtin_amdgcn_readfirstlane((unsigned)(ep >> 32)) << 32) |
                                    (unsigned)__builtin_amdgcn_readfirstlane((unsigned)ep)) + 1ull;
    if (lane >= 56) rv = u32x4{0u, 0u, 0u, 0u};
    const bf16x8 f = __builtin_bit_cast(bf16x8, rv);
    float mean, den;
    ln_stats(wave_sum(frag_sum(f, 0.f)), wave_sum(frag_sq(f, 0.f)), mean, den);
    const unsigned val = (__float_as_uint(mean) & 0xFFFF0000u) | (__float_as_uint(den) >> 16);
    if (lane == 0) __hip_atomic_store((gu64*)(ws + STAT_OFF) + row, ((unsigned long long)(unsigned)tag << 32) | val, RLX_AGENT);
    return tag;
}
__device__ __forceinline__ void stats_request(char* ws, int row, unsigned long long& v) {
    asm volatile("global_load_dwordx2 %0, %1, %2 sc1" : "=v"(v) : "v"(row * 8), "s"(ws + STAT_OFF) : "memory");
}
__device__ __forceinline__ unsigned stats_check(char* ws, int row, unsigned long long tag, unsigned long long v) {
    const unsigned long long t0 = __builtin_amdgcn_s_memrealtime();
    while (!__all((unsigned)(v >> 32) == (unsigned)tag)) {
        if (__builtin_amdgcn_s_memrealtime() - t0 > SPIN_TICKS) break;
        __builtin_amdgcn_s_sleep(2);
        v = __hip_atomic_load((gu64*)(ws + STAT_OFF) + row, RLX_AGENT);
    }
    return (unsigned)v;
}
__device__ __forceinline__ void wait_flags(char* ws, int i, unsigned long long tag) {
    const unsigned long long t0 = __builtin_amdgcn_s_memrealtime();
    for (;;) {
        const unsigned long long v = __hip_atomic_load((gu64*)(ws + FLAG_OFF + (size_t)i * 64), RLX_AGENT);
        if (__all(v == tag)) break;
        if (__builtin_amdgcn_s_memrealtime() - t0 > SPIN_TICKS) break;
        __builtin_amdgcn_s_sleep(1);
    }
}

struct P2 {
    const char* wd; const char* wo; const char* wg; const char* wu;
    const char* w1; const char* w2; unsigned ring; int n2, th, kg;
    unsigned wv0; int wa[4], own;
};
__device__ __forceinline__ void p2_lane(P2& P) {
    int lane = threadIdx.x & 63;
    asm volatile("" : "+v"(lane));
    const int g = lane >> 4, q = (lane >> 2) & 3, p = lane & 3, f = ((q >> 1) & 1) | ((g & 1) << 1);
    P.wv0 = (lane >> 3) * 896 + (((lane & 7) ^ (((lane >> 4) & 1) << 1)) << 4);
#pragma unroll
    for (int t = 0; t < 4; ++t) P.wa[t] = P.kg * 4096 + (8 * g + q) * 128 + ((t ^ f) << 5) + p * 8;
    P.own = P.th * 2048 + (g >> 1) * (16384 - 2048);
}
__device__ __forceinline__ void p2_dma(const P2& P, int u, int slot, int half) {
    const char* src = u < 14 ? P.w1 + u * (32 * 896) : P.w2 + (u - 14 < P.n2 ? u - 14 : P.n2 - 1) * (32 * 896);
    glds16s(src + half * (8 * 896), half ? (P.wv0 ^ 64) : P.wv0, P.ring + slot * 16384 + half * 1024);
}
__device__ __forceinline__ void p2_dma2(const P2& P, int u, int slot) { p2_dma(P, u, slot, 0); p2_dma(P, u, slot, 1); }
#define P2_WRD(t, SLOT) lds_tr2(L + P.wa[t] + ((SLOT) >> 2) * 65536 + ((SLOT) & 3) * 16384, 512)
__device__ __forceinline__ void p2_dma_own(const P2& P, int u, int sl, int j) {
    glds16s(P.w1 + (u * 32 + j * 8 - P.th * 16) * 896, (j & 1) ? (P.wv0 ^ 64) : P.wv0, P.ring + (sl + (j >> 1)) * 16384 + (j & 1) * 1024);
}
__device__ __forceinline__ void p2_dma_own4(const P2& P, int u, int sl) {
    p2_dma_own(P, u, sl, 0); p2_dma_own(P, u, sl, 1); p2_dma_own(P, u, sl, 2); p2_dma_own(P, u, sl, 3);
}
__device__ __forceinline__ void p2_to_regs(const P2& P, lds_ptr L, bf16x8 (&wr)[4], int sl) {
#pragma unroll
    for (int t = 0; t < 4; ++t) wr[t] = lds_tr2(L + P.wa[t] + P.own + (sl >> 2) * 65536 + (sl & 3) * 16384, 512);
}
__device__ __forceinline__ void p2_touch(const P2& P, int w, int lane, unsigned lb) {
    const unsigned o1 = blockIdx.x * 6272 + w * 1024 + lane * 16, o2 = blockIdx.x * 1568 + (w & 1) * 1024 + lane * 16;
    if (w < 7) glds16s(P.wd, o1 < F_ * 896 - 16 ? o1 : F_ * 896 - 16, lb + 162816);
    if (w >= 6) glds16s(P.wo, o2 < D_ * 896 - 16 ? o2 : D_ * 896 - 16, lb + 162816);
}
#define WAITL0() asm volatile("s_waitcnt lgkmcnt(0)" ::: "memory")

constexpr int FF_XS = 32768, FF_UOFF = 15376, FF_WS = 2 * FF_UOFF, FF_W0 = 2 * FF_XS, FF_CS = 163840 - 1024;

__device__ __forceinline__ unsigned long long ffn_block(const char* x, const char* wg, const char* wu, char* ws, int rb, int cb,
                                                        lds_ptr L, P2& P, bf16x8 (&wr)[5][4]) {
    const int tid = threadIdx.x, lane = tid & 63;
    const int w = __builtin_amdgcn_readfirstlane(tid >> 6);
    const int wm = w & 3, wn = w >> 2;
    const unsigned lb = (unsigned)(size_t)L;
    unsigned long long ep; u32x4 rv;
    const int srow = rb * 256 + (16 + cb) * 8 + w;
    start_loads(x, ws, srow, lane, ep, rv);
    const unsigned xo0 = (lane >> 3) * 896 + (((lane & 7) ^ (lane >> 4)) << 4), xo1 = xo0 ^ 64;
    const char* xb = x + (size_t)(rb * 256 + 64 * wm) * 896;
    auto dma_x = [&](int c, int j) { glds16s(xb + j * (8 * 896) + c * 128, (j & 1) ? xo1 : xo0, lb + (c & 1) * FF_XS + (64 * wm + 8 * j) * 128); };
    unsigned woff[8];
    auto dma_w1 = [&](int c, int buf, int j) {
        const int n = (j == 7 && wm > 1) ? 29 : wm + 4 * j, mat = n >= 15, nn = n - 15 * mat;
        glds16s((mat ? wu : wg) + cb * 224 + (size_t)c * (64 * F_ * 2), woff[j], lb + FF_W0 + mat * FF_UOFF + nn * 1024 + buf);
    };
    if (wn == 0) {
#pragma unroll
        for (int j = 0; j < 8; ++j) dma_x(0, j);
#pragma unroll
        for (int j = 0; j < 3; ++j) dma_x(1, j);
    }
#pragma unroll
    for (int j = 0; j < 8; ++j) {
        int n = wm + 4 * j; if (n > 29) n = 29;
        const int nn = n >= 15 ? n - 15 : n;
        const int g0 = (nn * 64) / 120, r0 = nn * 64 - g0 * 120;
        int rem = r0 + lane;
        const bool wrap = rem >= 120;
        rem = wrap ? rem - 120 : (rem >= 112 ? rem - 8 : rem);
        const int r = (rem * 147) >> 11;
        woff[j] = (8 * g0 + (wrap ? 8 : 0) + r) * (F_ * 2) + (rem - r * 14) * 16;
        if (wn == 1) dma_w1(0, 0, j);
    }
    if (wn == 1) {
#pragma unroll
        for (int j = 0; j < 8; ++j) dma_w1(1, FF_WS, j);
    }
    p2_touch(P, w, lane, lb);
    asm volatile("s_waitcnt vmcnt(12)" : "+v"(ep), "+v"(rv) :: "memory");
    const unsigned long long tag = publish_stats(ws, srow, lane, ep, rv);
    const int g = lane >> 4, q = (lane >> 2) & 3, p = lane & 3, tk = lane & 15;
    const int xa = (64 * wm + tk) * 128 + ((g ^ ((lane >> 1) & 7)) << 4);
    const int wa = FF_W0 + g * 1920 + q * 224 + (p & 1) * 8 + (p >> 1) * FF_UOFF + wn * 112;
    const int ce = tid < 224 ? tid : 223, crow = 2048 + ((ce >> 3) & 1) * F_ + cb * 112 + (ce >> 4) * 8 + (ce & 7);
    f32x4 acc[4][7];
#pragma unroll
    for (int i = 0; i < 4; ++i)
#pragma unroll
        for (int t = 0; t < 7; ++t) acc[i][t] = f32x4{0.f, 0.f, 0.f, 0.f};
    bf16x8 xf[2][4], wf[4];
    if (wn == 0) asm volatile("s_waitcnt vmcnt(5)\n\ts_barrier" ::: "memory");
    else asm volatile("s_waitcnt vmcnt(10)\n\ts_barrier" ::: "memory");
#define XRD(B, ks, i) xf[ks][i] = lds_b128((B) + (xa ^ ((ks) * 64)) + (i) * 2048)
#define WRD(B, s) lds_tr2((B) + ((s) / 7) * 7680 + ((s) % 7) * 16, 896)
    {
        lds_ptr W = L + wa;
        XRD(L, 0, 0); XRD(L, 0, 1); XRD(L, 0, 2); XRD(L, 0, 3);
        wf[0] = WRD(W, 0); wf[1] = WRD(W, 1); wf[2] = WRD(W, 2);
    }
    unsigned long long sreq, creq;
    int wb = 0;
#define FCHUNK(c, ROLE, MODE)                                                                                          \
    {                                                                                                                  \
        const int wb1 = (wb == 2 * FF_WS) ? 0 : wb + FF_WS, wb2 = (wb == 0) ? 2 * FF_WS : wb - FF_WS;                  \
        lds_ptr X = L + ((c) & 1) * FF_XS, Xn = L + (((c) + 1) & 1) * FF_XS, W = L + wa + wb, Wn = L + wa + wb1;       \
        _Pragma("unroll") for (int s = 0; s < 14; ++s) {                                                               \
            const int ks = s / 7, t = s % 7;                                                                           \
            if (s == 11 && MODE < 2) {                                                                                 \
                if (ROLE == 1 && MODE == 0) asm volatile("s_waitcnt vmcnt(8) lgkmcnt(0)\n\ts_barrier" ::: "memory");  \
                else WAIT_BAR0();                                                                                      \
            }                                                                                                          \
            if (s < 11) wf[(s + 3) % 4] = WRD(W, s + 3); else if (MODE < 2) wf[(s + 3) % 4] = WRD(Wn, s - 11);        \
            if (s < 4) XRD(X, 1, s);                                                                                   \
            if (s == 11 && MODE < 2) { XRD(Xn, 0, 0); XRD(Xn, 0, 1); }                                                 \
            if (s == 12 && MODE < 2) { XRD(Xn, 0, 2); XRD(Xn, 0, 3); }                                                 \
            if (ROLE == 0 && MODE < 2 && s < 5) dma_x((c) + 1, 3 + s);                                                 \
            if (ROLE > 0 && MODE == 0 && s >= 3 && s < 11) dma_w1((c) + 2, wb2, s - 3);                         \
            if (ROLE == 0 && MODE == 0 && s >= 11) dma_x((c) + 2, s - 11);                                             \
            if (MODE == 3 && s >= 2 && s < 10) p2_dma_own(P, (s - 2) >> 2, s < 6 ? 2 : 6, (s - 2) & 3);                \
            _Pragma("unroll") for (int i = 0; i < 4; ++i) acc[i][t] = MFMA16(wf[s % 4], xf[ks][i], acc[i][t]);        \
            SB();                                                                                                      \
        }                                                                                                              \
        { bf16x8 a_ = wf[2], b_ = wf[3], d_ = wf[0]; wf[0] = a_; wf[1] = b_; wf[2] = d_; }                             \
        wb = wb1;                                                                                                      \
    }
    if (wn == 0) {
#pragma unroll 1
        for (int c = 0; c < 5; ++c) FCHUNK(c, 0, 0)
        stats_request(ws, rb * 256 + 64 * wm + lane, sreq);
        FCHUNK(5, 0, 1)
    } else {
#pragma unroll 1
        for (int c = 0; c < 5; ++c) FCHUNK(c, 1, 0)
        stats_request(ws, rb * 256 + 64 * wm + lane, sreq);
        FCHUNK(5, 1, 1)
    }
    p2_lane(P);
    stats_request(ws, crow, creq);
    FCHUNK(6, 0, 3)
    asm volatile("s_waitcnt vmcnt(8)" : "+v"(creq) :: "memory");
    LDSF(L + FF_CS + ce * 4) = __uint_as_float(stats_check(ws, crow, tag, creq));
    asm volatile("" : "+v"(sreq));
    const unsigned sv = stats_check(ws, rb * 256 + 64 * wm + lane, tag, sreq);
    LBAR();
    const __amdgpu_buffer_rsrc_t ra = __builtin_amdgcn_make_buffer_rsrc(ws, (short)0, ACT_BYTES, 0x00020000);
    f32x4 csr[7];
#pragma unroll
    for (int t = 0; t < 7; ++t) csr[t] = lds_f4(L + FF_CS + (7 * wn + t) * 64 + g * 16);
    LBAR();
    const float K1 = -2.3022081986f, K2 = -0.1029432398f;
#pragma unroll
    for (int ip = 0; ip < 2; ++ip) {
        const unsigned sa = __shfl(sv, 32 * ip + tk), sb = __shfl(sv, 32 * ip + 16 + tk);
        const float ma = -__uint_as_float(sa & 0xFFFF0000u), mb = -__uint_as_float(sb & 0xFFFF0000u);
        const float dn = __uint_as_float((lane < 32 ? sa : sb) << 16);
        const float a = __builtin_amdgcn_rcpf(dn), ia2 = dn * dn, c1 = K1 * a, c2 = K2 * a * a * a;
        const unsigned dst = (rb * 16 + wm * 4 + 2 * ip + (lane >> 5)) * ACT_TILE_B + (cb * 14 + wn * 7) * 256 + tk * 16;
        u32x2 hp[7];
#pragma unroll
        for (int t = 0; t < 7; ++t) {
            const f32x4 cs = csr[t];
#pragma unroll
            for (int u = 0; u < 2; ++u) {
                const f32x2 c2v = u ? hi2(cs) : lo2(cs);
                const f32x2 va = fma2(sp2(ma), c2v, u ? hi2(acc[2 * ip][t]) : lo2(acc[2 * ip][t]));
                const f32x2 vb = fma2(sp2(mb), c2v, u ? hi2(acc[2 * ip + 1][t]) : lo2(acc[2 * ip + 1][t]));
                auto s0 = __builtin_amdgcn_permlane32_swap(__float_as_uint(va[0]), __float_as_uint(vb[0]), false, false);
                auto s1 = __builtin_amdgcn_permlane32_swap(__float_as_uint(va[1]), __float_as_uint(vb[1]), false, false);
                const f32x2 G = f32x2{__uint_as_float(s0[0]), __uint_as_float(s1[0])}, U = f32x2{__uint_as_float(s0[1]), __uint_as_float(s1[1])};
                const f32x2 z = G * fma2(G * G, sp2(c2), sp2(c1));
                const f32x2 e = f32x2{__builtin_amdgcn_exp2f(z[0]), __builtin_amdgcn_exp2f(z[1])};
                const f32x2 d = fma2(e, sp2(ia2), sp2(ia2));
                hp[t][u] = pk2((G * U) * f32x2{__builtin_amdgcn_rcpf(d[0]), __builtin_amdgcn_rcpf(d[1])});
            }
            if (t & 1) {
                auto s0 = __builtin_amdgcn_permlane16_swap(hp[t - 1][0], hp[t][0], false, false);
                auto s1 = __builtin_amdgcn_permlane16_swap(hp[t - 1][1], hp[t][1], false, false);
                __builtin_amdgcn_raw_buffer_store_b128(u32x4{s0[0], s1[0], s0[1], s1[1]}, ra, dst + (g & 1) * 256, (t - 1) * 256, 16);
            }
            if (t == 6) __builtin_amdgcn_raw_buffer_store_b64(hp[6], ra, dst + (g & 1) * 8, 6 * 256, 16);
            {
                if (ip == 0) {
                    if (t < 6) { p2_dma_own(P, 2 + (t >> 1), 4 * (t >> 1), 2 * (t & 1)); p2_dma_own(P, 2 + (t >> 1), 4 * (t >> 1), 2 * (t & 1) + 1); }
                } else if (t < 5) p2_dma2(P, 5 + t, t);
            }
            SB();
        }
        if (ip == 0) {
            asm volatile("s_waitcnt vmcnt(1)" ::: "memory");
            p2_to_regs(P, L, wr[0], 2); p2_to_regs(P, L, wr[1], 6); p2_to_regs(P, L, wr[2], 0); p2_to_regs(P, L, wr[3], 4); p2_to_regs(P, L, wr[4], 8);
            WAITL0();
            SB();
        }
    }
    WAIT_BAR0();
    if (tid == 0) __hip_atomic_store((gu64*)(ws + FLAG_OFF + (size_t)(rb * 32 + 16 + cb) * 64), tag, RLX_AGENT);
#pragma unroll
    for (int k = 5; k < 10; ++k) p2_dma2(P, 5 + k, k);
    return tag;
}

constexpr int AT_WM = 4096, AT_WS = 3 * AT_WM, AT_W = 131072;
constexpr int AT_K = 98304, AT_V = AT_K + 32768;

__device__ __forceinline__ unsigned long long att_block(const char* x, const char* wqkv, char* ws, int ch, int head, lds_ptr L,
                                                        P2& P, bf16x8 (&wr)[5][4]) {
    const int tid = threadIdx.x, lane = tid & 63;
    const int w = __builtin_amdgcn_readfirstlane(tid >> 6);
    const unsigned lb = (unsigned)(size_t)L;
    const int g = lane >> 4, q = (lane >> 2) & 3, p = lane & 3, tk = lane & 15;
    unsigned long long ep; u32x4 rv;
    const int srow = ch * 256 + head * 8 + w;
    start_loads(x, ws, srow, lane, ep, rv);
    const unsigned wo = (lane >> 2) * (QKV_ * 2) + (((lane & 3) ^ (((lane >> 5) & 1) << 1)) << 4);
    const char* wsrc0 = wqkv + (w & 3) * (16 * QKV_ * 2) + (w >> 2) * 896 + head * 56;
    const char* wsrc1 = wqkv + (w & 3) * (16 * QKV_ * 2) + 2 * 896 + head * 56 - 8;
    auto dma_w = [&](int c) {
        glds16s(wsrc0 + c * (64 * QKV_ * 2), wo, lb + AT_W + (c & 1) * AT_WS + w * 1024);
        if (w < 4) glds16s(wsrc1 + c * (64 * QKV_ * 2), wo, lb + AT_W + (c & 1) * AT_WS + 8192 + w * 1024);
    };
    const int orow = ch * 256 + 32 * w;
    const unsigned xo0 = (lane >> 3) * 896 + (((lane & 7) ^ (lane >> 4)) << 4), xo1 = xo0 ^ 64;
    const char* xs = x + (size_t)orow * 896;
    auto dma_x = [&](int c, int i) {
        const char* src = xs + i * (16 * 896) + c * 128;
        glds16s(src, xo0, lb + (c & 3) * 32768 + w * 4096 + i * 2048);
        glds16s(src + 8 * 896, xo1, lb + (c & 3) * 32768 + w * 4096 + i * 2048 + 1024);
    };
    dma_w(0); dma_x(0, 0); dma_x(0, 1);
    const int cblk = (ch * 16 + head) < 112 ? ch * 16 + head : 111;
    auto dma_cs = [&]() {
        const char* cp = (cblk < 56 ? P.wg + cblk * 64 : P.wu + (cblk - 56) * 64);
        const unsigned co = (lane >> 2) * (F_ * 2) + (((lane & 3) ^ (((lane >> 5) & 1) << 1)) << 4);
#pragma unroll
        for (int j = 0; j < 4; ++j) {
            const int ks = j < 2 ? w : (w < 6 ? w + 8 : 13);
            glds16s(cp + (size_t)(ks * 32 + (j & 1) * 16) * (F_ * 2), co, lb + 98304 + w * 4096 + j * 1024);
        }
    };
    dma_w(1); dma_x(1, 0); dma_x(1, 1); dma_x(2, 0); dma_x(2, 1);
    p2_touch(P, w, lane, lb);
    asm volatile("s_waitcnt vmcnt(15)" : "+v"(ep), "+v"(rv) :: "memory");
    const unsigned long long tag = publish_stats(ws, srow, lane, ep, rv);

    const int xa0 = w * 4096 + tk * 128 + ((g ^ ((lane >> 1) & 7)) << 4), xa1 = xa0 ^ 64;
    const int wA0 = AT_W + (8 * g + q) * 64 + ((g & 1) << 5) + p * 8, wA1 = wA0 ^ 32;
    const int cidx = w > 5 ? 5 : w;
    const int wC = (cidx >> 1) * AT_WM + (wA0 ^ ((cidx & 1) << 5));
    f32x4 aK[2][2], aV[2][2], aQ[2][2], cs = f32x4{0.f, 0.f, 0.f, 0.f};
#pragma unroll
    for (int i = 0; i < 2; ++i) aK[i][0] = aK[i][1] = aV[i][0] = aV[i][1] = aQ[i][0] = aQ[i][1] = f32x4{0.f, 0.f, 0.f, 0.f};
    const bf16x8 ones = __builtin_bit_cast(bf16x8, u32x4{0x3F803F80u, 0x3F803F80u, 0x3F803F80u, 0x3F803F80u});
    bf16x8 wf[2][3][2], cf[2], xf[2][2];
#define AWRD(B, ks) { _Pragma("unroll") for (int m = 0; m < 3; ++m) { wf[ks][m][0] = lds_tr2((B) + wA0 + m * AT_WM + (ks) * 2048, 256); \
                      wf[ks][m][1] = lds_tr2((B) + wA1 + m * AT_WM + (ks) * 2048, 256); } cf[ks] = lds_tr2((B) + wC + (ks) * 2048, 256); }
#define AXRD(c, i) { xf[i][0] = lds_b128(L + xa0 + ((c) & 3) * 32768 + (i) * 2048); xf[i][1] = lds_b128(L + xa1 + ((c) & 3) * 32768 + (i) * 2048); }
#define AMM(i, ks) { _Pragma("unroll") for (int t = 0; t < 2; ++t) { \
                       aK[i][t] = MFMA16(wf[ks][1][t], xf[i][ks], aK[i][t]); aV[i][t] = MFMA16(xf[i][ks], wf[ks][2][t], aV[i][t]); \
                       aQ[i][t] = MFMA16(wf[ks][0][t], xf[i][ks], aQ[i][t]); } }
    asm volatile("s_waitcnt vmcnt(%0)\n\ts_barrier" :: "i"(11) : "memory");
    AXRD(0, 0) AWRD(L, 0) AWRD(L, 1)
    SB();
    const int strow = orow + (lane & 31);
    unsigned long long sreq;
#pragma unroll
    for (int c = 0; c < 7; ++c) {
        AXRD(c, 1)
        AMM(0, 0)
        cs = MFMA16(cf[0], ones, cs);
        AMM(0, 1)
        cs = MFMA16(cf[1], ones, cs);
        SB();
        if (c + 3 < 7) dma_x(c + 3, 0);
        if (c == 4) stats_request(ws, strow, sreq);
        if (c < 6) {
            asm volatile("s_waitcnt vmcnt(%0)\n\ts_barrier" :: "i"(c == 0 ? 8 : c < 4 ? 4 : c == 4 ? 7 : 0) : "memory");
            AXRD(c + 1, 0)
            if (c + 2 < 7) dma_w(c + 2);
            if (c == 5)
    {
        lds_ptr C = L + 98304 + w * 4096 - AT_W;
        const f32x4 z4 = f32x4{0.f, 0.f, 0.f, 0.f};
        f32x4 p0 = MFMA16(lds_tr2(C + wA0, 256), ones, z4), p1 = MFMA16(lds_tr2(C + wA1, 256), ones, z4);
        const bf16x8 f0 = lds_tr2(C + wA0 + 2048, 256), f1 = lds_tr2(C + wA1 + 2048, 256);
        if (w < 6) { p0 = MFMA16(f0, ones, p0); p1 = MFMA16(f1, ones, p1); }
        if (tk == 0) { LDSV(f32x4, L + w * 4096 + 1024 + g * 16) = p0; LDSV(f32x4, L + w * 4096 + 1088 + g * 16) = p1; }
    }
        }
        AMM(1, 0)
        SB();
        if (c < 6) AWRD(L + ((c + 1) & 1) * AT_WS, 0)
        AMM(1, 1)
        SB();
        if (c < 6) AWRD(L + ((c + 1) & 1) * AT_WS, 1)
        if (c + 3 < 7) dma_x(c + 3, 1);
        if (c == 3) dma_cs();
        SB();
    }
    asm volatile("" : "+v"(sreq));
    lds_ptr ST = L + w * 4096;
    if (tk == 0) LDSV(f32x4, ST + 512 + g * 16) = cs;
    {
        const unsigned sv = stats_check(ws, strow, tag, sreq);
        LDSF(ST + lane * 4) = -__uint_as_float(sv & 0xFFFF0000u);
        LDSF(ST + 256 + lane * 4) = __builtin_amdgcn_rcpf(__uint_as_float(sv << 16));
    }
    LBAR();
    if (w == 7) {
        float t = 0.f;
#pragma unroll
        for (int k = 0; k < 8; ++k) t += LDSF(L + k * 4096 + 1024 + (lane & 31) * 4);
        if (lane < 32 && ch * 16 + head < 112)
            __hip_atomic_store((gu64*)(ws + STAT_OFF) + 2048 + cblk * 32 + lane,
                               ((unsigned long long)(unsigned)tag << 32) | __float_as_uint(t), RLX_AGENT);
    }
    bf16x8 qf[2];
    const bool pad = (lane >= 48);
    const __amdgpu_buffer_rsrc_t rk = __builtin_amdgcn_make_buffer_rsrc(ws + KV_OFF, (short)0, 128 * 32768, 0x00020000);
    {
        const unsigned kvo = (ch * 16 + head) * 32768 + w * 2048 + lane * 16;
        f32x4 cq[2], ck[2];
        float cv[2];
#pragma unroll
        for (int t = 0; t < 2; ++t) {
            cq[t] = lds_f4(L + 512 + t * 4096 + g * 16);
            ck[t] = lds_f4(L + 512 + (2 + t) * 4096 + g * 16);
            cv[t] = LDSF(L + 512 + (4 + t) * 4096 + tk * 4);
        }
#define CORR(A, C) u32x4{pk2(fma2(sp2(nm), lo2(C[0]), lo2(A[0])) * sp2(rs)), pk2(fma2(sp2(nm), hi2(C[0]), hi2(A[0])) * sp2(rs)), \
                         pk2(fma2(sp2(nm), lo2(C[1]), lo2(A[1])) * sp2(rs)), pk2(fma2(sp2(nm), hi2(C[1]), hi2(A[1])) * sp2(rs))}
#pragma unroll
        for (int i = 0; i < 2; ++i) {
            const float nm = LDSF(ST + i * 64 + tk * 4), rs = LDSF(ST + 256 + i * 64 + tk * 4);
            u32x4 kv = CORR(aK[i], ck);
            kv[2] = pad ? 0u : kv[2];
            kv[3] = pad ? 0u : kv[3];
            __builtin_amdgcn_raw_buffer_store_b128(kv, rk, kvo, i * 1024, 16);
            LDSV(u32x4, L + AT_K + (16 + 2 * w + i) * 1024 + lane * 16) = kv;
        }
        f32x4 s0[2], s1[2];
#pragma unroll
        for (int u = 0; u < 2; ++u) {
            s0[u] = lds_f4(ST + u * 64 + g * 16);
            s1[u] = lds_f4(ST + 256 + u * 64 + g * 16);
        }
#pragma unroll
        for (int t = 0; t < 2; ++t) {
            u32x4 o;
#pragma unroll
            for (int u = 0; u < 2; ++u) {
                o[2 * u] = pk2(fma2(lo2(s0[u]), sp2(cv[t]), lo2(aV[u][t])) * lo2(s1[u]));
                o[2 * u + 1] = pk2(fma2(hi2(s0[u]), sp2(cv[t]), hi2(aV[u][t])) * hi2(s1[u]));
            }
            if (t == 0 && tk == 0) o = u32x4{0x3F803F80u, 0x3F803F80u, 0x3F803F80u, 0x3F803F80u};
            __builtin_amdgcn_raw_buffer_store_b128(o, rk, kvo, 16384 + t * 1024, 16);
            LDSV(u32x4, L + AT_V + ((8 + w) * 2 + t) * 1024 + lane * 16) = o;
        }
#pragma unroll
        for (int i = 0; i < 2; ++i) {
            const float nm = LDSF(ST + i * 64 + tk * 4), rs = LDSF(ST + 256 + i * 64 + tk * 4);
            u32x4 qv = CORR(aQ[i], cq);
            qv[2] = pad ? 0x3F80u : qv[2];
            qv[3] = pad ? 0u : qv[3];
            qf[i] = __builtin_bit_cast(bf16x8, qv);
        }
    }
    asm volatile("s_waitcnt vmcnt(0)" ::: "memory");
    if (lane == 0) __hip_atomic_store((gu64*)(ws + FLAG_OFF + (size_t)(256 + (ch * 16 + head) * 8 + w) * 64), tag, RLX_AGENT);
    LBAR();
    p2_lane(P);
    p2_dma_own4(P, 0, 0); p2_dma_own4(P, 1, 2); p2_dma_own4(P, 2, 4);
    {
        u32x4 hk[2], hv[2];
        if (ch) {
            wait_flags(ws, 256 + ((ch - 1) * 16 + head) * 8 + w, tag);
            const unsigned kvo = ((ch - 1) * 16 + head) * 32768 + w * 2048 + lane * 16;
#pragma unroll
            for (int i = 0; i < 2; ++i) {
                hk[i] = __builtin_amdgcn_raw_buffer_load_b128(rk, kvo, i * 1024, 16);
                hv[i] = __builtin_amdgcn_raw_buffer_load_b128(rk, kvo, 16384 + i * 1024, 16);
            }
        } else {
            hk[0] = hk[1] = u32x4{0u, 0u, pad ? 0xF149u : 0u, 0u};
            hv[0] = hv[1] = u32x4{0u, 0u, 0u, 0u};
        }
#pragma unroll
        for (int i = 0; i < 2; ++i) {
            LDSV(u32x4, L + AT_K + (2 * w + i) * 1024 + lane * 16) = hk[i];
            LDSV(u32x4, L + AT_V + (w * 2 + i) * 1024 + lane * 16) = hv[i];
        }
    }
    asm volatile("s_waitcnt vmcnt(0)" ::: "memory");
    p2_to_regs(P, L, wr[0], 0); p2_to_regs(P, L, wr[1], 2); p2_to_regs(P, L, wr[2], 4);
    LBAR();
    const float NEG = -1e30f;
    f32x4 mle, mgt;
#pragma unroll
    for (int r = 0; r < 4; ++r) { mle[r] = (4 * g + r <= tk) ? 0.f : NEG; mgt[r] = (4 * g + r > tk) ? 0.f : NEG; }
    f32x4 Sc[2][18];
#pragma unroll
    for (int j = 0; j < 18; ++j) {
        const bf16x8 kf = lds_b128(L + AT_K + (2 * w + j) * 1024 + lane * 16);
#pragma unroll
        for (int qt = 0; qt < 2; ++qt) {
            if (j >= qt && j <= qt + 16) {
                f32x4 c0;
                if (j == qt + 16) c0 = mle;
                else if (j == qt) c0 = mgt;
                else c0 = f32x4{0.f, 0.f, 0.f, 0.f};
                Sc[qt][j] = MFMA16(kf, qf[qt], c0);
            } else {
                Sc[qt][j] = f32x4{0.f, 0.f, 0.f, 0.f};
            }
        }
        if (j < 8) p2_dma_own(P, 3 + (j >> 2), 2 * (j >> 2), j & 3);
        else if (j < 12) p2_dma(P, 9 + ((j - 8) >> 1), 4 + ((j - 8) >> 1), j & 1);
    }
    const float CS = 0.18898223650461362f * 1.4426950408889634f;
    u32x4 pf[2][9];
#pragma unroll
    for (int qt = 0; qt < 2; ++qt) {
        float m = NEG, m2 = NEG;
#pragma unroll
        for (int j = qt; j <= qt + 16; ++j) { m = max3f(m, Sc[qt][j][0], Sc[qt][j][1]); m2 = max3f(m2, Sc[qt][j][2], Sc[qt][j][3]); }
        m = max3f(m, m2, m2);
        m = xmax(m);
        const float mc = -m * CS;
#pragma unroll
        for (int j = qt; j <= qt + 16; ++j) {
            const f32x2 a = fma2(lo2(Sc[qt][j]), sp2(CS), sp2(mc)), b = fma2(hi2(Sc[qt][j]), sp2(CS), sp2(mc));
            Sc[qt][j] = f32x4{__builtin_amdgcn_exp2f(a[0]), __builtin_amdgcn_exp2f(a[1]), __builtin_amdgcn_exp2f(b[0]), __builtin_amdgcn_exp2f(b[1])};
            if (qt == 1 && (j & 1) == 0) { p2_dma(P, 5 + ((j - 2) >> 2), (j - 2) >> 2, ((j - 2) >> 1) & 1); SB(); }
        }
#pragma unroll
        for (int b = 0; b < 9; ++b)
            pf[qt][b] = u32x4{pk2(Sc[qt][2 * b][0], Sc[qt][2 * b][1]), pk2(Sc[qt][2 * b][2], Sc[qt][2 * b][3]),
                              pk2(Sc[qt][2 * b + 1][0], Sc[qt][2 * b + 1][1]), pk2(Sc[qt][2 * b + 1][2], Sc[qt][2 * b + 1][3])};
        if (qt == 0) {
            SB();
            asm volatile("s_waitcnt vmcnt(0)" ::: "memory");
            p2_to_regs(P, L, wr[3], 0); p2_to_regs(P, L, wr[4], 2);
            WAITL0();
            SB();
        }
    }
    f32x4 O[2][2];
    O[0][0] = O[0][1] = O[1][0] = O[1][1] = f32x4{0.f, 0.f, 0.f, 0.f};
#pragma unroll
    for (int b = 0; b < 9; ++b)
#pragma unroll
        for (int t = 0; t < 2; ++t) {
            const bf16x8 vf = lds_b128(L + AT_V + ((w + b) * 2 + t) * 1024 + lane * 16);
#pragma unroll
            for (int qt = 0; qt < 2; ++qt) O[qt][t] = MFMA16(vf, __builtin_bit_cast(bf16x8, pf[qt][b]), O[qt][t]);
        }
    const __amdgpu_buffer_rsrc_t ra = __builtin_amdgcn_make_buffer_rsrc(ws, (short)0, ACT_BYTES, 0x00020000);
#pragma unroll
    for (int qt = 0; qt < 2; ++qt) {
        const float l = __shfl(O[qt][0][0], tk);
        const float inv = __builtin_amdgcn_rcpf(l);
        const unsigned dst = (ch * 16 + 2 * w + qt) * ACT_TILE_B + 224 * 256 + tk * 16;
#pragma unroll
        for (int t = 0; t < 2; ++t) {
            const int u = 7 * head + 4 * t + g - 1;
            if (t == 1 || g > 0)
                __builtin_amdgcn_raw_buffer_store_b64(
                    u32x2{pk2(O[qt][t][0] * inv, O[qt][t][1] * inv), pk2(O[qt][t][2] * inv, O[qt][t][3] * inv)}, ra,
                    dst + (u >> 1) * 256 + (u & 1) * 8, 0, 16);
        }
    }
    WAIT_BAR0();
    if (tid == 0) __hip_atomic_store((gu64*)(ws + FLAG_OFF + (size_t)(ch * 32 + head) * 64), tag, RLX_AGENT);
#pragma unroll
    for (int k = 6; k < 10; ++k) p2_dma2(P, 5 + k, k);
    return tag;
}

constexpr int LDS_BYTES = 163840;

__global__ __launch_bounds__(512, 2) void k_fused(const char* x, const char* wqkv, const char* wo, const char* wg, const char* wu,
                                                  const char* wd, char* out, char* ws) {
    extern __shared__ __attribute__((aligned(16))) char smem[];
    lds_ptr L = (lds_ptr)smem;
    const unsigned lb = (unsigned)(size_t)L;
    const int tid = threadIdx.x, lane = tid & 63;
    const int w = __builtin_amdgcn_readfirstlane(tid >> 6);
    const int xcd = blockIdx.x & 7, idx = blockIdx.x >> 3;
    const int rblk = 4 * (xcd & 1) + (idx & 3), cblk = 4 * (xcd >> 1) + ((idx >> 2) & 3);
    if (w == 7) {
        unsigned long long pc;
        asm volatile("s_getpc_b64 %0" : "=s"(pc));
        glds16s((const char*)(pc & ~255ull), idx * 1024 + lane * 16, lb + 162816);
        if (idx < 15) glds16s((const char*)(pc & ~255ull), 32768 + idx * 1024 + lane * 16, lb + 162816);
    }
    const int th = w & 1, kg = w >> 1;
    const bool has_p2 = idx >= 4;
    const int rb = 4 * xcd + (idx & 3), cb = has_p2 ? (idx >> 2) - 1 : 0;
    const int a1 = kg ? 14 * (kg - 1) : 56;
    const int a2 = 42 + (kg < 2 ? 3 * kg : 4 * kg - 2);
    P2 P;
    P.n2 = kg < 2 ? 3 : 4;
    P.w1 = (kg ? wd + (size_t)(14 * (kg - 1)) * 32 * 896 : wo) + cb * 128 + th * 16 * 896;
    P.w2 = wd + (size_t)a2 * 32 * 896 + cb * 128 + th * 16 * 896;
    P.ring = lb + kg * 4096 + th * 2048;
    P.th = th; P.kg = kg; P.wd = wd; P.wo = wo; P.wg = wg; P.wu = wu;
    bf16x8 wr[5][4];
    unsigned long long tag;
    if (idx < 16) tag = att_block(x, wqkv, ws, rblk, cblk, L, P, wr);
    else tag = ffn_block(x, wg, wu, ws, rblk, cblk, L, P, wr);
    if (!has_p2) {
        asm volatile("s_waitcnt vmcnt(0)" ::: "memory");
        if (blockIdx.x == 0 && w == 0) {
#pragma unroll 1
            for (int i = 0; i < 4; ++i) wait_flags(ws, 64 * i + lane, tag);
            if (lane == 0) __hip_atomic_store((gu64*)(ws + SYNC_OFF), tag, RLX_AGENT);
        }
        return;
    }
    int ln = lane;
    asm volatile("" : "+v"(ln));
    const int g = ln >> 4, tk = ln & 15;
    const size_t oo = (size_t)(rb * 64 + 32 * th + tk) * 896 + cb * 128 + kg * 32 + g * 8;
    const u32x2 xr0 = *(const u32x2*)(x + oo), xr1 = *(const u32x2*)(x + oo + 16 * 896);
    wait_flags(ws, (rb >> 2) * 32 + (kg == 0 ? (ln < 16 ? ln : 28) : ln < 4 ? 12 + 4 * kg + ln : ln == 4 ? 27 + kg : 28 + kg), tag);
    asm volatile("" ::: "memory");
    const __amdgpu_buffer_rsrc_t ra = __builtin_amdgcn_make_buffer_rsrc(ws, (short)0, ACT_BYTES, 0x00020000);
    const unsigned ab = (rb * 4 + 2 * th) * ACT_TILE_B + lane * 16;
    bf16x8 af[18][2];
#define ALD(u) { const int ks_ = (u) < 14 ? a1 + (u) : a2 + ((u) - 14 < P.n2 ? (u) - 14 : P.n2 - 1);                                      \
                 af[u][0] = __builtin_bit_cast(bf16x8, __builtin_amdgcn_raw_buffer_load_b128(ra, ab, ks_ * 1024, 16));                    \
                 af[u][1] = __builtin_bit_cast(bf16x8, __builtin_amdgcn_raw_buffer_load_b128(ra, ab + ACT_TILE_B, ks_ * 1024, 16)); SB(); }
#pragma unroll
    for (int u = 0; u < 5; ++u) ALD(u)
    f32x4 c1[2][4], c2[2][4];
#pragma unroll
    for (int i = 0; i < 2; ++i)
#pragma unroll
        for (int t = 0; t < 4; ++t) c1[i][t] = c2[i][t] = f32x4{0.f, 0.f, 0.f, 0.f};
#define MM8(ACC, WF, u) { _Pragma("unroll") for (int t = 0; t < 4; ++t) {            \
            ACC[0][t] = MFMA16(WF[t], af[u][0], ACC[0][t]);                          \
            ACC[1][t] = MFMA16(WF[t], af[u][1], ACC[1][t]); } }
#define STEP(ACC, u, SLOT) { bf16x8 wf[4]; _Pragma("unroll") for (int t = 0; t < 4; ++t) wf[t] = P2_WRD(t, SLOT); MM8(ACC, wf, u) }
#pragma unroll
    for (int u = 0; u < 5; ++u) {
        MM8(c1, wr[u], u)
        SB();
        ALD(u + 5)
    }
    asm volatile("s_barrier" ::: "memory");
#pragma unroll
    for (int u = 5; u < 8; ++u) { STEP(c1, u, u - 5) SB(); ALD(u + 5) }
    LBAR();
#pragma unroll
    for (int u = 8; u < 14; ++u) {
        STEP(c1, u, u - 5)
        SB();
        if (u < 13) ALD(u + 5)
        if (u < 11) p2_dma2(P, u + 7, u - 8);
    }
    STEP(c2, 14, 9)
    SB();
    WAIT_BAR0();
    if (kg == 0) {
#pragma unroll
        for (int i = 0; i < 2; ++i)
#pragma unroll
            for (int t = 0; t < 4; ++t) LDSV(f32x4, L + 131072 + th * 8192 + (i * 4 + t) * 1024 + lane * 16) = c1[i][t];
    } else {
#pragma unroll
        for (int i = 0; i < 2; ++i)
#pragma unroll
            for (int t = 0; t < 4; ++t) c2[i][t] += c1[i][t];
    }
    STEP(c2, 15, 0) STEP(c2, 16, 1)
    if (P.n2 == 4) STEP(c2, 17, 2)
    f32x4 fs[2];
#define PUT(K)                                                                                             \
    {                                                                                                      \
        _Pragma("unroll") for (int i = 0; i < 2; ++i) {                                                    \
            _Pragma("unroll") for (int t = 0; t < 4; ++t)                                                  \
                if (t != K) LDSV(f32x4, L + 49152 + w * 8192 + (i * 4 + t) * 1024 + lane * 16) = c2[i][t]; \
            fs[i] = c2[i][K];                                                                              \
        }                                                                                                  \
    }
    if (kg == 0) PUT(0) else if (kg == 1) PUT(1) else if (kg == 2) PUT(2) else PUT(3)
    LBAR();
#pragma unroll
    for (int i = 0; i < 2; ++i) {
#pragma unroll
        for (int k = 1; k < 4; ++k) fs[i] += lds_f4(L + 49152 + (2 * ((kg + k) & 3) + th) * 8192 + (i * 4 + kg) * 1024 + lane * 16);
        const f32x4 sv = lds_f4(L + 131072 + th * 8192 + (i * 4 + kg) * 1024 + lane * 16);
        const u32x2 xr = i ? xr1 : xr0;
        u32x2 o;
#pragma unroll
        for (int u = 0; u < 2; ++u) {
            const f32x2 fv = u ? hi2(fs[i]) : lo2(fs[i]), z = fv * sp2(-1.4426950408889634f);
            const f32x2 d = f32x2{__builtin_amdgcn_exp2f(z[0]), __builtin_amdgcn_exp2f(z[1])} + sp2(1.0f);
            const f32x2 gt = fv * f32x2{__builtin_amdgcn_rcpf(d[0]), __builtin_amdgcn_rcpf(d[1])};
            const f32x2 xv = f32x2{__uint_as_float(xr[u] << 16), __uint_as_float(xr[u] & 0xFFFF0000u)};
            o[u] = pk2(fma2(u ? hi2(sv) : lo2(sv), gt, xv));
        }
        *(u32x2*)(out + oo + (size_t)i * 16 * 896) = o;
    }
}

extern "C" void kernel_launch(void* const* d_in, const int* in_sizes, int n_in, void* d_out, int out_size, void* d_ws,
                              size_t ws_size, hipStream_t stream) {
    (void)hipFuncSetAttribute((const void*)k_fused, hipFuncAttributeMaxDynamicSharedMemorySize, LDS_BYTES);
    k_fused<<<256, 512, LDS_BYTES, stream>>>((const char*)d_in[0], (const char*)d_in[1], (const char*)d_in[2], (const char*)d_in[4],
                                             (const char*)d_in[5], (const char*)d_in[6], (char*)d_out, (char*)d_ws);
}
```

```cpp
#include <hip/hip_runtime.h>
#include <hip/hip_bf16.h>

typedef short bf16x8 __attribute__((ext_vector_type(8)));
typedef short s16x4 __attribute__((ext_vector_type(4)));
typedef float f32x4 __attribute__((ext_vector_type(4)));
typedef float f32x2 __attribute__((ext_vector_type(2)));
typedef unsigned u32x4 __attribute__((ext_vector_type(4)));
typedef unsigned u32x2 __attribute__((ext_vector_type(2)));
typedef __bf16 bf16x2_t __attribute__((ext_vector_type(2)));
typedef __attribute__((address_space(3))) char* lds_ptr;

#define MFMA16(a, b, c) __builtin_amdgcn_mfma_f32_16x16x32_bf16(a, b, c, 0, 0, 0)
#define WAIT_BAR0() asm volatile("s_waitcnt vmcnt(0) lgkmcnt(0)\n\ts_barrier" ::: "memory")
#define LBAR() asm volatile("s_waitcnt lgkmcnt(0)\n\ts_barrier" ::: "memory")

constexpr int S_ = 2048, D_ = 448, F_ = 1792, QKV_ = 1344;
constexpr int ACT_OCT = 280;
constexpr int ACT_TILE_B = ACT_OCT * 256 + 256;

__device__ __forceinline__ void glds16s(const void* sbase, unsigned voff, unsigned lds_dst) {
    unsigned sv;
    asm volatile("s_mov_b32 %0, m0\n\ts_mov_b32 m0, %3\n\ts_nop 0\n\tglobal_load_lds_dwordx4 %1, %2\n\ts_mov_b32 m0, %0"
                 : "=&s"(sv) : "v"(voff), "s"(sbase), "s"(lds_dst) : "memory");
}
__device__ __forceinline__ bf16x8 lds_b128(lds_ptr p) { return *(const __attribute__((address_space(3))) bf16x8*)p; }
__device__ __forceinline__ f32x4 lds_f4(lds_ptr p) { return *(const __attribute__((address_space(3))) f32x4*)p; }
__device__ __forceinline__ s16x4 lds_tr(lds_ptr p) {
    return __builtin_amdgcn_ds_read_tr16_b64_v4i16((__attribute__((address_space(3))) s16x4*)p);
}
__device__ __forceinline__ bf16x8 lds_tr2(lds_ptr p, int second) {
    s16x4 a = lds_tr(p), b = lds_tr(p + second);
    return __builtin_shufflevector(a, b, 0, 1, 2, 3, 4, 5, 6, 7);
}
__device__ __forceinline__ float dot2(unsigned a, unsigned b, float c) {
    return __builtin_amdgcn_fdot2_f32_bf16(__builtin_bit_cast(bf16x2_t, a), __builtin_bit_cast(bf16x2_t, b), c, false);
}
__device__ __forceinline__ float frag_sum(bf16x8 f, float c) {
    u32x4 u = __builtin_bit_cast(u32x4, f);
    c = dot2(u[0], 0x3F803F80u, c); c = dot2(u[1], 0x3F803F80u, c);
    c = dot2(u[2], 0x3F803F80u, c); c = dot2(u[3], 0x3F803F80u, c);
    return c;
}
__device__ __forceinline__ float frag_sq(bf16x8 f, float c) {
    u32x4 u = __builtin_bit_cast(u32x4, f);
    c = dot2(u[0], u[0], c); c = dot2(u[1], u[1], c); c = dot2(u[2], u[2], c); c = dot2(u[3], u[3], c);
    return c;
}
__device__ __forceinline__ float xsum(float x) {
    auto s = __builtin_amdgcn_permlane16_swap(__float_as_uint(x), __float_as_uint(x), false, false);
    x = __uint_as_float(s[0]) + __uint_as_float(s[1]);
    auto t = __builtin_amdgcn_permlane32_swap(__float_as_uint(x), __float_as_uint(x), false, false);
    return __uint_as_float(t[0]) + __uint_as_float(t[1]);
}
__device__ __forceinline__ float max3f(float a, float b, float c) { return __builtin_fmaxf(__builtin_fmaxf(a, b), c); }
__device__ __forceinline__ float xmax(float x) {
    auto s = __builtin_amdgcn_permlane16_swap(__float_as_uint(x), __float_as_uint(x), false, false);
    x = fmaxf(__uint_as_float(s[0]), __uint_as_float(s[1]));
    auto t = __builtin_amdgcn_permlane32_swap(__float_as_uint(x), __float_as_uint(x), false, false);
    return fmaxf(__uint_as_float(t[0]), __uint_as_float(t[1]));
}
__device__ __forceinline__ unsigned pk2(float lo, float hi) {
    bf16x2_t v; v[0] = (__bf16)lo; v[1] = (__bf16)hi;
    return __builtin_bit_cast(unsigned, v);
}
__device__ __forceinline__ unsigned pk2(f32x2 v) { return pk2(v[0], v[1]); }
__device__ __forceinline__ f32x2 lo2(f32x4 v) { return __builtin_shufflevector(v, v, 0, 1); }
__device__ __forceinline__ f32x2 hi2(f32x4 v) { return __builtin_shufflevector(v, v, 2, 3); }
__device__ __forceinline__ f32x2 sp2(float v) { return f32x2{v, v}; }
__device__ __forceinline__ f32x2 fma2(f32x2 a, f32x2 b, f32x2 c) { return __builtin_elementwise_fma(a, b, c); }
__device__ __forceinline__ float rbf(float x) { return (float)(__bf16)x; }
__device__ __forceinline__ void ln_stats(float sum, float sq, float& mean, float& den) {
    float m = sum * (1.0f / D_);
    float v = fmaxf(sq * (1.0f / D_) - m * m, 0.0f);
    mean = rbf(m);
    den = rbf(sqrtf(rbf(rbf(v) + 1e-5f)));
}


typedef __attribute__((address_space(1))) unsigned long long gu64;
#define RLX_AGENT __ATOMIC_RELAXED, __HIP_MEMORY_SCOPE_AGENT
#define SB() __builtin_amdgcn_sched_barrier(0)
#define LDSF(p) (*(__attribute__((address_space(3))) float*)(p))
#define LDSV(T, p) (*(__attribute__((address_space(3))) T*)(p))
constexpr size_t SYNC_OFF = 16u << 20, FLAG_OFF = SYNC_OFF + 4096, STAT_OFF = SYNC_OFF + 131072, KV_OFF = 20u << 20;
constexpr int ACT_BYTES = 128 * ACT_TILE_B;
constexpr unsigned long long SPIN_TICKS = 20000000ull;


template <int N> __device__ __forceinline__ float ror_add(float x) {
    return x + __uint_as_float(__builtin_amdgcn_update_dpp(0u, __float_as_uint(x), 0x120 + N, 0xF, 0xF, false));
}
__device__ __forceinline__ float wave_sum(float x) { return xsum(ror_add<1>(ror_add<2>(ror_add<4>(ror_add<8>(x))))); }

__device__ __forceinline__ void start_loads(const char* x, char* ws, int row, int lane, unsigned long long& ep, u32x4& rv) {
    const unsigned zero = 0, ro = (lane < 56 ? lane : 55) * 16;
    asm volatile("global_load_dwordx2 %0, %1, %2 sc1" : "=v"(ep) : "v"(zero), "s"(ws + SYNC_OFF) : "memory");
    asm volatile("global_load_dwordx4 %0, %1, %2" : "=v"(rv) : "v"(ro), "s"(x + (size_t)row * 896) : "memory");
}
__device__ __forceinline__ unsigned long long publish_stats(char* ws, int row, int lane, unsigned long long ep, u32x4 rv) {
    const unsigned long long tag = (((unsigned long long)__builtin_amdgcn_readfirstlane((unsigned)(ep >> 32)) << 32) |
                                    (unsigned)__builtin_amdgcn_readfirstlane((unsigned)ep)) + 1ull;
    if (lane >= 56) rv = u32x4{0u, 0u, 0u, 0u};
    const bf16x8 f = __builtin_bit_cast(bf16x8, rv);
    float mean, den;
    ln_stats(wave_sum(frag_sum(f, 0.f)), wave_sum(frag_sq(f, 0.f)), mean, den);
    const unsigned val = (__float_as_uint(mean) & 0xFFFF0000u) | (__float_as_uint(den) >> 16);
    if (lane == 0) __hip_atomic_store((gu64*)(ws + STAT_OFF) + row, ((unsigned long long)(unsigned)tag << 32) | val, RLX_AGENT);
    return tag;
}
__device__ __forceinline__ void stats_request(char* ws, int row, unsigned long long& v) {
    asm volatile("global_load_dwordx2 %0, %1, %2 sc1" : "=v"(v) : "v"(row * 8), "s"(ws + STAT_OFF) : "memory");
}
__device__ __forceinline__ unsigned stats_check(char* ws, int row, unsigned long long tag, unsigned long long v) {
    const unsigned long long t0 = __builtin_amdgcn_s_memrealtime();
    while (!__all((unsigned)(v >> 32) == (unsigned)tag)) {
        if (__builtin_amdgcn_s_memrealtime() - t0 > SPIN_TICKS) break;
        __builtin_amdgcn_s_sleep(2);
        v = __hip_atomic_load((gu64*)(ws + STAT_OFF) + row, RLX_AGENT);
    }
    return (unsigned)v;
}
__device__ __forceinline__ void wait_flags(char* ws, int i, unsigned long long tag) {
    const unsigned long long t0 = __builtin_amdgcn_s_memrealtime();
    for (;;) {
        const unsigned long long v = __hip_atomic_load((gu64*)(ws + FLAG_OFF + (size_t)i * 64), RLX_AGENT);
        if (__all(v == tag)) break;
        if (__builtin_amdgcn_s_memrealtime() - t0 > SPIN_TICKS) break;
        __builtin_amdgcn_s_sleep(1);
    }
}

struct P2 {
    const char* wd; const char* wo; const char* wg; const char* wu;
    const char* w1; const char* w2; unsigned ring; int n2, th, kg;
    unsigned wv0; int wa[4], own;
};
__device__ __forceinline__ void p2_lane(P2& P) {
    int lane = threadIdx.x & 63;
    asm volatile("" : "+v"(lane));
    const int g = lane >> 4, q = (lane >> 2) & 3, p = lane & 3, f = ((q >> 1) & 1) | ((g & 1) << 1);
    P.wv0 = (lane >> 3) * 896 + (((lane & 7) ^ (((lane >> 4) & 1) << 1)) << 4);
#pragma unroll
    for (int t = 0; t < 4; ++t) P.wa[t] = P.kg * 4096 + (8 * g + q) * 128 + ((t ^ f) << 5) + p * 8;
    P.own = P.th * 2048 + (g >> 1) * (16384 - 2048);
}
__device__ __forceinline__ void p2_dma(const P2& P, int u, int slot, int half) {
    const char* src = u < 14 ? P.w1 + u * (32 * 896) : P.w2 + (u - 14 < P.n2 ? u - 14 : P.n2 - 1) * (32 * 896);
    glds16s(src + half * (8 * 896), half ? (P.wv0 ^ 64) : P.wv0, P.ring + slot * 16384 + half * 1024);
}
__device__ __forceinline__ void p2_dma2(const P2& P, int u, int slot) { p2_dma(P, u, slot, 0); p2_dma(P, u, slot, 1); }
#define P2_WRD(t, SLOT) lds_tr2(L + P.wa[t] + ((SLOT) >> 2) * 65536 + ((SLOT) & 3) * 16384, 512)
__device__ __forceinline__ void p2_dma_own(const P2& P, int u, int sl, int j) {
    glds16s(P.w1 + (u * 32 + j * 8 - P.th * 16) * 896, (j & 1) ? (P.wv0 ^ 64) : P.wv0, P.ring + (sl + (j >> 1)) * 16384 + (j & 1) * 1024);
}
__device__ __forceinline__ void p2_dma_own4(const P2& P, int u, int sl) {
    p2_dma_own(P, u, sl, 0); p2_dma_own(P, u, sl, 1); p2_dma_own(P, u, sl, 2); p2_dma_own(P, u, sl, 3);
}
__device__ __forceinline__ void p2_to_regs(const P2& P, lds_ptr L, bf16x8 (&wr)[4], int sl) {
#pragma unroll
    for (int t = 0; t < 4; ++t) wr[t] = lds_tr2(L + P.wa[t] + P.own + (sl >> 2) * 65536 + (sl & 3) * 16384, 512);
}
__device__ __forceinline__ void p2_touch(const P2& P, int w, int lane, unsigned lb) {
    const unsigned o1 = blockIdx.x * 6272 + w * 1024 + lane * 16, o2 = blockIdx.x * 1568 + (w & 1) * 1024 + lane * 16;
    if (w < 7) glds16s(P.wd, o1 < F_ * 896 - 16 ? o1 : F_ * 896 - 16, lb + 162816);
    if (w >= 6) glds16s(P.wo, o2 < D_ * 896 - 16 ? o2 : D_ * 896 - 16, lb + 162816);
}
#define WAITL0() asm volatile("s_waitcnt lgkmcnt(0)" ::: "memory")

constexpr int FF_XS = 32768, FF_UOFF = 15376, FF_WS = 2 * FF_UOFF, FF_W0 = 2 * FF_XS, FF_CS = 163840 - 1024;

__device__ __forceinline__ unsigned long long ffn_block(const char* x, const char* wg, const char* wu, char* ws, int rb, int cb,
                                                        lds_ptr L, P2& P, bf16x8 (&wr)[5][4]) {
    const int tid = threadIdx.x, lane = tid & 63;
    const int w = __builtin_amdgcn_readfirstlane(tid >> 6);
    const int wm = w & 3, wn = w >> 2;
    const unsigned lb = (unsigned)(size_t)L;
    unsigned long long ep; u32x4 rv;
    const int srow = rb * 256 + (16 + cb) * 8 + w;
    start_loads(x, ws, srow, lane, ep, rv);
    const unsigned xo0 = (lane >> 3) * 896 + (((lane & 7) ^ (lane >> 4)) << 4), xo1 = xo0 ^ 64;
    const char* xb = x + (size_t)(rb * 256 + 64 * wm) * 896;
    auto dma_x = [&](int c, int j) { glds16s(xb + j * (8 * 896) + c * 128, (j & 1) ? xo1 : xo0, lb + (c & 1) * FF_XS + (64 * wm + 8 * j) * 128); };
    unsigned woff[8];
    auto dma_w1 = [&](int c, int buf, int j) {
        const int n = (j == 7 && wm > 1) ? 29 : wm + 4 * j, mat = n >= 15, nn = n - 15 * mat;
        glds16s((mat ? wu : wg) + cb * 224 + (size_t)c * (64 * F_ * 2), woff[j], lb + FF_W0 + mat * FF_UOFF + nn * 1024 + buf);
    };
    if (wn == 0) {
#pragma unroll
        for (int j = 0; j < 8; ++j) dma_x(0, j);
#pragma unroll
        for (int j = 0; j < 3; ++j) dma_x(1, j);
    }
#pragma unroll
    for (int j = 0; j < 8; ++j) {
        int n = wm + 4 * j; if (n > 29) n = 29;
        const int nn = n >= 15 ? n - 15 : n;
        const int g0 = (nn * 64) / 120, r0 = nn * 64 - g0 * 120;
        int rem = r0 + lane;
        const bool wrap = rem >= 120;
        rem = wrap ? rem - 120 : (rem >= 112 ? rem - 8 : rem);
        const int r = (rem * 147) >> 11;
        woff[j] = (8 * g0 + (wrap ? 8 : 0) + r) * (F_ * 2) + (rem - r * 14) * 16;
        if (wn == 1) dma_w1(0, 0, j);
    }
    if (wn == 1) {
#pragma unroll
        for (int j = 0; j < 8; ++j) dma_w1(1, FF_WS, j);
    }
    p2_touch(P, w, lane, lb);
    asm volatile("s_waitcnt vmcnt(12)" : "+v"(ep), "+v"(rv) :: "memory");
    const unsigned long long tag = publish_stats(ws, srow, lane, ep, rv);
    const int g = lane >> 4, q = (lane >> 2) & 3, p = lane & 3, tk = lane & 15;
    const int xa = (64 * wm + tk) * 128 + ((g ^ ((lane >> 1) & 7)) << 4);
    const int wa = FF_W0 + g * 1920 + q * 224 + (p & 1) * 8 + (p >> 1) * FF_UOFF + wn * 112;
    const int ce = tid < 224 ? tid : 223, crow = 2048 + ((ce >> 3) & 1) * F_ + cb * 112 + (ce >> 4) * 8 + (ce & 7);
    f32x4 acc[4][7];
#pragma unroll
    for (int i = 0; i < 4; ++i)
#pragma unroll
        for (int t = 0; t < 7; ++t) acc[i][t] = f32x4{0.f, 0.f, 0.f, 0.f};
    bf16x8 xf[2][4], wf[4];
    if (wn == 0) asm volatile("s_waitcnt vmcnt(5)\n\ts_barrier" ::: "memory");
    else asm volatile("s_waitcnt vmcnt(10)\n\ts_barrier" ::: "memory");
#define XRD(B, ks, i) xf[ks][i] = lds_b128((B) + (xa ^ ((ks) * 64)) + (i) * 2048)
#define WRD(B, s) lds_tr2((B) + ((s) / 7) * 7680 + ((s) % 7) * 16, 896)
    {
        lds_ptr W = L + wa;
        XRD(L, 0, 0); XRD(L, 0, 1); XRD(L, 0, 2); XRD(L, 0, 3);
        wf[0] = WRD(W, 0); wf[1] = WRD(W, 1); wf[2] = WRD(W, 2);
    }
    unsigned long long sreq, creq;
    int wb = 0;
#define FCHUNK(c, ROLE, MODE)                                                                                          \
    {                                                                                                                  \
        const int wb1 = (wb == 2 * FF_WS) ? 0 : wb + FF_WS, wb2 = (wb == 0) ? 2 * FF_WS : wb - FF_WS;                  \
        lds_ptr X = L + ((c) & 1) * FF_XS, Xn = L + (((c) + 1) & 1) * FF_XS, W = L + wa + wb, Wn = L + wa + wb1;       \
        _Pragma("unroll") for (int s = 0; s < 14; ++s) {                                                               \
            const int ks = s / 7, t = s % 7;                                                                           \
            if (s == 11 && MODE < 2) {                                                                                 \
                if (ROLE == 1 && MODE == 0) asm volatile("s_waitcnt vmcnt(8) lgkmcnt(0)\n\ts_barrier" ::: "memory");  \
                else WAIT_BAR0();                                                                                      \
            }                                                                                                          \
            if (s < 11) wf[(s + 3) % 4] = WRD(W, s + 3); else if (MODE < 2) wf[(s + 3) % 4] = WRD(Wn, s - 11);        \
            if (s < 4) XRD(X, 1, s);                                                                                   \
            if (s == 11 && MODE < 2) { XRD(Xn, 0, 0); XRD(Xn, 0, 1); }                                                 \
            if (s == 12 && MODE < 2) { XRD(Xn, 0, 2); XRD(Xn, 0, 3); }                                                 \
            if (ROLE == 0 && MODE < 2 && s < 5) dma_x((c) + 1, 3 + s);                                                 \
            if (ROLE > 0 && MODE == 0 && s >= 3 && s < 11) dma_w1((c) + 2, wb2, s - 3);                         \
            if (ROLE == 0 && MODE == 0 && s >= 11) dma_x((c) + 2, s - 11);                                             \
            if (MODE == 3 && s >= 2 && s < 10) p2_dma_own(P, (s - 2) >> 2, s < 6 ? 2 : 6, (s - 2) & 3);                \
            _Pragma("unroll") for (int i = 0; i < 4; ++i) acc[i][t] = MFMA16(wf[s % 4], xf[ks][i], acc[i][t]);        \
            SB();                                                                                                      \
        }                                                                                                              \
        { bf16x8 a_ = wf[2], b_ = wf[3], d_ = wf[0]; wf[0] = a_; wf[1] = b_; wf[2] = d_; }                             \
        wb = wb1;                                                                                                      \
    }
    if (wn == 0) {
#pragma unroll 1
        for (int c = 0; c < 5; ++c) FCHUNK(c, 0, 0)
        stats_request(ws, rb * 256 + 64 * wm + lane, sreq);
        FCHUNK(5, 0, 1)
    } else {
#pragma unroll 1
        for (int c = 0; c < 5; ++c) FCHUNK(c, 1, 0)
        stats_request(ws, rb * 256 + 64 * wm + lane, sreq);
        FCHUNK(5, 1, 1)
    }
    p2_lane(P);
    stats_request(ws, crow, creq);
    FCHUNK(6, 0, 3)
    asm volatile("s_waitcnt vmcnt(8)" : "+v"(creq) :: "memory");
    LDSF(L + FF_CS + ce * 4) = __uint_as_float(stats_check(ws, crow, tag, creq));
    asm volatile("" : "+v"(sreq));
    const unsigned sv = stats_check(ws, rb * 256 + 64 * wm + lane, tag, sreq);
    LBAR();
    const __amdgpu_buffer_rsrc_t ra = __builtin_amdgcn_make_buffer_rsrc(ws, (short)0, ACT_BYTES, 0x00020000);
    f32x4 csr[7];
#pragma unroll
    for (int t = 0; t < 7; ++t) csr[t] = lds_f4(L + FF_CS + (7 * wn + t) * 64 + g * 16);
    LBAR();
    const float K1 = -2.3022081986f, K2 = -0.1029432398f;
#pragma unroll
    for (int ip = 0; ip < 2; ++ip) {
        const unsigned sa = __shfl(sv, 32 * ip + tk), sb = __shfl(sv, 32 * ip + 16 + tk);
        const float ma = -__uint_as_float(sa & 0xFFFF0000u), mb = -__uint_as_float(sb & 0xFFFF0000u);
        const float dn = __uint_as_float((lane < 32 ? sa : sb) << 16);
        const float a = __builtin_amdgcn_rcpf(dn), ia2 = dn * dn, c1 = K1 * a, c2 = K2 * a * a * a;
        const unsigned dst = (rb * 16 + wm * 4 + 2 * ip + (lane >> 5)) * ACT_TILE_B + (cb * 14 + wn * 7) * 256 + tk * 16;
        u32x2 hp[7];
#pragma unroll
        for (int t = 0; t < 7; ++t) {
            const f32x4 cs = csr[t];
#pragma unroll
            for (int u = 0; u < 2; ++u) {
                const f32x2 c2v = u ? hi2(cs) : lo2(cs);
                const f32x2 va = fma2(sp2(ma), c2v, u ? hi2(acc[2 * ip][t]) : lo2(acc[2 * ip][t]));
                const f32x2 vb = fma2(sp2(mb), c2v, u ? hi2(acc[2 * ip + 1][t]) : lo2(acc[2 * ip + 1][t]));
                auto s0 = __builtin_amdgcn_permlane32_swap(__float_as_uint(va[0]), __float_as_uint(vb[0]), false, false);
                auto s1 = __builtin_amdgcn_permlane32_swap(__float_as_uint(va[1]), __float_as_uint(vb[1]), false, false);
                const f32x2 G = f32x2{__uint_as_float(s0[0]), __uint_as_float(s1[0])}, U = f32x2{__uint_as_float(s0[1]), __uint_as_float(s1[1])};
                const f32x2 z = G * fma2(G * G, sp2(c2), sp2(c1));
                const f32x2 e = f32x2{__builtin_amdgcn_exp2f(z[0]), __builtin_amdgcn_exp2f(z[1])};
                const f32x2 d = fma2(e, sp2(ia2), sp2(ia2));
                hp[t][u] = pk2((G * U) * f32x2{__builtin_amdgcn_rcpf(d[0]), __builtin_amdgcn_rcpf(d[1])});
            }
            if (t & 1) {
                auto s0 = __builtin_amdgcn_permlane16_swap(hp[t - 1][0], hp[t][0], false, false);
                auto s1 = __builtin_amdgcn_permlane16_swap(hp[t - 1][1], hp[t][1], false, false);
                __builtin_amdgcn_raw_buffer_store_b128(u32x4{s0[0], s1[0], s0[1], s1[1]}, ra, dst + (g & 1) * 256, (t - 1) * 256, 16);
            }
            if (t == 6) __builtin_amdgcn_raw_buffer_store_b64(hp[6], ra, dst + (g & 1) * 8, 6 * 256, 16);
            {
                if (ip == 0) {
                    if (t < 6) { p2_dma_own(P, 2 + (t >> 1), 4 * (t >> 1), 2 * (t & 1)); p2_dma_own(P, 2 + (t >> 1), 4 * (t >> 1), 2 * (t & 1) + 1); }
                } else if (t < 5) p2_dma2(P, 5 + t, t);
            }
            SB();
        }
        if (ip == 0) {
            asm volatile("s_waitcnt vmcnt(1)" ::: "memory");
            p2_to_regs(P, L, wr[0], 2); p2_to_regs(P, L, wr[1], 6); p2_to_regs(P, L, wr[2], 0); p2_to_regs(P, L, wr[3], 4); p2_to_regs(P, L, wr[4], 8);
            WAITL0();
            SB();
        }
    }
    WAIT_BAR0();
    if (tid == 0) __hip_atomic_store((gu64*)(ws + FLAG_OFF + (size_t)(rb * 32 + 16 + cb) * 64), tag, RLX_AGENT);
#pragma unroll
    for (int k = 5; k < 10; ++k) p2_dma2(P, 5 + k, k);
    return tag;
}

constexpr int AT_WM = 4096, AT_WS = 3 * AT_WM, AT_W = 131072;
constexpr int AT_K = 98304, AT_V = AT_K + 32768;

__device__ __forceinline__ unsigned long long att_block(const char* x, const char* wqkv, char* ws, int ch, int head, lds_ptr L,
                                                        P2& P, bf16x8 (&wr)[5][4]) {
    const int tid = threadIdx.x, lane = tid & 63;
    const int w = __builtin_amdgcn_readfirstlane(tid >> 6);
    const unsigned lb = (unsigned)(size_t)L;
    const int g = lane >> 4, q = (lane >> 2) & 3, p = lane & 3, tk = lane & 15;
    unsigned long long ep; u32x4 rv;
    const int srow = ch * 256 + head * 8 + w;
    start_loads(x, ws, srow, lane, ep, rv);
    const unsigned wo = (lane >> 2) * (QKV_ * 2) + (((lane & 3) ^ (((lane >> 5) & 1) << 1)) << 4);
    const char* wsrc0 = wqkv + (w & 3) * (16 * QKV_ * 2) + (w >> 2) * 896 + head * 56;
    const char* wsrc1 = wqkv + (w & 3) * (16 * QKV_ * 2) + 2 * 896 + head * 56 - 8;
    auto dma_w = [&](int c) {
        glds16s(wsrc0 + c * (64 * QKV_ * 2), wo, lb + AT_W + (c & 1) * AT_WS + w * 1024);
        if (w < 4) glds16s(wsrc1 + c * (64 * QKV_ * 2), wo, lb + AT_W + (c & 1) * AT_WS + 8192 + w * 1024);
    };
    const int orow = ch * 256 + 32 * w;
    const unsigned xo0 = (lane >> 3) * 896 + (((lane & 7) ^ (lane >> 4)) << 4), xo1 = xo0 ^ 64;
    const char* xs = x + (size_t)orow * 896;
    auto dma_x = [&](int c, int i) {
        const char* src = xs + i * (16 * 896) + c * 128;
        glds16s(src, xo0, lb + (c & 3) * 32768 + w * 4096 + i * 2048);
        glds16s(src + 8 * 896, xo1, lb + (c & 3) * 32768 + w * 4096 + i * 2048 + 1024);
    };
    dma_w(0); dma_x(0, 0); dma_x(0, 1);
    const int cblk = (ch * 16 + head) < 112 ? ch * 16 + head : 111;
    auto dma_cs = [&]() {
        const char* cp = (cblk < 56 ? P.wg + cblk * 64 : P.wu + (cblk - 56) * 64);
        const unsigned co = (lane >> 2) * (F_ * 2) + (((lane & 3) ^ (((lane >> 5) & 1) << 1)) << 4);
#pragma unroll
        for (int j = 0; j < 4; ++j) {
            const int ks = j < 2 ? w : (w < 6 ? w + 8 : 13);
            glds16s(cp + (size_t)(ks * 32 + (j & 1) * 16) * (F_ * 2), co, lb + 98304 + w * 4096 + j * 1024);
        }
    };
    dma_w(1); dma_x(1, 0); dma_x(1, 1); dma_x(2, 0); dma_x(2, 1);
    p2_touch(P, w, lane, lb);
    asm volatile("s_waitcnt vmcnt(15)" : "+v"(ep), "+v"(rv) :: "memory");
    const unsigned long long tag = publish_stats(ws, srow, lane, ep, rv);

    const int xa0 = w * 4096 + tk * 128 + ((g ^ ((lane >> 1) & 7)) << 4), xa1 = xa0 ^ 64;
    const int wA0 = AT_W + (8 * g + q) * 64 + ((g & 1) << 5) + p * 8, wA1 = wA0 ^ 32;
    const int cidx = w > 5 ? 5 : w;
    const int wC = (cidx >> 1) * AT_WM + (wA0 ^ ((cidx & 1) << 5));
    f32x4 aK[2][2], aV[2][2], aQ[2][2], cs = f32x4{0.f, 0.f, 0.f, 0.f};
#pragma unroll
    for (int i = 0; i < 2; ++i) aK[i][0] = aK[i][1] = aV[i][0] = aV[i][1] = aQ[i][0] = aQ[i][1] = f32x4{0.f, 0.f, 0.f, 0.f};
    const bf16x8 ones = __builtin_bit_cast(bf16x8, u32x4{0x3F803F80u, 0x3F803F80u, 0x3F803F80u, 0x3F803F80u});
    bf16x8 wf[2][3][2], cf[2], xf[2][2];
#define AWRD(B, ks) { _Pragma("unroll") for (int m = 0; m < 3; ++m) { wf[ks][m][0] = lds_tr2((B) + wA0 + m * AT_WM + (ks) * 2048, 256); \
                      wf[ks][m][1] = lds_tr2((B) + wA1 + m * AT_WM + (ks) * 2048, 256); } cf[ks] = lds_tr2((B) + wC + (ks) * 2048, 256); }
#define AXRD(c, i) { xf[i][0] = lds_b128(L + xa0 + ((c) & 3) * 32768 + (i) * 2048); xf[i][1] = lds_b128(L + xa1 + ((c) & 3) * 32768 + (i) * 2048); }
#define AMM(i, ks) { _Pragma("unroll") for (int t = 0; t < 2; ++t) { \
                       aK[i][t] = MFMA16(wf[ks][1][t], xf[i][ks], aK[i][t]); aV[i][t] = MFMA16(xf[i][ks], wf[ks][2][t], aV[i][t]); \
                       aQ[i][t] = MFMA16(wf[ks][0][t], xf[i][ks], aQ[i][t]); } }
    asm volatile("s_waitcnt vmcnt(%0)\n\ts_barrier" :: "i"(11) : "memory");
    AXRD(0, 0) AWRD(L, 0) AWRD(L, 1)
    SB();
    const int strow = orow + (lane & 31);
    unsigned long long sreq;
#pragma unroll
    for (int c = 0; c < 7; ++c) {
        AXRD(c, 1)
        AMM(0, 0)
        cs = MFMA16(cf[0], ones, cs);
        AMM(0, 1)
        cs = MFMA16(cf[1], ones, cs);
        SB();
        if (c + 3 < 7) dma_x(c + 3, 0);
        if (c == 4) stats_request(ws, strow, sreq);
        if (c < 6) {
            asm volatile("s_waitcnt vmcnt(%0)\n\ts_barrier" :: "i"(c == 0 ? 8 : c < 4 ? 4 : c == 4 ? 7 : 0) : "memory");
            AXRD(c + 1, 0)
            if (c + 2 < 7) dma_w(c + 2);
            if (c == 5)
    {
        lds_ptr C = L + 98304 + w * 4096 - AT_W;
        const f32x4 z4 = f32x4{0.f, 0.f, 0.f, 0.f};
        f32x4 p0 = MFMA16(lds_tr2(C + wA0, 256), ones, z4), p1 = MFMA16(lds_tr2(C + wA1, 256), ones, z4);
        const bf16x8 f0 = lds_tr2(C + wA0 + 2048, 256), f1 = lds_tr2(C + wA1 + 2048, 256);
        if (w < 6) { p0 = MFMA16(f0, ones, p0); p1 = MFMA16(f1, ones, p1); }
        if (tk == 0) { LDSV(f32x4, L + w * 4096 + 1024 + g * 16) = p0; LDSV(f32x4, L + w * 4096 + 1088 + g * 16) = p1; }
    }
        }
        AMM(1, 0)
        SB();
        if (c < 6) AWRD(L + ((c + 1) & 1) * AT_WS, 0)
        AMM(1, 1)
        SB();
        if (c < 6) AWRD(L + ((c + 1) & 1) * AT_WS, 1)
        if (c + 3 < 7) dma_x(c + 3, 1);
        if (c == 3) dma_cs();
        SB();
    }
    asm volatile("" : "+v"(sreq));
    lds_ptr ST = L + w * 4096;
    if (tk == 0) LDSV(f32x4, ST + 512 + g * 16) = cs;
    {
        const unsigned sv = stats_check(ws, strow, tag, sreq);
        LDSF(ST + lane * 4) = -__uint_as_float(sv & 0xFFFF0000u);
        LDSF(ST + 256 + lane * 4) = __builtin_amdgcn_rcpf(__uint_as_float(sv << 16));
    }
    LBAR();
    if (w == 7) {
        float t = 0.f;
#pragma unroll
        for (int k = 0; k < 8; ++k) t += LDSF(L + k * 4096 + 1024 + (lane & 31) * 4);
        if (lane < 32 && ch * 16 + head < 112)
            __hip_atomic_store((gu64*)(ws + STAT_OFF) + 2048 + cblk * 32 + lane,
                               ((unsigned long long)(unsigned)tag << 32) | __float_as_uint(t), RLX_AGENT);
    }
    bf16x8 qf[2];
    const bool pad = (lane >= 48);
    const __amdgpu_buffer_rsrc_t rk = __builtin_amdgcn_make_buffer_rsrc(ws + KV_OFF, (short)0, 128 * 32768, 0x00020000);
    {
        const unsigned kvo = (ch * 16 + head) * 32768 + w * 2048 + lane * 16;
        f32x4 cq[2], ck[2];
        float cv[2];
#pragma unroll
        for (int t = 0; t < 2; ++t) {
            cq[t] = lds_f4(L + 512 + t * 4096 + g * 16);
            ck[t] = lds_f4(L + 512 + (2 + t) * 4096 + g * 16);
            cv[t] = LDSF(L + 512 + (4 + t) * 4096 + tk * 4);
        }
#define CORR(A, C) u32x4{pk2(fma2(sp2(nm), lo2(C[0]), lo2(A[0])) * sp2(rs)), pk2(fma2(sp2(nm), hi2(C[0]), hi2(A[0])) * sp2(rs)), \
                         pk2(fma2(sp2(nm), lo2(C[1]), lo2(A[1])) * sp2(rs)), pk2(fma2(sp2(nm), hi2(C[1]), hi2(A[1])) * sp2(rs))}
#pragma unroll
        for (int i = 0; i < 2; ++i) {
            const float nm = LDSF(ST + i * 64 + tk * 4), rs = LDSF(ST + 256 + i * 64 + tk * 4);
            u32x4 kv = CORR(aK[i], ck);
            kv[2] = pad ? 0u : kv[2];
            kv[3] = pad ? 0u : kv[3];
            __builtin_amdgcn_raw_buffer_store_b128(kv, rk, kvo, i * 1024, 16);
            LDSV(u32x4, L + AT_K + (16 + 2 * w + i) * 1024 + lane * 16) = kv;
        }
        f32x4 s0[2], s1[2];
#pragma unroll
        for (int u = 0; u < 2; ++u) {
            s0[u] = lds_f4(ST + u * 64 + g * 16);
            s1[u] = lds_f4(ST + 256 + u * 64 + g * 16);
        }
#pragma unroll
        for (int t = 0; t < 2; ++t) {
            u32x4 o;
#pragma unroll
            for (int u = 0; u < 2; ++u) {
                o[2 * u] = pk2(fma2(lo2(s0[u]), sp2(cv[t]), lo2(aV[u][t])) * lo2(s1[u]));
                o[2 * u + 1] = pk2(fma2(hi2(s0[u]), sp2(cv[t]), hi2(aV[u][t])) * hi2(s1[u]));
            }
            if (t == 0 && tk == 0) o = u32x4{0x3F803F80u, 0x3F803F80u, 0x3F803F80u, 0x3F803F80u};
            __builtin_amdgcn_raw_buffer_store_b128(o, rk, kvo, 16384 + t * 1024, 16);
            LDSV(u32x4, L + AT_V + ((8 + w) * 2 + t) * 1024 + lane * 16) = o;
        }
#pragma unroll
        for (int i = 0; i < 2; ++i) {
            const float nm = LDSF(ST + i * 64 + tk * 4), rs = LDSF(ST + 256 + i * 64 + tk * 4);
            u32x4 qv = CORR(aQ[i], cq);
            qv[2] = pad ? 0x3F80u : qv[2];
            qv[3] = pad ? 0u : qv[3];
            qf[i] = __builtin_bit_cast(bf16x8, qv);
        }
    }
    asm volatile("s_waitcnt vmcnt(0)" ::: "memory");
    if (lane == 0) __hip_atomic_store((gu64*)(ws + FLAG_OFF + (size_t)(256 + (ch * 16 + head) * 8 + w) * 64), tag, RLX_AGENT);
    LBAR();
    p2_lane(P);
    {
        u32x4 hk[2], hv[2];
        if (ch) {
            wait_flags(ws, 256 + ((ch - 1) * 16 + head) * 8 + w, tag);
            const unsigned kvo = ((ch - 1) * 16 + head) * 32768 + w * 2048 + lane * 16;
#pragma unroll
            for (int i = 0; i < 2; ++i) {
                hk[i] = __builtin_amdgcn_raw_buffer_load_b128(rk, kvo, i * 1024, 16);
                hv[i] = __builtin_amdgcn_raw_buffer_load_b128(rk, kvo, 16384 + i * 1024, 16);
            }
        } else {
            hk[0] = hk[1] = u32x4{0u, 0u, pad ? 0xF149u : 0u, 0u};
            hv[0] = hv[1] = u32x4{0u, 0u, 0u, 0u};
        }
#pragma unroll
        for (int i = 0; i < 2; ++i) {
            LDSV(u32x4, L + AT_K + (2 * w + i) * 1024 + lane * 16) = hk[i];
            LDSV(u32x4, L + AT_V + (w * 2 + i) * 1024 + lane * 16) = hv[i];
        }
    }
    LBAR();
    const float NEG = -1e30f;
    f32x4 mle, mgt;
#pragma unroll
    for (int r = 0; r < 4; ++r) { mle[r] = (4 * g + r <= tk) ? 0.f : NEG; mgt[r] = (4 * g + r > tk) ? 0.f : NEG; }
    f32x4 Sc[2][18];
#pragma unroll
    for (int j = 0; j < 18; ++j) {
        const bf16x8 kf = lds_b128(L + AT_K + (2 * w + j) * 1024 + lane * 16);
#pragma unroll
        for (int qt = 0; qt < 2; ++qt) {
            if (j >= qt && j <= qt + 16) {
                f32x4 c0;
                if (j == qt + 16) c0 = mle;
                else if (j == qt) c0 = mgt;
                else c0 = f32x4{0.f, 0.f, 0.f, 0.f};
                Sc[qt][j] = MFMA16(kf, qf[qt], c0);
            } else {
                Sc[qt][j] = f32x4{0.f, 0.f, 0.f, 0.f};
            }
        }
        if (j < 6) { p2_dma_own(P, j >> 1, j & 6, 2 * (j & 1)); p2_dma_own(P, j >> 1, j & 6, 2 * (j & 1) + 1); }
    }
    const float CS = 0.18898223650461362f * 1.4426950408889634f;
    u32x4 pf[2][9];
#pragma unroll
    for (int qt = 0; qt < 2; ++qt) {
        float m = NEG, m2 = NEG;
#pragma unroll
        for (int j = qt; j <= qt + 16; ++j) { m = max3f(m, Sc[qt][j][0], Sc[qt][j][1]); m2 = max3f(m2, Sc[qt][j][2], Sc[qt][j][3]); }
        m = max3f(m, m2, m2);
        m = xmax(m);
        if (qt == 0) {
            SB();
            asm volatile("s_waitcnt vmcnt(0)" ::: "memory");
            p2_to_regs(P, L, wr[0], 0); p2_to_regs(P, L, wr[1], 2); p2_to_regs(P, L, wr[2], 4);
            WAITL0();
            SB();
        }
        const float mc = -m * CS;
#pragma unroll
        for (int j = qt; j <= qt + 16; ++j) {
            const f32x2 a = fma2(lo2(Sc[qt][j]), sp2(CS), sp2(mc)), b = fma2(hi2(Sc[qt][j]), sp2(CS), sp2(mc));
            Sc[qt][j] = f32x4{__builtin_amdgcn_exp2f(a[0]), __builtin_amdgcn_exp2f(a[1]), __builtin_amdgcn_exp2f(b[0]), __builtin_amdgcn_exp2f(b[1])};
            if (qt == 0 && j < 12) {
                if (j < 8) p2_dma_own(P, 3 + (j >> 2), 2 * (j >> 2), j & 3);
                else p2_dma(P, 9 + ((j - 8) >> 1), 4 + ((j - 8) >> 1), j & 1);
                SB();
            }
            if (qt == 1 && (j & 1) == 0) { p2_dma(P, 5 + ((j - 2) >> 2), (j - 2) >> 2, ((j - 2) >> 1) & 1); SB(); }
        }
#pragma unroll
        for (int b = 0; b < 9; ++b)
            pf[qt][b] = u32x4{pk2(Sc[qt][2 * b][0], Sc[qt][2 * b][1]), pk2(Sc[qt][2 * b][2], Sc[qt][2 * b][3]),
                              pk2(Sc[qt][2 * b + 1][0], Sc[qt][2 * b + 1][1]), pk2(Sc[qt][2 * b + 1][2], Sc[qt][2 * b + 1][3])};
        if (qt == 0) {
            SB();
            asm volatile("s_waitcnt vmcnt(0)" ::: "memory");
            p2_to_regs(P, L, wr[3], 0); p2_to_regs(P, L, wr[4], 2);
            WAITL0();
            SB();
        }
    }
    f32x4 O[2][2];
    O[0][0] = O[0][1] = O[1][0] = O[1][1] = f32x4{0.f, 0.f, 0.f, 0.f};
#pragma unroll
    for (int b = 0; b < 9; ++b)
#pragma unroll
        for (int t = 0; t < 2; ++t) {
            const bf16x8 vf = lds_b128(L + AT_V + ((w + b) * 2 + t) * 1024 + lane * 16);
#pragma unroll
            for (int qt = 0; qt < 2; ++qt) O[qt][t] = MFMA16(vf, __builtin_bit_cast(bf16x8, pf[qt][b]), O[qt][t]);
        }
    const __amdgpu_buffer_rsrc_t ra = __builtin_amdgcn_make_buffer_rsrc(ws, (short)0, ACT_BYTES, 0x00020000);
#pragma unroll
    for (int qt = 0; qt < 2; ++qt) {
        const float l = __shfl(O[qt][0][0], tk);
        const float inv = __builtin_amdgcn_rcpf(l);
        const unsigned dst = (ch * 16 + 2 * w + qt) * ACT_TILE_B + 224 * 256 + tk * 16;
#pragma unroll
        for (int t = 0; t < 2; ++t) {
            const int u = 7 * head + 4 * t + g - 1;
            if (t == 1 || g > 0)
                __builtin_amdgcn_raw_buffer_store_b64(
                    u32x2{pk2(O[qt][t][0] * inv, O[qt][t][1] * inv), pk2(O[qt][t][2] * inv, O[qt][t][3] * inv)}, ra,
                    dst + (u >> 1) * 256 + (u & 1) * 8, 0, 16);
        }
    }
    WAIT_BAR0();
    if (tid == 0) __hip_atomic_store((gu64*)(ws + FLAG_OFF + (size_t)(ch * 32 + head) * 64), tag, RLX_AGENT);
#pragma unroll
    for (int k = 6; k < 10; ++k) p2_dma2(P, 5 + k, k);
    return tag;
}

constexpr int LDS_BYTES = 163840;

__global__ __launch_bounds__(512, 2) void k_fused(const char* x, const char* wqkv, const char* wo, const char* wg, const char* wu,
                                                  const char* wd, char* out, char* ws) {
    extern __shared__ __attribute__((aligned(16))) char smem[];
    lds_ptr L = (lds_ptr)smem;
    const unsigned lb = (unsigned)(size_t)L;
    const int tid = threadIdx.x, lane = tid & 63;
    const int w = __builtin_amdgcn_readfirstlane(tid >> 6);
    const int xcd = blockIdx.x & 7, idx = blockIdx.x >> 3;
    const int rblk = 4 * (xcd & 1) + (idx & 3), cblk = 4 * (xcd >> 1) + ((idx >> 2) & 3);
    if (w == 7) {
        unsigned long long pc;
        asm volatile("s_getpc_b64 %0" : "=s"(pc));
        glds16s((const char*)(pc & ~255ull), idx * 1024 + lane * 16, lb + 162816);
        if (idx < 15) glds16s((const char*)(pc & ~255ull), 32768 + idx * 1024 + lane * 16, lb + 162816);
    }
    const int th = w & 1, kg = w >> 1;
    const bool has_p2 = idx >= 4;
    const int rb = 4 * xcd + (idx & 3), cb = has_p2 ? (idx >> 2) - 1 : 0;
    const int a1 = kg ? 14 * (kg - 1) : 56;
    const int a2 = 42 + (kg < 2 ? 3 * kg : 4 * kg - 2);
    P2 P;
    P.n2 = kg < 2 ? 3 : 4;
    P.w1 = (kg ? wd + (size_t)(14 * (kg - 1)) * 32 * 896 : wo) + cb * 128 + th * 16 * 896;
    P.w2 = wd + (size_t)a2 * 32 * 896 + cb * 128 + th * 16 * 896;
    P.ring = lb + kg * 4096 + th * 2048;
    P.th = th; P.kg = kg; P.wd = wd; P.wo = wo; P.wg = wg; P.wu = wu;
    bf16x8 wr[5][4];
    unsigned long long tag;
    if (idx < 16) tag = att_block(x, wqkv, ws, rblk, cblk, L, P, wr);
    else tag = ffn_block(x, wg, wu, ws, rblk, cblk, L, P, wr);
    if (!has_p2) {
        asm volatile("s_waitcnt vmcnt(0)" ::: "memory");
        if (blockIdx.x == 0 && w == 0) {
#pragma unroll 1
            for (int i = 0; i < 4; ++i) wait_flags(ws, 64 * i + lane, tag);
            if (lane == 0) __hip_atomic_store((gu64*)(ws + SYNC_OFF), tag, RLX_AGENT);
        }
        return;
    }
    int ln = lane;
    asm volatile("" : "+v"(ln));
    const int g = ln >> 4, tk = ln & 15;
    const size_t oo = (size_t)(rb * 64 + 32 * th + tk) * 896 + cb * 128 + kg * 32 + g * 8;
    const u32x2 xr0 = *(const u32x2*)(x + oo), xr1 = *(const u32x2*)(x + oo + 16 * 896);
    wait_flags(ws, (rb >> 2) * 32 + (kg == 0 ? (ln < 16 ? ln : 28) : ln < 4 ? 12 + 4 * kg + ln : ln == 4 ? 27 + kg : 28 + kg), tag);
    asm volatile("" ::: "memory");
    const __amdgpu_buffer_rsrc_t ra = __builtin_amdgcn_make_buffer_rsrc(ws, (short)0, ACT_BYTES, 0x00020000);
    const unsigned ab = (rb * 4 + 2 * th) * ACT_TILE_B + lane * 16;
    bf16x8 af[18][2];
#define ALD(u) { const int ks_ = (u) < 14 ? a1 + (u) : a2 + ((u) - 14 < P.n2 ? (u) - 14 : P.n2 - 1);                                      \
                 af[u][0] = __builtin_bit_cast(bf16x8, __builtin_amdgcn_raw_buffer_load_b128(ra, ab, ks_ * 1024, 16));                    \
                 af[u][1] = __builtin_bit_cast(bf16x8, __builtin_amdgcn_raw_buffer_load_b128(ra, ab + ACT_TILE_B, ks_ * 1024, 16)); SB(); }
#pragma unroll
    for (int u = 0; u < 5; ++u) ALD(u)
    f32x4 c1[2][4], c2[2][4];
#pragma unroll
    for (int i = 0; i < 2; ++i)
#pragma unroll
        for (int t = 0; t < 4; ++t) c1[i][t] = c2[i][t] = f32x4{0.f, 0.f, 0.f, 0.f};
#define MM8(ACC, WF, u) { _Pragma("unroll") for (int t = 0; t < 4; ++t) {            \
            ACC[0][t] = MFMA16(WF[t], af[u][0], ACC[0][t]);                          \
            ACC[1][t] = MFMA16(WF[t], af[u][1], ACC[1][t]); } }
#define STEP(ACC, u, SLOT) { bf16x8 wf[4]; _Pragma("unroll") for (int t = 0; t < 4; ++t) wf[t] = P2_WRD(t, SLOT); MM8(ACC, wf, u) }
#pragma unroll
    for (int u = 0; u < 5; ++u) {
        MM8(c1, wr[u], u)
        SB();
        ALD(u + 5)
    }
    asm volatile("s_barrier" ::: "memory");
#pragma unroll
    for (int u = 5; u < 8; ++u) { STEP(c1, u, u - 5) SB(); ALD(u + 5) }
    LBAR();
#pragma unroll
    for (int u = 8; u < 14; ++u) {
        STEP(c1, u, u - 5)
        SB();
        if (u < 13) ALD(u + 5)
        if (u < 11) p2_dma2(P, u + 7, u - 8);
    }
    STEP(c2, 14, 9)
    SB();
    WAIT_BAR0();
    if (kg == 0) {
#pragma unroll
        for (int i = 0; i < 2; ++i)
#pragma unroll
            for (int t = 0; t < 4; ++t) LDSV(f32x4, L + 131072 + th * 8192 + (i * 4 + t) * 1024 + lane * 16) = c1[i][t];
    } else {
#pragma unroll
        for (int i = 0; i < 2; ++i)
#pragma unroll
            for (int t = 0; t < 4; ++t) c2[i][t] += c1[i][t];
    }
    STEP(c2, 15, 0) STEP(c2, 16, 1)
    if (P.n2 == 4) STEP(c2, 17, 2)
    f32x4 fs[2];
#define PUT(K)                                                                                             \
    {                                                                                                      \
        _Pragma("unroll") for (int i = 0; i < 2; ++i) {                                                    \
            _Pragma("unroll") for (int t = 0; t < 4; ++t)                                                  \
                if (t != K) LDSV(f32x4, L + 49152 + w * 8192 + (i * 4 + t) * 1024 + lane * 16) = c2[i][t]; \
            fs[i] = c2[i][K];                                                                              \
        }                                                                                                  \
    }
    if (kg == 0) PUT(0) else if (kg == 1) PUT(1) else if (kg == 2) PUT(2) else PUT(3)
    LBAR();
#pragma unroll
    for (int i = 0; i < 2; ++i) {
#pragma unroll
        for (int k = 1; k < 4; ++k) fs[i] += lds_f4(L + 49152 + (2 * ((kg + k) & 3) + th) * 8192 + (i * 4 + kg) * 1024 + lane * 16);
        const f32x4 sv = lds_f4(L + 131072 + th * 8192 + (i * 4 + kg) * 1024 + lane * 16);
        const u32x2 xr = i ? xr1 : xr0;
        u32x2 o;
#pragma unroll
        for (int u = 0; u < 2; ++u) {
            const f32x2 fv = u ? hi2(fs[i]) : lo2(fs[i]), z = fv * sp2(-1.4426950408889634f);
            const f32x2 d = f32x2{__builtin_amdgcn_exp2f(z[0]), __builtin_amdgcn_exp2f(z[1])} + sp2(1.0f);
            const f32x2 gt = fv * f32x2{__builtin_amdgcn_rcpf(d[0]), __builtin_amdgcn_rcpf(d[1])};
            const f32x2 xv = f32x2{__uint_as_float(xr[u] << 16), __uint_as_float(xr[u] & 0xFFFF0000u)};
            o[u] = pk2(fma2(u ? hi2(sv) : lo2(sv), gt, xv));
        }
        *(u32x2*)(out + oo + (size_t)i * 16 * 896) = o;
    }
}

extern "C" void kernel_launch(void* const* d_in, const int* in_sizes, int n_in, void* d_out, int out_size, void* d_ws,
                              size_t ws_size, hipStream_t stream) {
    (void)hipFuncSetAttribute((const void*)k_fused, hipFuncAttributeMaxDynamicSharedMemorySize, LDS_BYTES);
    k_fused<<<256, 512, LDS_BYTES, stream>>>((const char*)d_in[0], (const char*)d_in[1], (const char*)d_in[2], (const char*)d_in[4],
                                             (const char*)d_in[5], (const char*)d_in[6], (char*)d_out, (char*)d_ws);
}
```
